```python
import torch

def _softmax(x, axis=-1):
    return torch.softmax(x, dim=axis)

def _gelu(x):
    return torch.nn.functional.gelu(x, approximate='tanh')

def _silu(x):
    return torch.nn.functional.silu(x)

def _sigmoid(x):
    return torch.sigmoid(x)

def _layernorm(x, axis=-1, eps=1e-05):
    m = x.mean(dim=axis, keepdim=True)
    v = x.var(dim=axis, keepdim=True, unbiased=False)
    return (x - m) / torch.sqrt(v + eps)

def _scatter_add(tgt, idx, src, axis=0):
    return tgt.clone().index_add_(axis, idx, src)

def _segment_sum(src, seg, n):
    out = torch.zeros((n,) + src.shape[1:], dtype=src.dtype)
    return out.index_add_(0, seg, src)

def _pad(x, pw):
    flat = tuple((v for pair in reversed(pw) for v in pair))
    return torch.nn.functional.pad(x, flat)
kernel_name = 'standalone__mixer_gmlp__prompt'

def setup_inputs():

    _w = lambda a: (lambda t: t.to(torch.bfloat16) if t.is_floating_point() else t)(torch.as_tensor(a))
    B, S, D, F = (1, 2048, 448, 1792)
    Fh = F // 2
    return {'x': _w((lambda *s: torch.randn(*s, dtype=torch.bfloat16) * 0.05)(B, S, D)), 'w_in': _w((lambda *s: torch.randn(*s, dtype=torch.bfloat16) * 0.4)(D, 2 * Fh)), 'w_sp': _w(0.01 * (lambda *s: torch.randn(*s, dtype=torch.bfloat16) * 0.4)(S, S)), 'b_sp': _w(1.0 + 0.01 * (lambda *s: torch.randn(*s, dtype=torch.bfloat16) * 0.05)(S)), 'w_out': _w((lambda *s: torch.randn(*s, dtype=torch.bfloat16) * 0.05)(Fh, D))}

def reference(x, w_in, w_sp, b_sp, w_out):
    B, S, D = x.shape
    F = w_in.shape[-1]
    Fh = F // 2
    z = _gelu(torch.einsum('bsd,df->bsf', x, w_in))
    z1 = z[..., :Fh].clone()
    z2 = _layernorm(z[..., Fh:], axis=-1)
    g = torch.einsum('ts,bsf->btf', w_sp, z2) + b_sp[None, :, None]
    return torch.einsum('bsf,fd->bsd', z1 * g, w_out)
```

```cpp
#include <hip/hip_runtime.h>
#include <hip/hip_bf16.h>

typedef __bf16 bf16x8 __attribute__((ext_vector_type(8)));
typedef __bf16 bf16x4 __attribute__((ext_vector_type(4)));
typedef __bf16 bf16x2 __attribute__((ext_vector_type(2)));
typedef float f32x4 __attribute__((ext_vector_type(4)));
typedef float f32x2 __attribute__((ext_vector_type(2)));
typedef unsigned u32x4 __attribute__((ext_vector_type(4)));
typedef unsigned u32x2 __attribute__((ext_vector_type(2)));
typedef unsigned short u16;
#define LAS __attribute__((address_space(3)))
#define DI __device__ __forceinline__

constexpr int S_ = 2048, D_ = 448, F_ = 1792, FH = 896;

DI float bf_lo(unsigned u) { return __builtin_bit_cast(float, u << 16); }
DI float bf_hi(unsigned u) { return __builtin_bit_cast(float, u & 0xffff0000u); }
DI unsigned pack2(float a, float b) {
  f32x2 f = {a, b};
  return __builtin_bit_cast(unsigned, __builtin_convertvector(f, bf16x2));
}
DI float bfr(float a) { return (float)(__bf16)a; }
DI float gelu_tanh(float z) {
  const float u = 0.7978845608028654f * (z + 0.044715f * z * z * z);
  const float e = __expf(-2.0f * u);
  return z * __builtin_amdgcn_rcpf(1.0f + e);
}
DI bf16x8 tr_frag(const char* base, int off0, int off1) {
  bf16x4 lo = __builtin_amdgcn_ds_read_tr16_b64_v4bf16((LAS bf16x4*)(base + off0));
  bf16x4 hi = __builtin_amdgcn_ds_read_tr16_b64_v4bf16((LAS bf16x4*)(base + off1));
  return __builtin_shufflevector(lo, hi, 0, 1, 2, 3, 4, 5, 6, 7);
}

constexpr int K1_SLOT = 64 * 448 + 8 * 32;

__global__ __launch_bounds__(256, 1) void k1_gemm_gelu(const u16* __restrict__ x, const u16* __restrict__ w_in,
                                                      u16* __restrict__ z1, u16* __restrict__ z2T,
                                                      f32x2* __restrict__ stats) {
  __shared__ __attribute__((aligned(16))) char smem[2 * K1_SLOT];
  const int bid = blockIdx.x, cb = bid & 7, rb = bid >> 3;
  const int tid = threadIdx.x, l = tid & 63, w = tid >> 6;
  const int s0 = rb * 64 + w * 16, n0 = cb * 224;
  const int g = l >> 4, c16 = l & 15, q = c16 >> 2, p = l & 3;

  bf16x8 xf[14];
  {
    const u16* xp = x + (size_t)(s0 + c16) * D_ + 8 * g;
#pragma unroll
    for (int ks = 0; ks < 14; ++ks) xf[ks] = *(const bf16x8*)(xp + 32 * ks);
  }
  u32x4 wst[7][7];
  int wdst[7];
#pragma unroll
  for (int i = 0; i < 7; ++i) {
    const int c = tid + 256 * i, row = c / 28, cc = c - row * 28;
    wdst[i] = row * 448 + (row >> 3) * 32 + cc * 16;
    const u16* wp = w_in + (size_t)row * F_ + n0 + cc * 8;
#pragma unroll
    for (int st = 0; st < 7; ++st) wst[st][i] = *(const u32x4*)(wp + (size_t)st * 64 * F_);
  }
  f32x4 acc[14];
#pragma unroll
  for (int j = 0; j < 14; ++j) acc[j] = f32x4{0.f, 0.f, 0.f, 0.f};
  const int rbase = (8 * g + q) * 448 + g * 32 + 8 * p;
  const bool is_z2 = cb >= 4;

#pragma unroll
  for (int st = 0; st < 7; ++st) {
    char* slot = smem + (st & 1) * K1_SLOT;
#pragma unroll
    for (int i = 0; i < 7; ++i) *(u32x4*)(slot + wdst[i]) = wst[st][i];
    __syncthreads();
#pragma unroll
    for (int kk = 0; kk < 2; ++kk) {
#pragma unroll
      for (int j = 0; j < 14; ++j) {
        const int o = (32 * kk) * 448 + kk * 128 + 32 * j;
        bf16x8 wf = tr_frag(slot + rbase, o, o + 4 * 448);
        if (is_z2) acc[j] = __builtin_amdgcn_mfma_f32_16x16x32_bf16(xf[2 * st + kk], wf, acc[j], 0, 0, 0);
        else       acc[j] = __builtin_amdgcn_mfma_f32_16x16x32_bf16(wf, xf[2 * st + kk], acc[j], 0, 0, 0);
      }
    }
  }

#pragma unroll
  for (int j = 0; j < 14; ++j)
#pragma unroll
    for (int r = 0; r < 4; ++r) acc[j][r] = bfr(gelu_tanh(bfr(acc[j][r])));

  if (is_z2) {
    const int f0 = n0 - FH;
#pragma unroll
    for (int j = 0; j < 14; ++j) {
      u32x2 v = {pack2(acc[j][0], acc[j][1]), pack2(acc[j][2], acc[j][3])};
      *(u32x2*)(z2T + (size_t)(f0 + 16 * j + c16) * S_ + s0 + 4 * g) = v;
    }
#pragma unroll
    for (int r = 0; r < 4; ++r) {
      float sum = 0.f;
#pragma unroll
      for (int j = 0; j < 14; ++j) sum += acc[j][r];
#pragma unroll
      for (int m = 1; m < 16; m <<= 1) sum += __shfl_xor(sum, m);
      const float mean = sum * (1.0f / 224.0f);
      float m2 = 0.f;
#pragma unroll
      for (int j = 0; j < 14; ++j) { const float d = acc[j][r] - mean; m2 += d * d; }
#pragma unroll
      for (int m = 1; m < 16; m <<= 1) m2 += __shfl_xor(m2, m);
      if (c16 == 0) stats[(size_t)(s0 + 4 * g + r) * 4 + (cb - 4)] = f32x2{sum, m2};
    }
  } else {
#pragma unroll
    for (int j = 0; j < 14; ++j) {
      u32x2 v = {pack2(acc[j][0], acc[j][1]), pack2(acc[j][2], acc[j][3])};
      *(u32x2*)(z1 + (size_t)(s0 + c16) * FH + n0 + 16 * j + 4 * g) = v;
    }
  }
}

constexpr int K2_PD = 2;

__global__ __launch_bounds__(256, 1) void k2_spatial(const u16* __restrict__ w_sp, const u16* __restrict__ z2T,
                                                    const f32x2* __restrict__ stats, const u16* __restrict__ b_sp,
                                                    const u16* __restrict__ z1, u16* __restrict__ h) {
  __shared__ __attribute__((aligned(16))) char smem[16384 + 4 * 3 * 7 * 1024];
  float* rs = (float*)smem;
  float* cs = rs + 2048;
  char* red = smem + 16384;
  const int bid = blockIdx.x, xcd = bid & 7, idx = bid >> 3;
  const int rb = (xcd >> 1) * 8 + (idx & 7), cb = (xcd & 1) * 4 + (idx >> 3);
  const int tid = threadIdx.x, l = tid & 63, w = tid >> 6;
  const int g = l >> 4, c16 = l & 15;
  const int t0 = rb * 64, f0 = cb * 112, kw = w * 512;

  const u16* ap[4];
#pragma unroll
  for (int i = 0; i < 4; ++i) ap[i] = w_sp + (size_t)(t0 + 16 * ((i + w) & 3) + c16) * S_ + kw + 8 * g;
  const u16* bp = z2T + (size_t)(f0 + c16) * S_ + kw + 8 * g;

  constexpr int NB = K2_PD + 1;
  bf16x8 af[NB][4];
  u32x4 bfg[NB][7];
#pragma unroll
  for (int s = 0; s < K2_PD; ++s) {
#pragma unroll
    for (int i = 0; i < 4; ++i) af[s][i] = *(const bf16x8*)(ap[i] + 32 * s);
#pragma unroll
    for (int j = 0; j < 7; ++j) bfg[s][j] = *(const u32x4*)(bp + (size_t)j * 16 * S_ + 32 * s);
  }

#pragma unroll
  for (int i = 0; i < 8; ++i) {
    const int row = kw + l + 64 * i;
    const f32x4 a = *(const f32x4*)(stats + (size_t)row * 4);
    const f32x4 b = *(const f32x4*)(stats + (size_t)row * 4 + 2);
    const float mean = (a[0] + a[2] + b[0] + b[2]) * (1.0f / 896.0f);
    const float d0 = a[0] * (1.0f / 224.0f) - mean, d1 = a[2] * (1.0f / 224.0f) - mean;
    const float d2 = b[0] * (1.0f / 224.0f) - mean, d3 = b[2] * (1.0f / 224.0f) - mean;
    const float m2 = a[1] + a[3] + b[1] + b[3] + 224.0f * (d0 * d0 + d1 * d1 + d2 * d2 + d3 * d3);
    const float var = bfr(m2 * (1.0f / 896.0f));
    const float den = bfr(sqrtf(bfr(var + 1e-5f)));
    const float r = 1.0f / den;
    rs[row] = r;
    cs[row] = -bfr(mean) * r;
  }
  __syncthreads();

  f32x4 acc[4][7];
#pragma unroll
  for (int i = 0; i < 4; ++i)
#pragma unroll
    for (int j = 0; j < 7; ++j) acc[i][j] = f32x4{0.f, 0.f, 0.f, 0.f};

#pragma unroll
  for (int s = 0; s < 16; ++s) {
    if (s + K2_PD < 16) {
      const int sn = s + K2_PD, bn = sn % NB;
#pragma unroll
      for (int i = 0; i < 4; ++i) af[bn][i] = *(const bf16x8*)(ap[i] + 32 * sn);
#pragma unroll
      for (int j = 0; j < 7; ++j) bfg[bn][j] = *(const u32x4*)(bp + (size_t)j * 16 * S_ + 32 * sn);
    }
    const int b = s % NB;
    const f32x4 r0 = *(const f32x4*)(rs + kw + 32 * s + 8 * g), r1 = *(const f32x4*)(rs + kw + 32 * s + 8 * g + 4);
    const f32x4 c0 = *(const f32x4*)(cs + kw + 32 * s + 8 * g), c1 = *(const f32x4*)(cs + kw + 32 * s + 8 * g + 4);
#pragma unroll
    for (int j = 0; j < 7; ++j) {
      const u32x4 u = bfg[b][j];
      u32x4 v;
      v[0] = pack2(bf_lo(u[0]) * r0[0] + c0[0], bf_hi(u[0]) * r0[1] + c0[1]);
      v[1] = pack2(bf_lo(u[1]) * r0[2] + c0[2], bf_hi(u[1]) * r0[3] + c0[3]);
      v[2] = pack2(bf_lo(u[2]) * r1[0] + c1[0], bf_hi(u[2]) * r1[1] + c1[1]);
      v[3] = pack2(bf_lo(u[3]) * r1[2] + c1[2], bf_hi(u[3]) * r1[3] + c1[3]);
      const bf16x8 zf = __builtin_bit_cast(bf16x8, v);
#pragma unroll
      for (int i = 0; i < 4; ++i) acc[i][j] = __builtin_amdgcn_mfma_f32_16x16x32_bf16(zf, af[b][i], acc[i][j], 0, 0, 0);
    }
  }

  const int t = t0 + 16 * w + c16;
  u32x2 z1v[7];
#pragma unroll
  for (int j = 0; j < 7; ++j) z1v[j] = *(const u32x2*)(z1 + (size_t)t * FH + f0 + 16 * j + 4 * g);
  const float bias = bf_lo(b_sp[t]);

#pragma unroll
  for (int i = 1; i < 4; ++i)
#pragma unroll
    for (int j = 0; j < 7; ++j)
      *(f32x4*)(red + ((((w + i) & 3) * 3 + (i - 1)) * 7 + j) * 1024 + l * 16) = acc[i][j];
  __syncthreads();
#pragma unroll
  for (int i = 0; i < 3; ++i)
#pragma unroll
    for (int j = 0; j < 7; ++j) acc[0][j] += *(const f32x4*)(red + ((w * 3 + i) * 7 + j) * 1024 + l * 16);

#pragma unroll
  for (int j = 0; j < 7; ++j) {
    const float h0 = bf_lo(z1v[j][0]) * bfr(bfr(acc[0][j][0]) + bias);
    const float h1 = bf_hi(z1v[j][0]) * bfr(bfr(acc[0][j][1]) + bias);
    const float h2 = bf_lo(z1v[j][1]) * bfr(bfr(acc[0][j][2]) + bias);
    const float h3 = bf_hi(z1v[j][1]) * bfr(bfr(acc[0][j][3]) + bias);
    u32x2 v = {pack2(h0, h1), pack2(h2, h3)};
    *(u32x2*)(h + (size_t)t * FH + f0 + 16 * j + 4 * g) = v;
  }
}

__global__ __launch_bounds__(256, 1) void k3_out(const u16* __restrict__ h, const u16* __restrict__ w_out,
                                                u16* __restrict__ out) {
  __shared__ __attribute__((aligned(16))) char smem[896 * 128];
  const int bid = blockIdx.x, xcd = bid & 7, idx = bid >> 3;
  const int rb = xcd * 4 + idx / 7, nb = idx % 7;
  const int tid = threadIdx.x, l = tid & 63, w = tid >> 6;
  const int g = l >> 4, c16 = l & 15, q = c16 >> 2, p = l & 3;
  const int t0 = rb * 64 + 16 * w, n0 = nb * 64;

  u32x4 wst[28];
  {
    const u16* wp = w_out + (size_t)(tid >> 3) * D_ + n0 + (tid & 7) * 8;
#pragma unroll
    for (int i = 0; i < 28; ++i) wst[i] = *(const u32x4*)(wp + (size_t)i * 32 * D_);
  }
  bf16x8 hf[28];
  {
    const u16* hp = h + (size_t)(t0 + c16) * FH + 8 * g;
#pragma unroll
    for (int ks = 0; ks < 28; ++ks) hf[ks] = *(const bf16x8*)(hp + 32 * ks);
  }
  const int wrow = tid >> 3;
  const int wsw = (((wrow >> 1) & 1) | (((wrow >> 3) & 1) << 1)) << 1;
  const int wdst = wrow * 128 + (((tid & 7) ^ wsw) * 16);
  const int rsw = (((q >> 1) & 1) | ((g & 1) << 1)) << 1;
  int rb4[4];
#pragma unroll
  for (int j = 0; j < 4; ++j) rb4[j] = (8 * g + q) * 128 + (((2 * j + (p >> 1)) ^ rsw) * 16) + 8 * (p & 1);

  f32x4 acc[4];
#pragma unroll
  for (int j = 0; j < 4; ++j) acc[j] = f32x4{0.f, 0.f, 0.f, 0.f};
#pragma unroll
  for (int pc = 0; pc < 4; ++pc) {
#pragma unroll
    for (int i = 0; i < 7; ++i) *(u32x4*)(smem + (pc * 7 + i) * 4096 + wdst) = wst[pc * 7 + i];
    __syncthreads();
#pragma unroll
    for (int kk = 0; kk < 7; ++kk) {
      const int ks = pc * 7 + kk;
#pragma unroll
      for (int j = 0; j < 4; ++j) {
        bf16x8 wf = tr_frag(smem + rb4[j], ks * 4096, ks * 4096 + 512);
        acc[j] = __builtin_amdgcn_mfma_f32_16x16x32_bf16(wf, hf[ks], acc[j], 0, 0, 0);
      }
    }
  }
#pragma unroll
  for (int j = 0; j < 4; ++j) {
    u32x2 v = {pack2(acc[j][0], acc[j][1]), pack2(acc[j][2], acc[j][3])};
    *(u32x2*)(out + (size_t)(t0 + c16) * D_ + n0 + 16 * j + 4 * g) = v;
  }
}

extern "C" void kernel_launch(void* const* d_in, const int* in_sizes, int n_in, void* d_out, int out_size,
                              void* d_ws, size_t ws_size, hipStream_t stream) {
  const u16* x = (const u16*)d_in[0];
  const u16* w_in = (const u16*)d_in[1];
  const u16* w_sp = (const u16*)d_in[2];
  const u16* b_sp = (const u16*)d_in[3];
  const u16* w_out = (const u16*)d_in[4];
  char* ws = (char*)d_ws;
  u16* z1 = (u16*)ws;
  u16* z2T = (u16*)(ws + (size_t)4 * 1024 * 1024);
  u16* h = (u16*)(ws + (size_t)8 * 1024 * 1024);
  f32x2* stats = (f32x2*)(ws + (size_t)12 * 1024 * 1024);
  k1_gemm_gelu<<<256, 256, 0, stream>>>(x, w_in, z1, z2T, stats);
  k2_spatial<<<256, 256, 0, stream>>>(w_sp, z2T, stats, b_sp, z1, h);
  k3_out<<<224, 256, 0, stream>>>(h, w_out, (u16*)d_out);
}
```

```cpp
#include <hip/hip_runtime.h>
#include <hip/hip_bf16.h>

typedef __bf16 bf16x8 __attribute__((ext_vector_type(8)));
typedef __bf16 bf16x4 __attribute__((ext_vector_type(4)));
typedef __bf16 bf16x2 __attribute__((ext_vector_type(2)));
typedef float f32x4 __attribute__((ext_vector_type(4)));
typedef float f32x2 __attribute__((ext_vector_type(2)));
typedef unsigned u32x4 __attribute__((ext_vector_type(4)));
typedef unsigned u32x2 __attribute__((ext_vector_type(2)));
typedef unsigned short u16;
#define LAS __attribute__((address_space(3)))
#define DI __device__ __forceinline__

constexpr int S_ = 2048, D_ = 448, F_ = 1792, FH = 896;

DI float bf_lo(unsigned u) { return __builtin_bit_cast(float, u << 16); }
DI float bf_hi(unsigned u) { return __builtin_bit_cast(float, u & 0xffff0000u); }
DI unsigned pack2(float a, float b) {
  f32x2 f = {a, b};
  return __builtin_bit_cast(unsigned, __builtin_convertvector(f, bf16x2));
}
DI float bfr(float a) { return (float)(__bf16)a; }
DI float gelu_tanh(float z) {
  const float y = z * __builtin_fmaf(z * z, -0.10294324f, -2.3022082f);
  const float e = __builtin_amdgcn_exp2f(y);
  return z * __builtin_amdgcn_rcpf(1.0f + e);
}
DI bf16x8 tr_frag(const char* a0, const char* a1) {
  bf16x4 lo = __builtin_amdgcn_ds_read_tr16_b64_v4bf16((LAS bf16x4*)a0);
  bf16x4 hi = __builtin_amdgcn_ds_read_tr16_b64_v4bf16((LAS bf16x4*)a1);
  return __builtin_shufflevector(lo, hi, 0, 1, 2, 3, 4, 5, 6, 7);
}
template <int CTRL> DI float dpp_mov(float x) {
  return __builtin_bit_cast(float, __builtin_amdgcn_update_dpp(0, __builtin_bit_cast(int, x), CTRL, 0xf, 0xf, true));
}
DI float sum16(float x) {
  x += dpp_mov<0xB1>(x);
  x += dpp_mov<0x4E>(x);
  x += dpp_mov<0x141>(x);
  x += dpp_mov<0x140>(x);
  return x;
}

constexpr int K1_XB = 64 * 896, K1_SLOT = 64 * 448;

__global__ __launch_bounds__(512, 2) void k1_gemm_gelu(const u16* __restrict__ x, const u16* __restrict__ w_in,
                                                      u16* __restrict__ z1F, u16* __restrict__ z2F,
                                                      f32x2* __restrict__ stats) {
  __shared__ __attribute__((aligned(16))) char smem[K1_XB + 2 * K1_SLOT + 4 * 64 * 8];
  const int bid = blockIdx.x, cb = bid & 7, rb = bid >> 3;
  const int tid = threadIdx.x, l = tid & 63, w = tid >> 6, mw = w & 1, nw = w >> 1;
  const int n0 = cb * 224;
  const int g = l >> 4, c16 = l & 15, q = c16 >> 2, p = l & 3;
  const bool is_z2 = cb >= 4;

  u32x4 xst[7];
  {
    const u16* xp = x + (size_t)rb * 64 * D_ + tid * 8;
#pragma unroll
    for (int i = 0; i < 7; ++i) xst[i] = *(const u32x4*)(xp + i * 4096);
  }
  u32x4 wst[7][4];
  int wdst[4];
#pragma unroll
  for (int i = 0; i < 4; ++i) {
    const int c = (i < 3) ? tid + 512 * i : (tid & 255) + 1536;
    const int row = c / 28, cc = c - row * 28;
    wdst[i] = K1_XB + row * 448 + ((cc ^ (((row >> 3) & 1) << 1)) * 16);
    const u16* wp = w_in + (size_t)row * F_ + n0 + cc * 8;
    if (i < 3 || tid < 256) {
#pragma unroll
      for (int st = 0; st < 7; ++st) wst[st][i] = *(const u32x4*)(wp + (size_t)st * 64 * F_);
    }
  }
#pragma unroll
  for (int i = 0; i < 7; ++i) {
    const int c = tid + 512 * i, row = c / 56, ch = c - row * 56;
    const int swz = is_z2 ? (((row >> 1) & 1) | (((row >> 3) & 3) << 1)) : ((row >> 1) & 7);
    *(u32x4*)(smem + row * 896 + ((ch ^ swz) * 16)) = xst[i];
  }
  int xa[2];
#pragma unroll
  for (int rt = 0; rt < 2; ++rt) {
    const int row = 32 * mw + (is_z2 ? (8 * q + 4 * rt + p) : (16 * rt + c16));
    const int swz = is_z2 ? (((row >> 1) & 1) | (((row >> 3) & 3) << 1)) : ((row >> 1) & 7);
    xa[rt] = row * 896 + ((g ^ swz) * 16);
  }
  const int ts = (nw * 7 + 1) >> 1, cnt = 4 - (nw & 1);
  int wa[4];
#pragma unroll
  for (int jj = 0; jj < 4; ++jj) {
    const int j = min(ts + jj, 13);
    wa[jj] = K1_XB + (8 * g + q) * 448 + (j ^ (g & 1)) * 32 + 8 * p;
  }
  f32x4 acc[2][4];
#pragma unroll
  for (int rt = 0; rt < 2; ++rt)
#pragma unroll
    for (int jj = 0; jj < 4; ++jj) acc[rt][jj] = f32x4{0.f, 0.f, 0.f, 0.f};

#pragma unroll
  for (int st = 0; st < 7; ++st) {
    const int so = (st & 1) * K1_SLOT;
#pragma unroll
    for (int i = 0; i < 4; ++i)
      if (i < 3 || tid < 256) *(u32x4*)(smem + so + wdst[i]) = wst[st][i];
    __syncthreads();
#pragma unroll
    for (int kk = 0; kk < 2; ++kk) {
      bf16x8 xf[2];
#pragma unroll
      for (int rt = 0; rt < 2; ++rt) xf[rt] = *(const bf16x8*)(smem + (xa[rt] ^ (64 * kk)) + 128 * st);
#pragma unroll
      for (int jj = 0; jj < 4; ++jj) {
        const char* a = smem + so + wa[jj] + 32 * kk * 448;
        const bf16x8 wf = tr_frag(a, a + 4 * 448);
#pragma unroll
        for (int rt = 0; rt < 2; ++rt) {
          if (is_z2) acc[rt][jj] = __builtin_amdgcn_mfma_f32_16x16x32_bf16(xf[rt], wf, acc[rt][jj], 0, 0, 0);
          else       acc[rt][jj] = __builtin_amdgcn_mfma_f32_16x16x32_bf16(wf, xf[rt], acc[rt][jj], 0, 0, 0);
        }
      }
    }
  }

#pragma unroll
  for (int rt = 0; rt < 2; ++rt)
#pragma unroll
    for (int jj = 0; jj < 4; ++jj)
      if (jj < 3 || cnt == 4) {
#pragma unroll
        for (int r = 0; r < 4; ++r) acc[rt][jj][r] = bfr(gelu_tanh(bfr(acc[rt][jj][r])));
      }

  if (is_z2) {
    const int kstep = rb * 2 + mw;
#pragma unroll
    for (int jj = 0; jj < 4; ++jj)
      if (jj < 3 || cnt == 4) {
        const u32x4 v = {pack2(acc[0][jj][0], acc[0][jj][1]), pack2(acc[0][jj][2], acc[0][jj][3]),
                         pack2(acc[1][jj][0], acc[1][jj][1]), pack2(acc[1][jj][2], acc[1][jj][3])};
        const int ft = (cb - 4) * 14 + ts + jj;
        *(u32x4*)(z2F + ((size_t)(ft * 64 + kstep) * 64 + l) * 8) = v;
      }
    f32x2* sst = (f32x2*)(smem + K1_XB + 2 * K1_SLOT);
    const float inv_n = (cnt == 4) ? (1.0f / 64.0f) : (1.0f / 48.0f);
#pragma unroll
    for (int rt = 0; rt < 2; ++rt)
#pragma unroll
      for (int r = 0; r < 4; ++r) {
        float sum = acc[rt][0][r] + acc[rt][1][r] + acc[rt][2][r];
        if (cnt == 4) sum += acc[rt][3][r];
        sum = sum16(sum);
        const float mean = sum * inv_n;
        const float d0 = acc[rt][0][r] - mean, d1 = acc[rt][1][r] - mean, d2 = acc[rt][2][r] - mean;
        float m2 = d0 * d0 + d1 * d1 + d2 * d2;
        if (cnt == 4) { const float d3 = acc[rt][3][r] - mean; m2 += d3 * d3; }
        m2 = sum16(m2);
        if (c16 == 0) sst[nw * 64 + 32 * mw + 8 * g + 4 * rt + r] = f32x2{sum, m2};
      }
    __syncthreads();
    if (tid < 64) {
      const f32x2 a = sst[tid], b = sst[64 + tid], c = sst[128 + tid], d = sst[192 + tid];
      const float sum = a[0] + b[0] + c[0] + d[0], mean = sum * (1.0f / 224.0f);
      const float da = a[0] * (1.0f / 64.0f) - mean, db = b[0] * (1.0f / 48.0f) - mean;
      const float dc = c[0] * (1.0f / 64.0f) - mean, dd = d[0] * (1.0f / 48.0f) - mean;
      const float m2 = a[1] + b[1] + c[1] + d[1] + 64.0f * (da * da + dc * dc) + 48.0f * (db * db + dd * dd);
      stats[(size_t)(rb * 64 + tid) * 4 + (cb - 4)] = f32x2{sum, m2};
    }
  } else {
#pragma unroll
    for (int rt = 0; rt < 2; ++rt)
#pragma unroll
      for (int jj = 0; jj < 4; ++jj)
        if (jj < 3 || cnt == 4) {
          const u32x2 v = {pack2(acc[rt][jj][0], acc[rt][jj][1]), pack2(acc[rt][jj][2], acc[rt][jj][3])};
          const int tt = rb * 4 + mw * 2 + rt, ft = cb * 14 + ts + jj;
          *(u32x2*)(z1F + ((size_t)(tt * 56 + ft) * 64 + l) * 4) = v;
        }
  }
}

__global__ __launch_bounds__(256, 1) void k2_spatial(const u16* __restrict__ w_sp, const u16* __restrict__ z2F,
                                                    const f32x2* __restrict__ stats, const u16* __restrict__ b_sp,
                                                    const u16* __restrict__ z1F, u16* __restrict__ hF) {
  __shared__ __attribute__((aligned(16))) char smem[16384 + 4 * 8192 + 4 * 3 * 7 * 1024];
  float* rs = (float*)smem;
  float* cs = rs + 2048;
  char* red = smem + 16384 + 4 * 8192;
  const int bid = blockIdx.x, xcd = bid & 7, idx = bid >> 3;
  const int rb = (xcd >> 1) * 8 + (idx & 7), cb = (xcd & 1) * 4 + (idx >> 3);
  const int tid = threadIdx.x, l = tid & 63, w = __builtin_amdgcn_readfirstlane(tid >> 6);
  const int g = l >> 4, c16 = l & 15, r8 = l >> 3, c8 = l & 7;
  const int t0 = rb * 64, kw = w * 512;
  char* abuf = smem + 16384 + w * 8192;

  const u16* ap = w_sp + (size_t)(t0 + r8) * S_ + kw + 8 * c8;
  const u16* bp = z2F + ((size_t)(cb * 7 * 64 + w * 16) * 64 + l) * 8;
  const int awr = r8 * 128 + ((c8 ^ (r8 >> 1)) * 16);
  int ard[4];
#pragma unroll
  for (int i = 0; i < 4; ++i) ard[i] = (16 * ((i + w) & 3) + c16) * 128 + ((g ^ (c16 >> 1)) * 16);

  u32x4 ast[8];
  u32x4 bst[2][14];
#pragma unroll
  for (int i = 0; i < 8; ++i) ast[i] = *(const u32x4*)(ap + (size_t)i * 8 * S_);
#pragma unroll
  for (int j = 0; j < 7; ++j)
#pragma unroll
    for (int kk = 0; kk < 2; ++kk) bst[0][2 * j + kk] = *(const u32x4*)(bp + (size_t)j * 64 * 512 + kk * 512);

#pragma unroll
  for (int i = 0; i < 8; ++i) {
    const int row = kw + l + 64 * i;
    const f32x4 a = *(const f32x4*)(stats + (size_t)row * 4);
    const f32x4 b = *(const f32x4*)(stats + (size_t)row * 4 + 2);
    const float mean = (a[0] + a[2] + b[0] + b[2]) * (1.0f / 896.0f);
    const float d0 = a[0] * (1.0f / 224.0f) - mean, d1 = a[2] * (1.0f / 224.0f) - mean;
    const float d2 = b[0] * (1.0f / 224.0f) - mean, d3 = b[2] * (1.0f / 224.0f) - mean;
    const float m2 = a[1] + a[3] + b[1] + b[3] + 224.0f * (d0 * d0 + d1 * d1 + d2 * d2 + d3 * d3);
    const float var = bfr(m2 * (1.0f / 896.0f));
    const float den = bfr(sqrtf(bfr(var + 1e-5f)));
    const float r = 1.0f / den;
    rs[row] = r;
    cs[row] = -bfr(mean) * r;
  }
#pragma unroll
  for (int i = 0; i < 8; ++i) *(u32x4*)(abuf + ((awr ^ (64 * (i & 1))) + 1024 * i)) = ast[i];

  f32x4 acc[4][7];
#pragma unroll
  for (int i = 0; i < 4; ++i)
#pragma unroll
    for (int j = 0; j < 7; ++j) acc[i][j] = f32x4{0.f, 0.f, 0.f, 0.f};

#pragma unroll 2
  for (int s = 0; s < 8; ++s) {
    const int b = s & 1;
    if (s + 1 < 8) {
#pragma unroll
      for (int i = 0; i < 8; ++i) ast[i] = *(const u32x4*)(ap + (size_t)i * 8 * S_ + 64 * (s + 1));
#pragma unroll
      for (int j = 0; j < 7; ++j)
#pragma unroll
        for (int kk = 0; kk < 2; ++kk)
          bst[b ^ 1][2 * j + kk] = *(const u32x4*)(bp + (size_t)j * 64 * 512 + (2 * (s + 1) + kk) * 512);
    }
#pragma unroll
    for (int kk = 0; kk < 2; ++kk) {
      bf16x8 af[4];
#pragma unroll
      for (int i = 0; i < 4; ++i) af[i] = *(const bf16x8*)(abuf + (ard[i] ^ (64 * kk)));
      const int ko = kw + 64 * s + 32 * kk + 8 * g;
      const f32x4 r0 = *(const f32x4*)(rs + ko), r1 = *(const f32x4*)(rs + ko + 4);
      const f32x4 c0 = *(const f32x4*)(cs + ko), c1 = *(const f32x4*)(cs + ko + 4);
#pragma unroll
      for (int j = 0; j < 7; ++j) {
        const u32x4 u = bst[b][2 * j + kk];
        u32x4 v;
        v[0] = pack2(bf_lo(u[0]) * r0[0] + c0[0], bf_hi(u[0]) * r0[1] + c0[1]);
        v[1] = pack2(bf_lo(u[1]) * r0[2] + c0[2], bf_hi(u[1]) * r0[3] + c0[3]);
        v[2] = pack2(bf_lo(u[2]) * r1[0] + c1[0], bf_hi(u[2]) * r1[1] + c1[1]);
        v[3] = pack2(bf_lo(u[3]) * r1[2] + c1[2], bf_hi(u[3]) * r1[3] + c1[3]);
        const bf16x8 zf = __builtin_bit_cast(bf16x8, v);
#pragma unroll
        for (int i = 0; i < 4; ++i) acc[i][j] = __builtin_amdgcn_mfma_f32_16x16x32_bf16(zf, af[i], acc[i][j], 0, 0, 0);
      }
    }
    if (s + 1 < 8) {
#pragma unroll
      for (int i = 0; i < 8; ++i) *(u32x4*)(abuf + ((awr ^ (64 * (i & 1))) + 1024 * i)) = ast[i];
    }
  }

  const int tt = rb * 4 + w;
  u32x2 z1v[7];
#pragma unroll
  for (int j = 0; j < 7; ++j) z1v[j] = *(const u32x2*)(z1F + ((size_t)(tt * 56 + cb * 7 + j) * 64 + l) * 4);
  const float bias = bf_lo(b_sp[tt * 16 + c16]);

#pragma unroll
  for (int i = 1; i < 4; ++i)
#pragma unroll
    for (int j = 0; j < 7; ++j)
      *(f32x4*)(red + ((((w + i) & 3) * 3 + (i - 1)) * 7 + j) * 1024 + l * 16) = acc[i][j];
  __syncthreads();
#pragma unroll
  for (int i = 0; i < 3; ++i)
#pragma unroll
    for (int j = 0; j < 7; ++j) acc[0][j] += *(const f32x4*)(red + ((w * 3 + i) * 7 + j) * 1024 + l * 16);

  u32x2 hv[7];
#pragma unroll
  for (int j = 0; j < 7; ++j) {
    const float h0 = bf_lo(z1v[j][0]) * bfr(bfr(acc[0][j][0]) + bias);
    const float h1 = bf_hi(z1v[j][0]) * bfr(bfr(acc[0][j][1]) + bias);
    const float h2 = bf_lo(z1v[j][1]) * bfr(bfr(acc[0][j][2]) + bias);
    const float h3 = bf_hi(z1v[j][1]) * bfr(bfr(acc[0][j][3]) + bias);
    hv[j] = u32x2{pack2(h0, h1), pack2(h2, h3)};
  }
  u16* hp = hF + ((size_t)(tt * 28 + 7 * (cb >> 1)) * 64 + l) * 8;
#pragma unroll
  for (int jp = 0; jp < 3; ++jp)
    *(u32x4*)(hp + (size_t)(3 * (cb & 1) + jp) * 512) = u32x4{hv[2 * jp][0], hv[2 * jp][1], hv[2 * jp + 1][0], hv[2 * jp + 1][1]};
  *(u32x2*)(hp + 6 * 512 + 4 * (cb & 1)) = hv[6];
}

__global__ __launch_bounds__(256, 1) void k3_out(const u16* __restrict__ hF, const u16* __restrict__ w_out,
                                                u16* __restrict__ out) {
  __shared__ __attribute__((aligned(16))) char smem[896 * 128];
  const int bid = blockIdx.x, xcd = bid & 7, idx = bid >> 3;
  const int rb = xcd * 4 + idx / 7, nb = idx % 7;
  const int tid = threadIdx.x, l = tid & 63, w = tid >> 6;
  const int g = l >> 4, c16 = l & 15, q = c16 >> 2, p = l & 3;
  const int tt = rb * 4 + w, n0 = nb * 64;

  u32x4 wst[28];
  {
    const u16* wp = w_out + (size_t)(tid >> 3) * D_ + n0 + (tid & 7) * 8;
#pragma unroll
    for (int i = 0; i < 28; ++i) wst[i] = *(const u32x4*)(wp + (size_t)i * 32 * D_);
  }
  bf16x8 hf[28];
  {
    const u16* hp = hF + ((size_t)tt * 28 * 64 + l) * 8;
#pragma unroll
    for (int ks = 0; ks < 28; ++ks) hf[ks] = *(const bf16x8*)(hp + ks * 512);
  }
  const int wrow = tid >> 3;
  const int wdst = wrow * 128 + (((tid & 7) ^ (((wrow >> 1) & 3) << 1)) * 16);
  const int rrow = 4 * g + q;
  int rb4[4];
#pragma unroll
  for (int j = 0; j < 4; ++j) rb4[j] = rrow * 128 + ((j ^ ((rrow >> 1) & 3)) * 32) + 8 * p;

  f32x4 acc[4];
#pragma unroll
  for (int j = 0; j < 4; ++j) acc[j] = f32x4{0.f, 0.f, 0.f, 0.f};
#pragma unroll
  for (int pc = 0; pc < 4; ++pc) {
#pragma unroll
    for (int i = 0; i < 7; ++i) *(u32x4*)(smem + (pc * 7 + i) * 4096 + wdst) = wst[pc * 7 + i];
    __syncthreads();
#pragma unroll
    for (int e = 0; e < 7; ++e) {
      const int tA = (e < 6) ? (14 * pc + 7 * (e / 3) + 2 * (e % 3)) : (14 * pc + 6);
      const int tB = (e < 6) ? (tA + 1) : (14 * pc + 13);
#pragma unroll
      for (int j = 0; j < 4; ++j) {
        const bf16x8 wf = tr_frag(smem + rb4[j] + tA * 2048, smem + rb4[j] + tB * 2048);
        acc[j] = __builtin_amdgcn_mfma_f32_16x16x32_bf16(wf, hf[pc * 7 + e], acc[j], 0, 0, 0);
      }
    }
  }
#pragma unroll
  for (int j = 0; j < 4; ++j) {
    const u32x2 v = {pack2(acc[j][0], acc[j][1]), pack2(acc[j][2], acc[j][3])};
    *(u32x2*)(out + (size_t)(tt * 16 + c16) * D_ + n0 + 16 * j + 4 * g) = v;
  }
}

extern "C" void kernel_launch(void* const* d_in, const int* in_sizes, int n_in, void* d_out, int out_size,
                              void* d_ws, size_t ws_size, hipStream_t stream) {
  const u16* x = (const u16*)d_in[0];
  const u16* w_in = (const u16*)d_in[1];
  const u16* w_sp = (const u16*)d_in[2];
  const u16* b_sp = (const u16*)d_in[3];
  const u16* w_out = (const u16*)d_in[4];
  char* ws = (char*)d_ws;
  u16* z1F = (u16*)ws;
  u16* z2F = (u16*)(ws + (size_t)4 * 1024 * 1024);
  u16* hF = (u16*)(ws + (size_t)8 * 1024 * 1024);
  f32x2* stats = (f32x2*)(ws + (size_t)12 * 1024 * 1024);
  k1_gemm_gelu<<<256, 512, 0, stream>>>(x, w_in, z1F, z2F, stats);
  k2_spatial<<<256, 256, 0, stream>>>(w_sp, z2F, stats, b_sp, z1F, hF);
  k3_out<<<224, 256, 0, stream>>>(hF, w_out, (u16*)d_out);
}
```

```cpp
#include <hip/hip_runtime.h>
#include <hip/hip_bf16.h>

typedef __bf16 bf16x8 __attribute__((ext_vector_type(8)));
typedef __bf16 bf16x4 __attribute__((ext_vector_type(4)));
typedef __bf16 bf16x2 __attribute__((ext_vector_type(2)));
typedef float f32x4 __attribute__((ext_vector_type(4)));
typedef float f32x2 __attribute__((ext_vector_type(2)));
typedef unsigned u32x4 __attribute__((ext_vector_type(4)));
typedef unsigned u32x2 __attribute__((ext_vector_type(2)));
typedef unsigned short u16;
#define LAS __attribute__((address_space(3)))
#define DI __device__ __forceinline__

#define STAMP(k, i)
#define STAMPR(k, i)
constexpr int S_ = 2048, D_ = 448, F_ = 1792, FH = 896;

DI float bf_lo(unsigned u) { return __builtin_bit_cast(float, u << 16); }
DI float bf_hi(unsigned u) { return __builtin_bit_cast(float, u & 0xffff0000u); }
DI unsigned pack2(float a, float b) {
  f32x2 f = {a, b};
  return __builtin_bit_cast(unsigned, __builtin_convertvector(f, bf16x2));
}
DI float bfr(float a) { return (float)(__bf16)a; }
DI float gelu_tanh(float z) {
  const float y = z * __builtin_fmaf(z * z, -0.10294324f, -2.3022082f);
  const float e = __builtin_amdgcn_exp2f(y);
  return z * __builtin_amdgcn_rcpf(1.0f + e);
}
DI bf16x8 tr_frag(const char* a0, const char* a1) {
  bf16x4 lo = __builtin_amdgcn_ds_read_tr16_b64_v4bf16((LAS bf16x4*)a0);
  bf16x4 hi = __builtin_amdgcn_ds_read_tr16_b64_v4bf16((LAS bf16x4*)a1);
  return __builtin_shufflevector(lo, hi, 0, 1, 2, 3, 4, 5, 6, 7);
}
template <int CTRL> DI float dpp_mov(float x) {
  return __builtin_bit_cast(float, __builtin_amdgcn_update_dpp(0, __builtin_bit_cast(int, x), CTRL, 0xf, 0xf, true));
}
DI float sum16(float x) {
  x += dpp_mov<0xB1>(x);
  x += dpp_mov<0x4E>(x);
  x += dpp_mov<0x141>(x);
  x += dpp_mov<0x140>(x);
  return x;
}

constexpr int XW_XB = 64 * 896, XW_WB = 448 * 224, XW_LDS = XW_XB + XW_WB;

template <bool Z2> DI int x_swz(int row) { return Z2 ? (((row >> 1) & 1) | (((row >> 3) & 3) << 1)) : ((row >> 1) & 7); }

struct XwStage {
  u32x4 xst[7];
  u32x4 wst[13];
};
DI void xw_issue_x(XwStage& sg, const u16* x, int rb, int tid) {
  const u16* xp = x + (size_t)rb * 64 * D_ + tid * 8;
#pragma unroll
  for (int i = 0; i < 7; ++i) sg.xst[i] = *(const u32x4*)(xp + i * 4096);
}
DI void xw_issue_w(XwStage& sg, const u16* w_in, int n0, int tid, int i) {
  const int c = min(tid + 512 * i, 6271), row = c / 14, cc = c - row * 14;
  sg.wst[i] = *(const u32x4*)(w_in + (size_t)row * F_ + n0 + cc * 8);
}
template <bool Z2>
DI void xw_gemm(XwStage& sg, const u16* w_in, int n0, char* smem, int tid, int fg, int mh, f32x4 (&acc)[2][2]) {
  const int l = tid & 63, g = l >> 4, c16 = l & 15, q = c16 >> 2, p = l & 3;
#pragma unroll
  for (int i = 0; i < 7; ++i) {
    const int c = tid + 512 * i, row = c / 56, ch = c - row * 56;
    *(u32x4*)(smem + row * 896 + ((ch ^ x_swz<Z2>(row)) * 16)) = sg.xst[i];
  }
  int xa[2], wa[2];
#pragma unroll
  for (int rt = 0; rt < 2; ++rt) {
    const int row = 32 * mh + (Z2 ? (8 * q + 4 * rt + p) : (16 * rt + c16));
    xa[rt] = row * 896 + (((g >> 1) ^ x_swz<Z2>(row)) * 16) + (g & 1) * 8;
  }
#pragma unroll
  for (int jj = 0; jj < 2; ++jj) wa[jj] = XW_XB + (4 * g + q) * 224 + min(2 * fg + jj, 6) * 32 + 8 * p;
#pragma unroll
  for (int rt = 0; rt < 2; ++rt)
#pragma unroll
    for (int jj = 0; jj < 2; ++jj) acc[rt][jj] = f32x4{0.f, 0.f, 0.f, 0.f};
  constexpr int bend[7] = {2, 4, 6, 7, 9, 11, 13};
#pragma unroll
  for (int st = 0; st < 7; ++st) {
#pragma unroll
    for (int i = (st ? bend[st - 1] : 0); i < bend[st]; ++i) *(u32x4*)(smem + XW_XB + min(tid + 512 * i, 6271) * 16) = sg.wst[i];
    if (st + 3 < 7) {
#pragma unroll
      for (int i = bend[st + 2]; i < bend[st + 3]; ++i) xw_issue_w(sg, w_in, n0, tid, i);
    }
    __syncthreads();
#pragma unroll
    for (int kk = 0; kk < 2; ++kk) {
      bf16x8 xf[2], wf[2];
#pragma unroll
      for (int rt = 0; rt < 2; ++rt) {
        const int o = (xa[rt] ^ (64 * kk)) + 128 * st;
        const bf16x4 lo = *(const bf16x4*)(smem + o), hi = *(const bf16x4*)(smem + (o ^ 32));
        xf[rt] = __builtin_shufflevector(lo, hi, 0, 1, 2, 3, 4, 5, 6, 7);
      }
#pragma unroll
      for (int jj = 0; jj < 2; ++jj) {
        const char* a = smem + wa[jj] + (64 * st + 32 * kk) * 224;
        wf[jj] = tr_frag(a, a + 16 * 224);
      }
#pragma unroll
      for (int rt = 0; rt < 2; ++rt)
#pragma unroll
        for (int jj = 0; jj < 2; ++jj) {
          if (Z2) acc[rt][jj] = __builtin_amdgcn_mfma_f32_16x16x32_bf16(xf[rt], wf[jj], acc[rt][jj], 0, 0, 0);
          else    acc[rt][jj] = __builtin_amdgcn_mfma_f32_16x16x32_bf16(wf[jj], xf[rt], acc[rt][jj], 0, 0, 0);
        }
    }
  }
#pragma unroll
  for (int rt = 0; rt < 2; ++rt)
#pragma unroll
    for (int jj = 0; jj < 2; ++jj)
#pragma unroll
      for (int r = 0; r < 4; ++r) acc[rt][jj][r] = bfr(gelu_tanh(bfr(acc[rt][jj][r])));
}

DI void tile_of_block(int bid, int& rb, int& cb) {
  const int xcd = bid & 7, idx = bid >> 3;
  rb = (xcd >> 1) * 8 + (idx & 7);
  cb = (xcd & 1) * 4 + (idx >> 3);
}

__global__ __launch_bounds__(512, 2) void k1_z2(const u16* __restrict__ x, const u16* __restrict__ w_in,
                                               u16* __restrict__ z2R, f32x2* __restrict__ stats,
                                               unsigned* __restrict__ flags, const u16* __restrict__ w_sp,
                                               const u16* __restrict__ w_out) {
  __shared__ __attribute__((aligned(16))) char smem[XW_LDS + 4 * 64 * 8];
  const int bid = blockIdx.x;
  int rb, cb;
  tile_of_block(bid, rb, cb);
  const int tid = threadIdx.x, l = tid & 63, w = __builtin_amdgcn_readfirstlane(tid >> 6), fg = w & 3, mh = w >> 2;
  const int g = l >> 4, c16 = l & 15;
  const bool two = fg < 3;
  STAMPR(0, 0); STAMP(0, 1);
  if (tid == 0) flags[bid] = 0u;
  XwStage sg;
  xw_issue_x(sg, x, rb, tid);
#pragma unroll
  for (int i = 0; i < 6; ++i) xw_issue_w(sg, w_in, FH + cb * 112, tid, i);
  u32x4 pf[5];
#pragma unroll
  for (int i = 0; i < 4; ++i) pf[i] = *(const u32x4*)(w_sp + ((size_t)bid * 2048 + i * 512 + tid) * 8);
  pf[4] = *(const u32x4*)(w_out + ((size_t)bid * 196 + min(tid, 195)) * 8);
  STAMP(0, 2);
  f32x4 acc[2][2];
  xw_gemm<true>(sg, w_in, FH + cb * 112, smem, tid, fg, mh, acc);
  STAMP(0, 3);

  const int kstep = rb * 2 + mh;
#pragma unroll
  for (int jj = 0; jj < 2; ++jj)
    if (jj == 0 || two) {
      const u32x4 v = {pack2(acc[0][jj][0], acc[0][jj][1]), pack2(acc[0][jj][2], acc[0][jj][3]),
                       pack2(acc[1][jj][0], acc[1][jj][1]), pack2(acc[1][jj][2], acc[1][jj][3])};
      *(u32x4*)(z2R + ((size_t)((cb * 7 + 2 * fg + jj) * 64 + kstep) * 64 + l) * 8) = v;
    }
  f32x2* sst = (f32x2*)(smem + XW_LDS);
  const float inv_n = two ? (1.0f / 32.0f) : (1.0f / 16.0f);
#pragma unroll
  for (int rt = 0; rt < 2; ++rt)
#pragma unroll
    for (int r = 0; r < 4; ++r) {
      const float a0 = acc[rt][0][r], a1 = two ? acc[rt][1][r] : 0.f;
      const float sum = sum16(a0 + a1);
      const float mean = sum * inv_n;
      const float d0 = a0 - mean, d1 = two ? a1 - mean : 0.f;
      const float m2 = sum16(d0 * d0 + d1 * d1);
      if (c16 == 0) sst[fg * 64 + 32 * mh + 8 * g + 4 * rt + r] = f32x2{sum, m2};
    }
  __syncthreads();
  if (tid < 64) {
    const f32x2 a = sst[tid], b = sst[64 + tid], c = sst[128 + tid], d = sst[192 + tid];
    const float sum = a[0] + b[0] + c[0] + d[0], mean = sum * (1.0f / 112.0f);
    const float da = a[0] * (1.0f / 32.0f) - mean, db = b[0] * (1.0f / 32.0f) - mean;
    const float dc = c[0] * (1.0f / 32.0f) - mean, dd = d[0] * (1.0f / 16.0f) - mean;
    const float m2 = a[1] + b[1] + c[1] + d[1] + 32.0f * (da * da + db * db + dc * dc) + 16.0f * dd * dd;
    stats[(size_t)(rb * 64 + tid) * 8 + cb] = f32x2{sum, m2};
  }
#pragma unroll
  for (int i = 0; i < 5; ++i) asm volatile("" ::"v"(pf[i]));
  STAMP(0, 4); STAMPR(0, 15);
}

typedef __attribute__((address_space(1))) unsigned gu32;
#define RLX_AGENT __ATOMIC_RELAXED, __HIP_MEMORY_SCOPE_AGENT

__global__ __launch_bounds__(512, 2) void k2_spatial(const u16* __restrict__ x, const u16* __restrict__ w_in,
                                                    const u16* __restrict__ w_sp, const u16* __restrict__ z2R,
                                                    const f32x2* __restrict__ stats, const u16* __restrict__ b_sp,
                                                    u16* z2N, unsigned* flags, u16* __restrict__ hF) {
  __shared__ __attribute__((aligned(16))) char smem[XW_LDS + 64 * 8];
  char* abuf = smem;
  const int bid = blockIdx.x;
  int rb, cb;
  tile_of_block(bid, rb, cb);
  const int tid = threadIdx.x, l = tid & 63, w = __builtin_amdgcn_readfirstlane(tid >> 6);
  const int fg = w & 3, kg = w >> 2;
  const bool two = fg < 3;
  const int g = l >> 4, c16 = l & 15;
  const int t0 = rb * 64;
  STAMPR(1, 0); STAMP(1, 1);
  const __amdgpu_buffer_rsrc_t rsN = __builtin_amdgcn_make_buffer_rsrc((void*)z2N, 0, S_ * FH * 2, 0x00020000);

  XwStage sg;
  {
    const int wj = min(w, 6);
    const unsigned zoff = (unsigned)(((cb * 7 + wj) * 64 + 2 * rb) * 64 + l) * 16u;
    const u32x4 zr0 = *(const u32x4*)((const char*)z2R + zoff), zr1 = *(const u32x4*)((const char*)z2R + zoff + 1024);
    f32x4 sp[4];
#pragma unroll
    for (int i = 0; i < 4; ++i) sp[i] = *(const f32x4*)(stats + (size_t)(t0 + l) * 8 + 2 * i);
    xw_issue_x(sg, x, rb, tid);
#pragma unroll
    for (int i = 0; i < 6; ++i) xw_issue_w(sg, w_in, cb * 112, tid, i);
    f32x2* mrs = (f32x2*)(smem + XW_LDS);
    if (w == 7) {
      float sum = 0.f, m2 = 0.f;
#pragma unroll
      for (int i = 0; i < 4; ++i) { sum += sp[i][0] + sp[i][2]; m2 += sp[i][1] + sp[i][3]; }
      const float mean = sum * (1.0f / 896.0f);
#pragma unroll
      for (int i = 0; i < 4; ++i) {
        const float d0 = sp[i][0] * (1.0f / 112.0f) - mean, d1 = sp[i][2] * (1.0f / 112.0f) - mean;
        m2 += 112.0f * (d0 * d0 + d1 * d1);
      }
      const float var = bfr(m2 * (1.0f / 896.0f));
      const float den = bfr(sqrtf(bfr(var + 1e-5f)));
      mrs[l] = f32x2{bfr(mean), den};
    }
    __syncthreads();
    if (w < 7) {
#pragma unroll
      for (int hh = 0; hh < 2; ++hh) {
        const u32x4 u = hh ? zr1 : zr0;
        u32x4 v;
#pragma unroll
        for (int e = 0; e < 4; ++e) {
          const f32x4 mr = *(const f32x4*)(mrs + 32 * hh + 8 * g + 2 * e);
          v[e] = pack2(bfr(bf_lo(u[e]) - mr[0]) / mr[1], bfr(bf_hi(u[e]) - mr[2]) / mr[3]);
        }
        __builtin_amdgcn_raw_buffer_store_b128(v, rsN, zoff + 1024u * hh, 0, 16);
      }
    }
    asm volatile("s_waitcnt vmcnt(0)" ::: "memory");
    __syncthreads();
    if (tid == 0) __hip_atomic_store((gu32*)flags + cb * 32 + rb, 1u, RLX_AGENT);
  }
  STAMP(1, 2);

  u32x2 z1p[2][2];
  {
    f32x4 za[2][2];
    xw_gemm<false>(sg, w_in, cb * 112, smem, tid, fg, kg, za);
#pragma unroll
    for (int i = 0; i < 2; ++i)
#pragma unroll
      for (int j = 0; j < 2; ++j) z1p[i][j] = u32x2{pack2(za[i][j][0], za[i][j][1]), pack2(za[i][j][2], za[i][j][3])};
  }
  STAMP(1, 3);

  const int koff = (2 * (rb & 7) + (cb & 1)) & 15;
  const __amdgpu_buffer_rsrc_t rsA = __builtin_amdgcn_make_buffer_rsrc((void*)w_sp, 0, S_ * S_ * 2, 0x00020000);
  const __amdgpu_buffer_rsrc_t rsN1 = __builtin_amdgcn_make_buffer_rsrc((void*)z2N, 0, two ? S_ * FH * 2 : 0, 0x00020000);
  const int arow = tid >> 4, ach = tid & 15;
  const unsigned aoff = (unsigned)((t0 + arow) * S_ + 8 * ach) * 2u;
  const unsigned boff = (unsigned)(((cb * 7 + 2 * fg) * 64 + kg) * 64 + l) * 16u;
  const int awr = arow * 256 + ((ach ^ (arow & 15)) * 16);
  int ard[4];
#pragma unroll
  for (int i = 0; i < 4; ++i) ard[i] = (16 * ((i + 2 * kg) & 3) + c16) * 256 + (((4 * kg + g) ^ c16) * 16);

  u32x4 ast[4][2];
  u32x4 bst[4][4];
#define K2_LOAD_A(slot, st)                                                                       \
  {                                                                                               \
    const int sk_ = ((st) + koff) & 15;                                                           \
    ast[slot][0] = __builtin_amdgcn_raw_buffer_load_b128(rsA, aoff, 256 * sk_, 0);                \
    ast[slot][1] = __builtin_amdgcn_raw_buffer_load_b128(rsA, aoff + 32 * S_ * 2, 256 * sk_, 0);  \
  }
#define K2_LOAD_B(slot, st)                                                                       \
  {                                                                                               \
    const int sk_ = ((st) + koff) & 15;                                                           \
    bst[slot][0] = __builtin_amdgcn_raw_buffer_load_b128(rsN, boff, 4096 * sk_, 16);              \
    bst[slot][1] = __builtin_amdgcn_raw_buffer_load_b128(rsN1, boff + 65536, 4096 * sk_, 16);     \
    bst[slot][2] = __builtin_amdgcn_raw_buffer_load_b128(rsN, boff + 2048, 4096 * sk_, 16);       \
    bst[slot][3] = __builtin_amdgcn_raw_buffer_load_b128(rsN1, boff + 65536 + 2048, 4096 * sk_, 16); \
  }
  K2_LOAD_A(0, 0) K2_LOAD_A(1, 1) K2_LOAD_A(2, 2)
  if (w == 0) {
    const gu32* fp = (const gu32*)flags + cb * 32 + (l & 31);
    for (unsigned spins = 0; spins < (1u << 22); ++spins) {
      if (__all(__hip_atomic_load(fp, RLX_AGENT) == 1u)) break;
      __builtin_amdgcn_s_sleep(1);
    }
  }
  __syncthreads();
  __builtin_amdgcn_fence(__ATOMIC_ACQUIRE, "wavefront");
  K2_LOAD_B(0, 0) K2_LOAD_B(1, 1) K2_LOAD_B(2, 2)
#pragma unroll
  for (int i = 0; i < 2; ++i) *(u32x4*)(abuf + awr + 8192 * i) = ast[0][i];
  __syncthreads();
  STAMP(1, 4);

  f32x4 acc[4][2];
#pragma unroll
  for (int i = 0; i < 4; ++i)
#pragma unroll
    for (int j = 0; j < 2; ++j) acc[i][j] = f32x4{0.f, 0.f, 0.f, 0.f};

#define K2_STAGE(u, st, PF, WR)                                                                        \
  {                                                                                                    \
    if (PF) { K2_LOAD_A(((u) + 3) & 3, (st) + 3) K2_LOAD_B(((u) + 3) & 3, (st) + 3) }                  \
    __builtin_amdgcn_sched_barrier(0);                                                                 \
    const char* ab = abuf + ((u) & 1) * 16384;                                                         \
    _Pragma("unroll") for (int h = 0; h < 2; ++h) {                                                    \
      bf16x8 af[4];                                                                                    \
      _Pragma("unroll") for (int i = 0; i < 4; ++i) af[i] = *(const bf16x8*)(ab + (ard[i] ^ (128 * h))); \
      _Pragma("unroll") for (int j = 0; j < 2; ++j) {                                                  \
        const bf16x8 zf = __builtin_bit_cast(bf16x8, bst[u][2 * h + j]);                               \
        _Pragma("unroll") for (int i = 0; i < 4; ++i)                                                  \
          acc[i][j] = __builtin_amdgcn_mfma_f32_16x16x32_bf16(zf, af[i], acc[i][j], 0, 0, 0);          \
      }                                                                                                \
    }                                                                                                  \
    if (WR) {                                                                                          \
      _Pragma("unroll") for (int i = 0; i < 2; ++i)                                                    \
        *(u32x4*)(abuf + (((u) + 1) & 1) * 16384 + awr + 8192 * i) = ast[((u) + 1) & 3][i];            \
    }                                                                                                  \
    __syncthreads();                                                                                   \
  }
  for (int so = 0; so < 12; so += 4) {
    K2_STAGE(0, so, true, true) K2_STAGE(1, so + 1, true, true) K2_STAGE(2, so + 2, true, true) K2_STAGE(3, so + 3, true, true)
    STAMP(1, 5 + (so >> 2));
  }
  K2_STAGE(0, 12, true, true) K2_STAGE(1, 13, false, true) K2_STAGE(2, 14, false, true) K2_STAGE(3, 15, false, false)
  STAMP(1, 8);

  float bias[2];
#pragma unroll
  for (int i = 0; i < 2; ++i) bias[i] = bf_lo(b_sp[(rb * 4 + 2 * kg + i) * 16 + c16]);
#pragma unroll
  for (int i = 0; i < 2; ++i)
#pragma unroll
    for (int j = 0; j < 2; ++j) *(f32x4*)(abuf + ((w ^ 4) * 4 + 2 * i + j) * 1024 + l * 16) = acc[2 + i][j];
  __syncthreads();
  STAMP(1, 12);
#pragma unroll
  for (int i = 0; i < 2; ++i) {
    u32x2 hv[2];
#pragma unroll
    for (int j = 0; j < 2; ++j) {
      const f32x4 a = acc[i][j] + *(const f32x4*)(abuf + (w * 4 + 2 * i + j) * 1024 + l * 16);
      const float h0 = bf_lo(z1p[i][j][0]) * bfr(bfr(a[0]) + bias[i]);
      const float h1 = bf_hi(z1p[i][j][0]) * bfr(bfr(a[1]) + bias[i]);
      const float h2 = bf_lo(z1p[i][j][1]) * bfr(bfr(a[2]) + bias[i]);
      const float h3 = bf_hi(z1p[i][j][1]) * bfr(bfr(a[3]) + bias[i]);
      hv[j] = u32x2{pack2(h0, h1), pack2(h2, h3)};
    }
    const int tt = rb * 4 + 2 * kg + i;
    u16* hp = hF + ((size_t)(tt * 28 + 7 * (cb >> 1)) * 64 + l) * 8;
    if (two) *(u32x4*)(hp + (size_t)(3 * (cb & 1) + fg) * 512) = u32x4{hv[0][0], hv[0][1], hv[1][0], hv[1][1]};
    else     *(u32x2*)(hp + 6 * 512 + 4 * (cb & 1)) = hv[0];
  }
  STAMP(1, 13); STAMPR(1, 15);
}

__global__ __launch_bounds__(256, 1) void k3_out(const u16* __restrict__ hF, const u16* __restrict__ w_out,
                                                u16* __restrict__ out) {
  __shared__ __attribute__((aligned(16))) char smem[896 * 128];
  const int bid = blockIdx.x, xcd = bid & 7, idx = bid >> 3;
  const int rb = xcd * 4 + idx / 7, nb = idx % 7;
  const int tid = threadIdx.x, l = tid & 63, w = tid >> 6;
  const int g = l >> 4, c16 = l & 15, q = c16 >> 2, p = l & 3;
  const int tt = rb * 4 + w, n0 = nb * 64;
  STAMPR(2, 0); STAMP(2, 1);

  u32x4 wst[28];
  {
    const u16* wp = w_out + (size_t)(tid >> 3) * D_ + n0 + (tid & 7) * 8;
#pragma unroll
    for (int i = 0; i < 28; ++i) wst[i] = *(const u32x4*)(wp + (size_t)i * 32 * D_);
  }
  bf16x8 hf[28];
  {
    const u16* hp = hF + ((size_t)tt * 28 * 64 + l) * 8;
#pragma unroll
    for (int ks = 0; ks < 28; ++ks) hf[ks] = *(const bf16x8*)(hp + ks * 512);
  }
  STAMP(2, 2);
  const int wrow = tid >> 3;
  const int wdst = wrow * 128 + (((tid & 7) ^ (((wrow >> 1) & 3) << 1)) * 16);
  const int rrow = 4 * g + q;
  int rb4[4];
#pragma unroll
  for (int j = 0; j < 4; ++j) rb4[j] = rrow * 128 + ((j ^ ((rrow >> 1) & 3)) * 32) + 8 * p;

  f32x4 acc[4];
#pragma unroll
  for (int j = 0; j < 4; ++j) acc[j] = f32x4{0.f, 0.f, 0.f, 0.f};
#pragma unroll
  for (int pc = 0; pc < 4; ++pc) {
#pragma unroll
    for (int i = 0; i < 7; ++i) *(u32x4*)(smem + (pc * 7 + i) * 4096 + wdst) = wst[pc * 7 + i];
    __syncthreads();
    STAMP(2, 3 + pc);
#pragma unroll
    for (int e = 0; e < 7; ++e) {
      const int tA = (e < 6) ? (14 * pc + 7 * (e / 3) + 2 * (e % 3)) : (14 * pc + 6);
      const int tB = (e < 6) ? (tA + 1) : (14 * pc + 13);
#pragma unroll
      for (int j = 0; j < 4; ++j) {
        const bf16x8 wf = tr_frag(smem + rb4[j] + tA * 2048, smem + rb4[j] + tB * 2048);
        acc[j] = __builtin_amdgcn_mfma_f32_16x16x32_bf16(wf, hf[pc * 7 + e], acc[j], 0, 0, 0);
      }
    }
  }
  STAMP(2, 7);
#pragma unroll
  for (int j = 0; j < 4; ++j) {
    const u32x2 v = {pack2(acc[j][0], acc[j][1]), pack2(acc[j][2], acc[j][3])};
    *(u32x2*)(out + (size_t)(tt * 16 + c16) * D_ + n0 + 16 * j + 4 * g) = v;
  }
  STAMP(2, 8); STAMPR(2, 15);
}

extern "C" void kernel_launch(void* const* d_in, const int* in_sizes, int n_in, void* d_out, int out_size,
                              void* d_ws, size_t ws_size, hipStream_t stream) {
  const u16* x = (const u16*)d_in[0];
  const u16* w_in = (const u16*)d_in[1];
  const u16* w_sp = (const u16*)d_in[2];
  const u16* b_sp = (const u16*)d_in[3];
  const u16* w_out = (const u16*)d_in[4];
  char* ws = (char*)d_ws;
  u16* z2R = (u16*)ws;
  u16* z2N = (u16*)(ws + (size_t)4 * 1024 * 1024);
  u16* hF = (u16*)(ws + (size_t)8 * 1024 * 1024);
  f32x2* stats = (f32x2*)(ws + (size_t)12 * 1024 * 1024);
  unsigned* flags = (unsigned*)(ws + (size_t)13 * 1024 * 1024);
  k1_z2<<<256, 512, 0, stream>>>(x, w_in, z2R, stats, flags, w_sp, w_out);
  k2_spatial<<<256, 512, 0, stream>>>(x, w_in, w_sp, z2R, stats, b_sp, z2N, flags, hF);
  k3_out<<<224, 256, 0, stream>>>(hF, w_out, (u16*)d_out);
}
```

```cpp
#include <hip/hip_runtime.h>
#include <hip/hip_bf16.h>

typedef __bf16 bf16x8 __attribute__((ext_vector_type(8)));
typedef __bf16 bf16x4 __attribute__((ext_vector_type(4)));
typedef __bf16 bf16x2 __attribute__((ext_vector_type(2)));
typedef float f32x4 __attribute__((ext_vector_type(4)));
typedef float f32x2 __attribute__((ext_vector_type(2)));
typedef unsigned u32x4 __attribute__((ext_vector_type(4)));
typedef unsigned u32x2 __attribute__((ext_vector_type(2)));
typedef unsigned short u16;
#define LAS __attribute__((address_space(3)))
#define DI __device__ __forceinline__

#define STAMP(k, i)
#define STAMPR(k, i)
constexpr int S_ = 2048, D_ = 448, F_ = 1792, FH = 896;

DI float bf_lo(unsigned u) { return __builtin_bit_cast(float, u << 16); }
DI float bf_hi(unsigned u) { return __builtin_bit_cast(float, u & 0xffff0000u); }
DI unsigned pack2(float a, float b) {
  f32x2 f = {a, b};
  return __builtin_bit_cast(unsigned, __builtin_convertvector(f, bf16x2));
}
DI float bfr(float a) { return (float)(__bf16)a; }
DI float gelu_tanh(float z) {
  const float y = z * __builtin_fmaf(z * z, -0.10294324f, -2.3022082f);
  const float e = __builtin_amdgcn_exp2f(y);
  return z * __builtin_amdgcn_rcpf(1.0f + e);
}
DI bf16x8 tr_frag(const char* a0, const char* a1) {
  bf16x4 lo = __builtin_amdgcn_ds_read_tr16_b64_v4bf16((LAS bf16x4*)a0);
  bf16x4 hi = __builtin_amdgcn_ds_read_tr16_b64_v4bf16((LAS bf16x4*)a1);
  return __builtin_shufflevector(lo, hi, 0, 1, 2, 3, 4, 5, 6, 7);
}
template <int CTRL> DI float dpp_mov(float x) {
  return __builtin_bit_cast(float, __builtin_amdgcn_update_dpp(0, __builtin_bit_cast(int, x), CTRL, 0xf, 0xf, true));
}
DI float sum16(float x) {
  x += dpp_mov<0xB1>(x);
  x += dpp_mov<0x4E>(x);
  x += dpp_mov<0x141>(x);
  x += dpp_mov<0x140>(x);
  return x;
}

#define BAR()                                              \
  do {                                                     \
    asm volatile("s_waitcnt lgkmcnt(0)" ::: "memory");     \
    __builtin_amdgcn_s_barrier();                          \
    asm volatile("" ::: "memory");                         \
  } while (0)
#define WAITV(n) asm volatile("s_waitcnt vmcnt(" #n ")" ::: "memory")
DI void glds16(const void* src, char* lds_wave_base) {
  __builtin_amdgcn_global_load_lds((const unsigned*)src, (unsigned*)lds_wave_base, 16, 0, 0);
}

constexpr int XW_XB = 7 * 8192, XW_WB = 448 * 224, XW_LDS = XW_XB + XW_WB;

template <bool Z2> DI int x_swz(int row) { return Z2 ? (((row >> 1) & 1) | (((row >> 3) & 3) << 1)) : ((row >> 1) & 7); }

template <bool Z2>
DI void xw_load_stage(const u16* x, const u16* w_in, int rb, int n0, char* smem, int lw, int l, int s) {
  const int nxp = s < 3 ? 4 : 2;
#pragma unroll
  for (int i = 0; i < nxp; ++i) {
    const int pc = 4 * i + lw, blk = 2 * s + (pc >> 3), row = 8 * (pc & 7) + (l >> 3);
    glds16(x + (size_t)(rb * 64 + row) * D_ + blk * 64 + (((l & 7) ^ x_swz<Z2>(row)) * 8), smem + blk * 8192 + (pc & 7) * 1024);
  }
  const int nwp = s < 3 ? 7 : 4;
#pragma unroll
  for (int i = 0; i < nwp; ++i) {
    const int pc = 28 * s + 4 * i + lw;
    if (s < 3 || pc < 98) {
      const int c = pc * 64 + l, row = c / 14, cc = c - row * 14;
      glds16(w_in + (size_t)row * F_ + n0 + cc * 8, smem + XW_XB + pc * 1024);
    }
  }
}
template <bool Z2> struct XwAddr {
  int xa[2], wa[2];
  DI XwAddr(int l, int fg, int mh) {
    const int g = l >> 4, c16 = l & 15, q = c16 >> 2, p = l & 3;
#pragma unroll
    for (int rt = 0; rt < 2; ++rt) {
      const int row = 32 * mh + (Z2 ? (8 * q + 4 * rt + p) : (16 * rt + c16));
      xa[rt] = row * 128 + (((g >> 1) ^ x_swz<Z2>(row)) * 16) + (g & 1) * 8;
    }
#pragma unroll
    for (int jj = 0; jj < 2; ++jj) wa[jj] = XW_XB + (4 * g + q) * 224 + min(2 * fg + jj, 6) * 32 + 8 * p;
  }
};
template <bool Z2>
DI void xw_compute(const XwAddr<Z2>& ad, const char* smem, int ks0, int nks, f32x4 (&acc)[2][2]) {
#pragma unroll
  for (int k = 0; k < nks; ++k) {
    const int ks = ks0 + k;
    bf16x8 xf[2], wf[2];
#pragma unroll
    for (int rt = 0; rt < 2; ++rt) {
      const int o = (ad.xa[rt] ^ (64 * (ks & 1))) + 8192 * (ks >> 1);
      const bf16x4 lo = *(const bf16x4*)(smem + o), hi = *(const bf16x4*)(smem + (o ^ 32));
      xf[rt] = __builtin_shufflevector(lo, hi, 0, 1, 2, 3, 4, 5, 6, 7);
    }
#pragma unroll
    for (int jj = 0; jj < 2; ++jj) {
      const char* a = smem + ad.wa[jj] + 32 * ks * 224;
      wf[jj] = tr_frag(a, a + 16 * 224);
    }
#pragma unroll
    for (int rt = 0; rt < 2; ++rt)
#pragma unroll
      for (int jj = 0; jj < 2; ++jj) {
        if (Z2) acc[rt][jj] = __builtin_amdgcn_mfma_f32_16x16x32_bf16(xf[rt], wf[jj], acc[rt][jj], 0, 0, 0);
        else    acc[rt][jj] = __builtin_amdgcn_mfma_f32_16x16x32_bf16(wf[jj], xf[rt], acc[rt][jj], 0, 0, 0);
      }
  }
}
DI void gelu_tile(f32x4 (&acc)[2][2]) {
#pragma unroll
  for (int rt = 0; rt < 2; ++rt)
#pragma unroll
    for (int jj = 0; jj < 2; ++jj)
#pragma unroll
      for (int r = 0; r < 4; ++r) acc[rt][jj][r] = bfr(gelu_tanh(bfr(acc[rt][jj][r])));
}

DI void tile_of_block(int bid, int& rb, int& cb) {
  const int xcd = bid & 7, idx = bid >> 3;
  rb = (xcd >> 1) * 8 + (idx & 7);
  cb = (xcd & 1) * 4 + (idx >> 3);
}

__global__ __launch_bounds__(768, 3) void k1_z2(const u16* __restrict__ x, const u16* __restrict__ w_in,
                                               u16* __restrict__ z2R, f32x2* __restrict__ stats,
                                               unsigned* __restrict__ flags, const u16* __restrict__ w_sp,
                                               const u16* __restrict__ w_out) {
  __shared__ __attribute__((aligned(16))) char smem[XW_LDS + 4 * 64 * 8];
  const int bid = blockIdx.x;
  int rb, cb;
  tile_of_block(bid, rb, cb);
  const int tid = threadIdx.x, l = tid & 63, w = __builtin_amdgcn_readfirstlane(tid >> 6);
  const int n0 = FH + cb * 112;
  if (w >= 8) {
    const int lw = w - 8;
    xw_load_stage<true>(x, w_in, rb, n0, smem, lw, l, 0);
    xw_load_stage<true>(x, w_in, rb, n0, smem, lw, l, 1);
    xw_load_stage<true>(x, w_in, rb, n0, smem, lw, l, 2);
    WAITV(22); BAR();
    xw_load_stage<true>(x, w_in, rb, n0, smem, lw, l, 3);
    WAITV(16); BAR();
    WAITV(5); BAR();
    WAITV(0); BAR();
    u32x4 pf[9];
#pragma unroll
    for (int i = 0; i < 8; ++i) pf[i] = *(const u32x4*)(w_sp + ((size_t)bid * 2048 + i * 256 + lw * 64 + l) * 8);
    pf[8] = *(const u32x4*)(w_out + ((size_t)bid * 196 + min(lw * 64 + l, 195)) * 8);
#pragma unroll
    for (int i = 0; i < 9; ++i) asm volatile("" ::"v"(pf[i]));
    return;
  }
  const int fg = w & 3, mh = w >> 2, g = l >> 4, c16 = l & 15;
  const bool two = fg < 3;
  STAMPR(0, 0); STAMP(0, 1);
  if (tid == 0) flags[bid] = 0u;
  const XwAddr<true> ad(l, fg, mh);
  f32x4 acc[2][2];
#pragma unroll
  for (int rt = 0; rt < 2; ++rt)
#pragma unroll
    for (int jj = 0; jj < 2; ++jj) acc[rt][jj] = f32x4{0.f, 0.f, 0.f, 0.f};
  BAR(); STAMP(0, 2); xw_compute<true>(ad, smem, 0, 4, acc);
  BAR(); STAMP(0, 3); xw_compute<true>(ad, smem, 4, 4, acc);
  BAR(); STAMP(0, 4); xw_compute<true>(ad, smem, 8, 4, acc);
  BAR(); STAMP(0, 5); xw_compute<true>(ad, smem, 12, 2, acc);
  STAMP(0, 6);
  gelu_tile(acc);
  STAMP(0, 7);

  const int kstep = rb * 2 + mh;
#pragma unroll
  for (int jj = 0; jj < 2; ++jj)
    if (jj == 0 || two) {
      const u32x4 v = {pack2(acc[0][jj][0], acc[0][jj][1]), pack2(acc[0][jj][2], acc[0][jj][3]),
                       pack2(acc[1][jj][0], acc[1][jj][1]), pack2(acc[1][jj][2], acc[1][jj][3])};
      *(u32x4*)(z2R + ((size_t)((cb * 7 + 2 * fg + jj) * 64 + kstep) * 64 + l) * 8) = v;
    }
  f32x2* sst = (f32x2*)(smem + XW_LDS);
  const float inv_n = two ? (1.0f / 32.0f) : (1.0f / 16.0f);
#pragma unroll
  for (int rt = 0; rt < 2; ++rt)
#pragma unroll
    for (int r = 0; r < 4; ++r) {
      const float a0 = acc[rt][0][r], a1 = two ? acc[rt][1][r] : 0.f;
      const float sum = sum16(a0 + a1);
      const float mean = sum * inv_n;
      const float d0 = a0 - mean, d1 = two ? a1 - mean : 0.f;
      const float m2 = sum16(d0 * d0 + d1 * d1);
      if (c16 == 0) sst[fg * 64 + 32 * mh + 8 * g + 4 * rt + r] = f32x2{sum, m2};
    }
  BAR();
  if (tid < 64) {
    const f32x2 a = sst[tid], b = sst[64 + tid], c = sst[128 + tid], d = sst[192 + tid];
    const float sum = a[0] + b[0] + c[0] + d[0], mean = sum * (1.0f / 112.0f);
    const float da = a[0] * (1.0f / 32.0f) - mean, db = b[0] * (1.0f / 32.0f) - mean;
    const float dc = c[0] * (1.0f / 32.0f) - mean, dd = d[0] * (1.0f / 16.0f) - mean;
    const float m2 = a[1] + b[1] + c[1] + d[1] + 32.0f * (da * da + db * db + dc * dc) + 16.0f * dd * dd;
    stats[(size_t)(rb * 64 + tid) * 8 + cb] = f32x2{sum, m2};
  }
  STAMP(0, 8); STAMPR(0, 15);
}

typedef __attribute__((address_space(1))) unsigned gu32;
#define RLX_AGENT __ATOMIC_RELAXED, __HIP_MEMORY_SCOPE_AGENT
DI int k2_slot(int i) { return (i & 1) ? (XW_XB + (i >> 1) * 28672) : ((i >> 1) * 16384); }

__global__ __launch_bounds__(768, 3) void k2_spatial(const u16* __restrict__ x, const u16* __restrict__ w_in,
                                                    const u16* __restrict__ w_sp, const u16* __restrict__ z2R,
                                                    const f32x2* __restrict__ stats, const u16* __restrict__ b_sp,
                                                    u16* z2N, unsigned* flags, u16* __restrict__ hF) {
  __shared__ __attribute__((aligned(16))) char smem[XW_LDS + 8 * 512];
  const int bid = blockIdx.x;
  int rb, cb;
  tile_of_block(bid, rb, cb);
  const int tid = threadIdx.x, l = tid & 63, w = __builtin_amdgcn_readfirstlane(tid >> 6);
  const int t0 = rb * 64, n0 = cb * 112;
  const int koff = (2 * (rb & 7) + (cb & 1)) & 15;

  if (w >= 8) {
    const int lw = w - 8;
    const u16* ap[4];
#pragma unroll
    for (int i = 0; i < 4; ++i) {
      const int row = 4 * (4 * i + lw) + (l >> 4);
      ap[i] = w_sp + (size_t)(t0 + row) * S_ + (((l & 15) ^ (row & 15)) * 8);
    }
#define K2_LOAD_W(st)                                                                          \
  {                                                                                            \
    const int sk_ = ((st) + koff) & 15;                                                        \
    char* dst_ = smem + k2_slot((st) & 3) + lw * 1024;                                         \
    _Pragma("unroll") for (int i = 0; i < 4; ++i) glds16(ap[i] + 128 * sk_, dst_ + i * 4096);  \
  }
    xw_load_stage<false>(x, w_in, rb, n0, smem, lw, l, 0);
    xw_load_stage<false>(x, w_in, rb, n0, smem, lw, l, 1);
    xw_load_stage<false>(x, w_in, rb, n0, smem, lw, l, 2);
    WAITV(22); BAR();
    xw_load_stage<false>(x, w_in, rb, n0, smem, lw, l, 3);
    WAITV(16); BAR();
    K2_LOAD_W(0) K2_LOAD_W(1)
    WAITV(13); BAR();
    K2_LOAD_W(2) K2_LOAD_W(3)
    WAITV(16); BAR();
    WAITV(12); BAR();
    WAITV(8); BAR();
    for (int st = 1; st < 13; ++st) {
      K2_LOAD_W(st + 3)
      WAITV(8); BAR();
    }
    WAITV(4); BAR();
    WAITV(0); BAR();
    return;
  }

  const int fg = w & 3, kg = w >> 2, g = l >> 4, c16 = l & 15;
  const bool two = fg < 3;
  STAMPR(1, 0); STAMP(1, 1);
  const __amdgpu_buffer_rsrc_t rsN = __builtin_amdgcn_make_buffer_rsrc((void*)z2N, 0, S_ * FH * 2, 0x00020000);
  const __amdgpu_buffer_rsrc_t rsN1 = __builtin_amdgcn_make_buffer_rsrc((void*)z2N, 0, two ? S_ * FH * 2 : 0, 0x00020000);

  if (w < 7) {
    const unsigned zoff = (unsigned)(((cb * 7 + w) * 64 + 2 * rb) * 64 + l) * 16u;
    f32x4 sp[4];
#pragma unroll
    for (int i = 0; i < 4; ++i) sp[i] = *(const f32x4*)(stats + (size_t)(t0 + l) * 8 + 2 * i);
    const u32x4 zr0 = *(const u32x4*)((const char*)z2R + zoff), zr1 = *(const u32x4*)((const char*)z2R + zoff + 1024);
    f32x2* mrs = (f32x2*)(smem + XW_LDS + w * 512);
    float sum = 0.f, m2 = 0.f;
#pragma unroll
    for (int i = 0; i < 4; ++i) { sum += sp[i][0] + sp[i][2]; m2 += sp[i][1] + sp[i][3]; }
    const float mean = sum * (1.0f / 896.0f);
#pragma unroll
    for (int i = 0; i < 4; ++i) {
      const float d0 = sp[i][0] * (1.0f / 112.0f) - mean, d1 = sp[i][2] * (1.0f / 112.0f) - mean;
      m2 += 112.0f * (d0 * d0 + d1 * d1);
    }
    const float var = bfr(m2 * (1.0f / 896.0f));
    const float den = bfr(sqrtf(bfr(var + 1e-5f)));
    mrs[l] = f32x2{bfr(mean), 1.0f / den};
#pragma unroll
    for (int hh = 0; hh < 2; ++hh) {
      const u32x4 u = hh ? zr1 : zr0;
      u32x4 v;
#pragma unroll
      for (int e = 0; e < 4; ++e) {
        const f32x4 mr = *(const f32x4*)(mrs + 32 * hh + 8 * g + 2 * e);
        v[e] = pack2(bfr(bf_lo(u[e]) - mr[0]) * mr[1], bfr(bf_hi(u[e]) - mr[2]) * mr[3]);
      }
      __builtin_amdgcn_raw_buffer_store_b128(v, rsN, zoff + 1024u * hh, 0, 16);
    }
    WAITV(0);
    if (l == 0) __hip_atomic_fetch_add((gu32*)flags + cb * 32 + rb, 1u, RLX_AGENT);
  }
  STAMP(1, 2);

  const unsigned boff = (unsigned)(((cb * 7 + 2 * fg) * 64 + kg) * 64 + l) * 16u;
  u32x4 bst[4][4];
#define K2_LOAD_B(slot, st)                                                                       \
  {                                                                                               \
    const int sk_ = ((st) + koff) & 15;                                                           \
    bst[slot][0] = __builtin_amdgcn_raw_buffer_load_b128(rsN, boff, 4096 * sk_, 16);              \
    bst[slot][1] = __builtin_amdgcn_raw_buffer_load_b128(rsN1, boff + 65536, 4096 * sk_, 16);     \
    bst[slot][2] = __builtin_amdgcn_raw_buffer_load_b128(rsN, boff + 2048, 4096 * sk_, 16);       \
    bst[slot][3] = __builtin_amdgcn_raw_buffer_load_b128(rsN1, boff + 65536 + 2048, 4096 * sk_, 16); \
  }
  u32x2 z1p[2][2];
  {
    const XwAddr<false> ad(l, fg, kg);
    f32x4 za[2][2];
#pragma unroll
    for (int i = 0; i < 2; ++i)
#pragma unroll
      for (int j = 0; j < 2; ++j) za[i][j] = f32x4{0.f, 0.f, 0.f, 0.f};
    BAR(); xw_compute<false>(ad, smem, 0, 4, za);
    BAR(); xw_compute<false>(ad, smem, 4, 4, za);
    BAR(); xw_compute<false>(ad, smem, 8, 4, za);
    if (w == 0) {
      const gu32* fp = (const gu32*)flags + cb * 32 + (l & 31);
      for (unsigned spins = 0; spins < (1u << 22); ++spins) {
        if (__all(__hip_atomic_load(fp, RLX_AGENT) == 7u)) break;
        __builtin_amdgcn_s_sleep(1);
      }
    }
    BAR();
    STAMP(1, 3);
    K2_LOAD_B(0, 0) K2_LOAD_B(1, 1) K2_LOAD_B(2, 2)
    xw_compute<false>(ad, smem, 12, 2, za);
    gelu_tile(za);
#pragma unroll
    for (int i = 0; i < 2; ++i)
#pragma unroll
      for (int j = 0; j < 2; ++j) z1p[i][j] = u32x2{pack2(za[i][j][0], za[i][j][1]), pack2(za[i][j][2], za[i][j][3])};
  }
  STAMP(1, 4);

  int ard[4];
#pragma unroll
  for (int i = 0; i < 4; ++i) ard[i] = (16 * ((i + 2 * kg) & 3) + c16) * 256 + (((4 * kg + g) ^ c16) * 16);
  f32x4 acc[4][2];
#pragma unroll
  for (int i = 0; i < 4; ++i)
#pragma unroll
    for (int j = 0; j < 2; ++j) acc[i][j] = f32x4{0.f, 0.f, 0.f, 0.f};

#define K2_STAGE(u, st, PF)                                                                            \
  {                                                                                                    \
    BAR();                                                                                             \
    if (PF) { K2_LOAD_B(((u) + 3) & 3, (st) + 3) }                                                     \
    __builtin_amdgcn_sched_barrier(0);                                                                 \
    const char* ab = smem + k2_slot(u);                                                                \
    _Pragma("unroll") for (int h = 0; h < 2; ++h) {                                                    \
      bf16x8 af[4];                                                                                    \
      _Pragma("unroll") for (int i = 0; i < 4; ++i) af[i] = *(const bf16x8*)(ab + (ard[i] ^ (128 * h))); \
      _Pragma("unroll") for (int j = 0; j < 2; ++j) {                                                  \
        const bf16x8 zf = __builtin_bit_cast(bf16x8, bst[u][2 * h + j]);                               \
        _Pragma("unroll") for (int i = 0; i < 4; ++i)                                                  \
          acc[i][j] = __builtin_amdgcn_mfma_f32_16x16x32_bf16(zf, af[i], acc[i][j], 0, 0, 0);          \
      }                                                                                                \
    }                                                                                                  \
  }
  for (int so = 0; so < 12; so += 4) {
    K2_STAGE(0, so, true) K2_STAGE(1, so + 1, true) K2_STAGE(2, so + 2, true) K2_STAGE(3, so + 3, true)
    STAMP(1, 5 + (so >> 2));
  }
  K2_STAGE(0, 12, true) K2_STAGE(1, 13, false) K2_STAGE(2, 14, false) K2_STAGE(3, 15, false)
  STAMP(1, 8);

  char* red = smem + 32768;
  float bias[2];
#pragma unroll
  for (int i = 0; i < 2; ++i) bias[i] = bf_lo(b_sp[(rb * 4 + 2 * kg + i) * 16 + c16]);
  char* redw = red + ((w ^ 4) < 6 ? (w ^ 4) * 4096 : XW_XB + 57344 - 32768 + ((w ^ 4) - 6) * 4096);
  const char* redr = red + (w < 6 ? w * 4096 : XW_XB + 57344 - 32768 + (w - 6) * 4096);
#pragma unroll
  for (int i = 0; i < 2; ++i)
#pragma unroll
    for (int j = 0; j < 2; ++j) *(f32x4*)(redw + (2 * i + j) * 1024 + l * 16) = acc[2 + i][j];
  BAR();
  STAMP(1, 12);
#pragma unroll
  for (int i = 0; i < 2; ++i) {
    u32x2 hv[2];
#pragma unroll
    for (int j = 0; j < 2; ++j) {
      const f32x4 a = acc[i][j] + *(const f32x4*)(redr + (2 * i + j) * 1024 + l * 16);
      const float h0 = bf_lo(z1p[i][j][0]) * bfr(bfr(a[0]) + bias[i]);
      const float h1 = bf_hi(z1p[i][j][0]) * bfr(bfr(a[1]) + bias[i]);
      const float h2 = bf_lo(z1p[i][j][1]) * bfr(bfr(a[2]) + bias[i]);
      const float h3 = bf_hi(z1p[i][j][1]) * bfr(bfr(a[3]) + bias[i]);
      hv[j] = u32x2{pack2(h0, h1), pack2(h2, h3)};
    }
    const int tt = rb * 4 + 2 * kg + i;
    u16* hp = hF + ((size_t)(tt * 28 + 7 * (cb >> 1)) * 64 + l) * 8;
    if (two) *(u32x4*)(hp + (size_t)(3 * (cb & 1) + fg) * 512) = u32x4{hv[0][0], hv[0][1], hv[1][0], hv[1][1]};
    else     *(u32x2*)(hp + 6 * 512 + 4 * (cb & 1)) = hv[0];
  }
  STAMP(1, 13); STAMPR(1, 15);
}

__global__ __launch_bounds__(768, 3) void k3_out(const u16* __restrict__ hF, const u16* __restrict__ w_out,
                                                u16* __restrict__ out) {
  __shared__ __attribute__((aligned(16))) char smem[896 * 128 + 8 * 2048];
  const int bid = blockIdx.x, xcd = bid & 7, idx = bid >> 3;
  const int rb = xcd * 4 + idx / 7, nb = idx % 7;
  const int tid = threadIdx.x, l = tid & 63, w = __builtin_amdgcn_readfirstlane(tid >> 6);
  const int n0 = nb * 64;
  if (w >= 8) {
    const int lw = w - 8, r8 = l >> 3;
    const u16* wp = w_out + (size_t)(8 * lw + r8) * D_ + n0 + (((l & 7) ^ (((r8 >> 1) & 3) << 1)) * 8);
#pragma unroll
    for (int i = 0; i < 28; ++i) glds16(wp + (size_t)i * 32 * D_, smem + (4 * i + lw) * 1024);
    WAITV(21); BAR();
    WAITV(14); BAR();
    WAITV(7); BAR();
    WAITV(0); BAR();
    return;
  }
  const int tq = w & 3, kg = w >> 2, g = l >> 4, c16 = l & 15, q = c16 >> 2, p = l & 3;
  const int tt = rb * 4 + tq;
  STAMPR(2, 0); STAMP(2, 1);
  bf16x8 hf[4][4];
  {
    const u16* hp = hF + ((size_t)tt * 28 * 64 + l) * 8;
#pragma unroll
    for (int pc = 0; pc < 4; ++pc)
#pragma unroll
      for (int i = 0; i < 4; ++i) hf[pc][i] = *(const bf16x8*)(hp + (7 * pc + min(2 * i + ((pc + kg) & 1), 6)) * 512);
  }
  const int rrow = 4 * g + q;
  int rb4[4];
#pragma unroll
  for (int j = 0; j < 4; ++j) rb4[j] = rrow * 128 + ((j ^ ((rrow >> 1) & 3)) * 32) + 8 * p;
  f32x4 acc[4];
#pragma unroll
  for (int j = 0; j < 4; ++j) acc[j] = f32x4{0.f, 0.f, 0.f, 0.f};
  STAMP(2, 2);
#pragma unroll
  for (int pc = 0; pc < 4; ++pc) {
    BAR();
    STAMP(2, 3 + pc);
    const int par = (pc + kg) & 1;
#pragma unroll
    for (int i = 0; i < 4; ++i) {
      if (i < 3 || par == 0) {
        const int e = 2 * i + par;
        const int tA = (e < 6) ? (14 * pc + 7 * (e / 3) + 2 * (e % 3)) : (14 * pc + 6);
        const int tB = (e < 6) ? (tA + 1) : (14 * pc + 13);
#pragma unroll
        for (int j = 0; j < 4; ++j) {
          const bf16x8 wf = tr_frag(smem + rb4[j] + tA * 2048, smem + rb4[j] + tB * 2048);
          acc[j] = __builtin_amdgcn_mfma_f32_16x16x32_bf16(wf, hf[pc][i], acc[j], 0, 0, 0);
        }
      }
    }
  }
  STAMP(2, 7);
  char* red = smem + 896 * 128;
  if (kg == 0) {
    *(f32x4*)(red + (w ^ 4) * 2048 + l * 16) = acc[2];
    *(f32x4*)(red + (w ^ 4) * 2048 + 1024 + l * 16) = acc[3];
  } else {
    *(f32x4*)(red + (w ^ 4) * 2048 + l * 16) = acc[0];
    *(f32x4*)(red + (w ^ 4) * 2048 + 1024 + l * 16) = acc[1];
  }
  BAR();
  const f32x4 a0 = (kg ? acc[2] : acc[0]) + *(const f32x4*)(red + w * 2048 + l * 16);
  const f32x4 a1 = (kg ? acc[3] : acc[1]) + *(const f32x4*)(red + w * 2048 + 1024 + l * 16);
  u16* op = out + (size_t)(tt * 16 + c16) * D_ + n0 + 32 * kg + 4 * g;
  *(u32x2*)op = u32x2{pack2(a0[0], a0[1]), pack2(a0[2], a0[3])};
  *(u32x2*)(op + 16) = u32x2{pack2(a1[0], a1[1]), pack2(a1[2], a1[3])};
  STAMP(2, 8); STAMPR(2, 15);
}

extern "C" void kernel_launch(void* const* d_in, const int* in_sizes, int n_in, void* d_out, int out_size,
                              void* d_ws, size_t ws_size, hipStream_t stream) {
  const u16* x = (const u16*)d_in[0];
  const u16* w_in = (const u16*)d_in[1];
  const u16* w_sp = (const u16*)d_in[2];
  const u16* b_sp = (const u16*)d_in[3];
  const u16* w_out = (const u16*)d_in[4];
  char* ws = (char*)d_ws;
  u16* z2R = (u16*)ws;
  u16* z2N = (u16*)(ws + (size_t)4 * 1024 * 1024);
  u16* hF = (u16*)(ws + (size_t)8 * 1024 * 1024);
  f32x2* stats = (f32x2*)(ws + (size_t)12 * 1024 * 1024);
  unsigned* flags = (unsigned*)(ws + (size_t)13 * 1024 * 1024);
  k1_z2<<<256, 768, 0, stream>>>(x, w_in, z2R, stats, flags, w_sp, w_out);
  k2_spatial<<<256, 768, 0, stream>>>(x, w_in, w_sp, z2R, stats, b_sp, z2N, flags, hF);
  k3_out<<<224, 768, 0, stream>>>(hF, w_out, (u16*)d_out);
}
```

```cpp
#include <hip/hip_runtime.h>
#include <hip/hip_bf16.h>

typedef __bf16 bf16x8 __attribute__((ext_vector_type(8)));
typedef __bf16 bf16x4 __attribute__((ext_vector_type(4)));
typedef __bf16 bf16x2 __attribute__((ext_vector_type(2)));
typedef float f32x4 __attribute__((ext_vector_type(4)));
typedef float f32x2 __attribute__((ext_vector_type(2)));
typedef unsigned u32x4 __attribute__((ext_vector_type(4)));
typedef unsigned u32x2 __attribute__((ext_vector_type(2)));
typedef unsigned short u16;
#define LAS __attribute__((address_space(3)))
#define DI __device__ __forceinline__

#define STAMP(k, i)
#define STAMPR(k, i)
constexpr int S_ = 2048, D_ = 448, F_ = 1792, FH = 896;

DI float bf_lo(unsigned u) { return __builtin_bit_cast(float, u << 16); }
DI float bf_hi(unsigned u) { return __builtin_bit_cast(float, u & 0xffff0000u); }
DI unsigned pack2(float a, float b) {
  f32x2 f = {a, b};
  return __builtin_bit_cast(unsigned, __builtin_convertvector(f, bf16x2));
}
DI float bfr(float a) { return (float)(__bf16)a; }
DI float gelu_tanh(float z) {
  const float y = z * __builtin_fmaf(z * z, -0.10294324f, -2.3022082f);
  const float e = __builtin_amdgcn_exp2f(y);
  return z * __builtin_amdgcn_rcpf(1.0f + e);
}
DI bf16x8 tr_frag(const char* a0, const char* a1) {
  bf16x4 lo = __builtin_amdgcn_ds_read_tr16_b64_v4bf16((LAS bf16x4*)a0);
  bf16x4 hi = __builtin_amdgcn_ds_read_tr16_b64_v4bf16((LAS bf16x4*)a1);
  return __builtin_shufflevector(lo, hi, 0, 1, 2, 3, 4, 5, 6, 7);
}
template <int CTRL> DI float dpp_mov(float x) {
  return __builtin_bit_cast(float, __builtin_amdgcn_update_dpp(0, __builtin_bit_cast(int, x), CTRL, 0xf, 0xf, true));
}
DI float sum16(float x) {
  x += dpp_mov<0xB1>(x);
  x += dpp_mov<0x4E>(x);
  x += dpp_mov<0x141>(x);
  x += dpp_mov<0x140>(x);
  return x;
}

#define BAR()                                              \
  do {                                                     \
    asm volatile("s_waitcnt lgkmcnt(0)" ::: "memory");     \
    __builtin_amdgcn_s_barrier();                          \
    asm volatile("" ::: "memory");                         \
  } while (0)
#define WAITV(n) asm volatile("s_waitcnt vmcnt(" #n ")" ::: "memory")
DI void glds16(const void* src, char* lds_wave_base) {
  __builtin_amdgcn_global_load_lds((const unsigned*)src, (unsigned*)lds_wave_base, 16, 0, 0);
}

constexpr int XW_XB = 7 * 8192, XW_WB = 448 * 224, XW_LDS = XW_XB + XW_WB;

template <bool Z2> DI int x_swz(int row) { return Z2 ? (((row >> 1) & 1) | (((row >> 3) & 3) << 1)) : ((row >> 1) & 7); }

template <bool Z2>
DI void xw_load_stage(const u16* x, const u16* w_in, int rb, int n0, char* smem, int lw, int l, int s) {
  const int nxp = s < 3 ? 4 : 2;
#pragma unroll
  for (int i = 0; i < nxp; ++i) {
    const int pc = 4 * i + lw, blk = 2 * s + (pc >> 3), row = 8 * (pc & 7) + (l >> 3);
    glds16(x + (size_t)(rb * 64 + row) * D_ + blk * 64 + (((l & 7) ^ x_swz<Z2>(row)) * 8), smem + blk * 8192 + (pc & 7) * 1024);
  }
  const int nwp = s < 3 ? 7 : 4;
#pragma unroll
  for (int i = 0; i < nwp; ++i) {
    const int pc = 28 * s + 4 * i + lw;
    if (s < 3 || pc < 98) {
      const int c = pc * 64 + l, row = c / 14, cc = c - row * 14;
      glds16(w_in + (size_t)row * F_ + n0 + cc * 8, smem + XW_XB + pc * 1024);
    }
  }
}
template <bool Z2> struct XwAddr {
  int xa[2], wa[2];
  DI XwAddr(int l, int fg, int mh) {
    const int g = l >> 4, c16 = l & 15, q = c16 >> 2, p = l & 3;
#pragma unroll
    for (int rt = 0; rt < 2; ++rt) {
      const int row = 32 * mh + (Z2 ? (8 * q + 4 * rt + p) : (16 * rt + c16));
      xa[rt] = row * 128 + (((g >> 1) ^ x_swz<Z2>(row)) * 16) + (g & 1) * 8;
    }
#pragma unroll
    for (int jj = 0; jj < 2; ++jj) wa[jj] = XW_XB + (4 * g + q) * 224 + min(2 * fg + jj, 6) * 32 + 8 * p;
  }
};
template <bool Z2, int NKS>
DI void xw_compute(const XwAddr<Z2>& ad, const char* smem, int ks0, f32x4 (&acc)[2][2]) {
  bf16x8 xf[NKS][2], wf[NKS][2];
#pragma unroll
  for (int k = 0; k < NKS; ++k) {
    const int ks = ks0 + k;
#pragma unroll
    for (int rt = 0; rt < 2; ++rt) {
      const int o = (ad.xa[rt] ^ (64 * (ks & 1))) + 8192 * (ks >> 1);
      const bf16x4 lo = *(const bf16x4*)(smem + o), hi = *(const bf16x4*)(smem + (o ^ 32));
      xf[k][rt] = __builtin_shufflevector(lo, hi, 0, 1, 2, 3, 4, 5, 6, 7);
    }
#pragma unroll
    for (int jj = 0; jj < 2; ++jj) {
      const char* a = smem + ad.wa[jj] + 32 * ks * 224;
      wf[k][jj] = tr_frag(a, a + 16 * 224);
    }
  }
  __builtin_amdgcn_sched_barrier(0);
#pragma unroll
  for (int k = 0; k < NKS; ++k)
#pragma unroll
    for (int rt = 0; rt < 2; ++rt)
#pragma unroll
      for (int jj = 0; jj < 2; ++jj) {
        if (Z2) acc[rt][jj] = __builtin_amdgcn_mfma_f32_16x16x32_bf16(xf[k][rt], wf[k][jj], acc[rt][jj], 0, 0, 0);
        else    acc[rt][jj] = __builtin_amdgcn_mfma_f32_16x16x32_bf16(wf[k][jj], xf[k][rt], acc[rt][jj], 0, 0, 0);
      }
}
DI void gelu_tile(f32x4 (&acc)[2][2]) {
#pragma unroll
  for (int rt = 0; rt < 2; ++rt)
#pragma unroll
    for (int jj = 0; jj < 2; ++jj)
#pragma unroll
      for (int r = 0; r < 4; ++r) acc[rt][jj][r] = bfr(gelu_tanh(bfr(acc[rt][jj][r])));
}

DI void tile_of_block(int bid, int& rb, int& cb) {
  const int xcd = bid & 7, idx = bid >> 3;
  rb = (xcd >> 1) * 8 + (idx & 7);
  cb = (xcd & 1) * 4 + (idx >> 3);
}

__global__ __launch_bounds__(768, 3) void k1_z2(const u16* __restrict__ x, const u16* __restrict__ w_in,
                                               u16* __restrict__ z2R, f32x2* __restrict__ stats,
                                               unsigned* __restrict__ flags, const u16* __restrict__ w_sp,
                                               const u16* __restrict__ w_out) {
  __shared__ __attribute__((aligned(16))) char smem[XW_LDS + 4 * 64 * 8];
  const int bid = blockIdx.x;
  int rb, cb;
  tile_of_block(bid, rb, cb);
  const int tid = threadIdx.x, l = tid & 63, w = __builtin_amdgcn_readfirstlane(tid >> 6);
  const int n0 = FH + cb * 112;
  if (w >= 8) {
    const int lw = w - 8;
    xw_load_stage<true>(x, w_in, rb, n0, smem, lw, l, 0);
    xw_load_stage<true>(x, w_in, rb, n0, smem, lw, l, 1);
    xw_load_stage<true>(x, w_in, rb, n0, smem, lw, l, 2);
    WAITV(22); BAR();
    xw_load_stage<true>(x, w_in, rb, n0, smem, lw, l, 3);
    u32x4 pf[10];
    {
      const int c = bid * 196 + min(lw * 64 + l, 195);
      pf[0] = *(const u32x4*)(w_in + (size_t)(c / 112) * F_ + (c % 112) * 8);
      pf[1] = *(const u32x4*)(w_out + (size_t)c * 8);
    }
#pragma unroll
    for (int i = 0; i < 8; ++i) pf[2 + i] = *(const u32x4*)(w_sp + ((size_t)bid * 2048 + i * 256 + lw * 64 + l) * 8);
    WAITV(26); BAR();
    WAITV(15); BAR();
    WAITV(10); BAR();
#pragma unroll
    for (int i = 0; i < 10; ++i) asm volatile("" ::"v"(pf[i]));
    return;
  }
  const int fg = w & 3, mh = w >> 2, g = l >> 4, c16 = l & 15;
  const bool two = fg < 3;
  STAMPR(0, 0); STAMP(0, 1);
  if (tid == 0) flags[bid] = 0u;
  const XwAddr<true> ad(l, fg, mh);
  f32x4 acc[2][2];
#pragma unroll
  for (int rt = 0; rt < 2; ++rt)
#pragma unroll
    for (int jj = 0; jj < 2; ++jj) acc[rt][jj] = f32x4{0.f, 0.f, 0.f, 0.f};
  BAR(); STAMP(0, 2); xw_compute<true, 4>(ad, smem, 0, acc);
  BAR(); STAMP(0, 3); xw_compute<true, 4>(ad, smem, 4, acc);
  BAR(); STAMP(0, 4); xw_compute<true, 4>(ad, smem, 8, acc);
  BAR(); STAMP(0, 5); xw_compute<true, 2>(ad, smem, 12, acc);
  STAMP(0, 6);
  gelu_tile(acc);
  STAMP(0, 7);

  const int kstep = rb * 2 + mh;
#pragma unroll
  for (int jj = 0; jj < 2; ++jj)
    if (jj == 0 || two) {
      const u32x4 v = {pack2(acc[0][jj][0], acc[0][jj][1]), pack2(acc[0][jj][2], acc[0][jj][3]),
                       pack2(acc[1][jj][0], acc[1][jj][1]), pack2(acc[1][jj][2], acc[1][jj][3])};
      *(u32x4*)(z2R + ((size_t)((cb * 7 + 2 * fg + jj) * 64 + kstep) * 64 + l) * 8) = v;
    }
  f32x2* sst = (f32x2*)(smem + XW_LDS);
  const float inv_n = two ? (1.0f / 32.0f) : (1.0f / 16.0f);
#pragma unroll
  for (int rt = 0; rt < 2; ++rt)
#pragma unroll
    for (int r = 0; r < 4; ++r) {
      const float a0 = acc[rt][0][r], a1 = two ? acc[rt][1][r] : 0.f;
      const float sum = sum16(a0 + a1);
      const float mean = sum * inv_n;
      const float d0 = a0 - mean, d1 = two ? a1 - mean : 0.f;
      const float m2 = sum16(d0 * d0 + d1 * d1);
      if (c16 == 0) sst[fg * 64 + 32 * mh + 8 * g + 4 * rt + r] = f32x2{sum, m2};
    }
  BAR();
  if (tid < 64) {
    const f32x2 a = sst[tid], b = sst[64 + tid], c = sst[128 + tid], d = sst[192 + tid];
    const float sum = a[0] + b[0] + c[0] + d[0], mean = sum * (1.0f / 112.0f);
    const float da = a[0] * (1.0f / 32.0f) - mean, db = b[0] * (1.0f / 32.0f) - mean;
    const float dc = c[0] * (1.0f / 32.0f) - mean, dd = d[0] * (1.0f / 16.0f) - mean;
    const float m2 = a[1] + b[1] + c[1] + d[1] + 32.0f * (da * da + db * db + dc * dc) + 16.0f * dd * dd;
    stats[(size_t)(rb * 64 + tid) * 8 + cb] = f32x2{sum, m2};
  }
  STAMP(0, 8); STAMPR(0, 15);
}

typedef __attribute__((address_space(1))) unsigned gu32;
#define RLX_AGENT __ATOMIC_RELAXED, __HIP_MEMORY_SCOPE_AGENT
DI int k2_slot(int i) { return (i & 1) ? (XW_XB + (i >> 1) * 28672) : ((i >> 1) * 16384); }

__global__ __launch_bounds__(768, 3) void k2_spatial(const u16* __restrict__ x, const u16* __restrict__ w_in,
                                                    const u16* __restrict__ w_sp, const u16* __restrict__ z2R,
                                                    const f32x2* __restrict__ stats, const u16* __restrict__ b_sp,
                                                    u16* z2N, unsigned* flags, u16* __restrict__ hF) {
  __shared__ __attribute__((aligned(16))) char smem[XW_LDS + 8 * 512];
  const int bid = blockIdx.x;
  int rb, cb;
  tile_of_block(bid, rb, cb);
  const int tid = threadIdx.x, l = tid & 63, w = __builtin_amdgcn_readfirstlane(tid >> 6);
  const int t0 = rb * 64, n0 = cb * 112;
  const int koff = (2 * (rb & 7) + (cb & 1)) & 15;

  if (w >= 8) {
    const int lw = w - 8;
    const u16* ap[4];
#pragma unroll
    for (int i = 0; i < 4; ++i) {
      const int row = 4 * (4 * i + lw) + (l >> 4);
      ap[i] = w_sp + (size_t)(t0 + row) * S_ + (((l & 15) ^ (row & 15)) * 8);
    }
#define K2_LOAD_W(st)                                                                          \
  {                                                                                            \
    const int sk_ = ((st) + koff) & 15;                                                        \
    char* dst_ = smem + k2_slot((st) & 3) + lw * 1024;                                         \
    _Pragma("unroll") for (int i = 0; i < 4; ++i) glds16(ap[i] + 128 * sk_, dst_ + i * 4096);  \
  }
    xw_load_stage<false>(x, w_in, rb, n0, smem, lw, l, 0);
    xw_load_stage<false>(x, w_in, rb, n0, smem, lw, l, 1);
    WAITV(11); BAR();
    xw_load_stage<false>(x, w_in, rb, n0, smem, lw, l, 2);
    WAITV(11); BAR();
    xw_load_stage<false>(x, w_in, rb, n0, smem, lw, l, 3);
    K2_LOAD_W(0) K2_LOAD_W(1)
    WAITV(13); BAR();
    K2_LOAD_W(2) K2_LOAD_W(3)
    WAITV(16); BAR();
    WAITV(12); BAR();
    WAITV(8); BAR();
    for (int st = 1; st < 13; ++st) {
      K2_LOAD_W(st + 3)
      WAITV(8); BAR();
    }
    WAITV(4); BAR();
    WAITV(0); BAR();
    return;
  }

  const int fg = w & 3, kg = w >> 2, g = l >> 4, c16 = l & 15;
  const bool two = fg < 3;
  STAMPR(1, 0); STAMP(1, 1);
  const __amdgpu_buffer_rsrc_t rsN = __builtin_amdgcn_make_buffer_rsrc((void*)z2N, 0, S_ * FH * 2, 0x00020000);
  const __amdgpu_buffer_rsrc_t rsN1 = __builtin_amdgcn_make_buffer_rsrc((void*)z2N, 0, two ? S_ * FH * 2 : 0, 0x00020000);

  if (w < 7) {
    const unsigned zoff = (unsigned)(((cb * 7 + w) * 64 + 2 * rb) * 64 + l) * 16u;
    f32x4 sp[4];
#pragma unroll
    for (int i = 0; i < 4; ++i) sp[i] = *(const f32x4*)(stats + (size_t)(t0 + l) * 8 + 2 * i);
    const u32x4 zr0 = *(const u32x4*)((const char*)z2R + zoff), zr1 = *(const u32x4*)((const char*)z2R + zoff + 1024);
    f32x2* mrs = (f32x2*)(smem + XW_LDS + w * 512);
    float sum = 0.f, m2 = 0.f;
#pragma unroll
    for (int i = 0; i < 4; ++i) { sum += sp[i][0] + sp[i][2]; m2 += sp[i][1] + sp[i][3]; }
    const float mean = sum * (1.0f / 896.0f);
#pragma unroll
    for (int i = 0; i < 4; ++i) {
      const float d0 = sp[i][0] * (1.0f / 112.0f) - mean, d1 = sp[i][2] * (1.0f / 112.0f) - mean;
      m2 += 112.0f * (d0 * d0 + d1 * d1);
    }
    const float var = bfr(m2 * (1.0f / 896.0f));
    const float den = bfr(sqrtf(bfr(var + 1e-5f)));
    mrs[l] = f32x2{bfr(mean), 1.0f / den};
#pragma unroll
    for (int hh = 0; hh < 2; ++hh) {
      const u32x4 u = hh ? zr1 : zr0;
      u32x4 v;
#pragma unroll
      for (int e = 0; e < 4; ++e) {
        const f32x4 mr = *(const f32x4*)(mrs + 32 * hh + 8 * g + 2 * e);
        v[e] = pack2(bfr(bf_lo(u[e]) - mr[0]) * mr[1], bfr(bf_hi(u[e]) - mr[2]) * mr[3]);
      }
      __builtin_amdgcn_raw_buffer_store_b128(v, rsN, zoff + 1024u * hh, 0, 16);
    }
  }
  float bias[2];
#pragma unroll
  for (int i = 0; i < 2; ++i) bias[i] = bf_lo(b_sp[(rb * 4 + 2 * kg + i) * 16 + c16]);
  STAMP(1, 2);

  const unsigned boff = (unsigned)(((cb * 7 + 2 * fg) * 64 + kg) * 64 + l) * 16u;
  u32x4 bst[4][4];
#define K2_LOAD_B(slot, st)                                                                       \
  {                                                                                               \
    const int sk_ = ((st) + koff) & 15;                                                           \
    bst[slot][0] = __builtin_amdgcn_raw_buffer_load_b128(rsN, boff, 4096 * sk_, 16);              \
    bst[slot][1] = __builtin_amdgcn_raw_buffer_load_b128(rsN1, boff + 65536, 4096 * sk_, 16);     \
    bst[slot][2] = __builtin_amdgcn_raw_buffer_load_b128(rsN, boff + 2048, 4096 * sk_, 16);       \
    bst[slot][3] = __builtin_amdgcn_raw_buffer_load_b128(rsN1, boff + 65536 + 2048, 4096 * sk_, 16); \
  }
  u32x2 z1p[2][2];
  {
    const XwAddr<false> ad(l, fg, kg);
    f32x4 za[2][2];
#pragma unroll
    for (int i = 0; i < 2; ++i)
#pragma unroll
      for (int j = 0; j < 2; ++j) za[i][j] = f32x4{0.f, 0.f, 0.f, 0.f};
    BAR(); STAMP(1, 9); xw_compute<false, 4>(ad, smem, 0, za);
    WAITV(0);
    if (w < 7 && l == 0) __hip_atomic_fetch_add((gu32*)flags + cb * 32 + rb, 1u, RLX_AGENT);
    BAR(); STAMP(1, 10); xw_compute<false, 4>(ad, smem, 4, za);
    BAR(); STAMP(1, 11); xw_compute<false, 4>(ad, smem, 8, za);
    STAMP(1, 14);
    if (w == 0) {
      const gu32* fp = (const gu32*)flags + cb * 32 + (l & 31);
      for (unsigned spins = 0; spins < (1u << 22); ++spins) {
        if (__all(__hip_atomic_load(fp, RLX_AGENT) == 7u)) break;
        __builtin_amdgcn_s_sleep(1);
      }
    }
    BAR();
    STAMP(1, 3);
    K2_LOAD_B(0, 0) K2_LOAD_B(1, 1) K2_LOAD_B(2, 2)
    xw_compute<false, 2>(ad, smem, 12, za);
    gelu_tile(za);
#pragma unroll
    for (int i = 0; i < 2; ++i)
#pragma unroll
      for (int j = 0; j < 2; ++j) z1p[i][j] = u32x2{pack2(za[i][j][0], za[i][j][1]), pack2(za[i][j][2], za[i][j][3])};
  }
  STAMP(1, 4);

  int ard[4];
#pragma unroll
  for (int i = 0; i < 4; ++i) ard[i] = (16 * ((i + 2 * kg) & 3) + c16) * 256 + (((4 * kg + g) ^ c16) * 16);
  f32x4 acc[4][2];
#pragma unroll
  for (int i = 0; i < 4; ++i)
#pragma unroll
    for (int j = 0; j < 2; ++j) acc[i][j] = f32x4{0.f, 0.f, 0.f, 0.f};

#define K2_STAGE(u, st, PF)                                                                            \
  {                                                                                                    \
    BAR();                                                                                             \
    if (PF) { K2_LOAD_B(((u) + 3) & 3, (st) + 3) }                                                     \
    __builtin_amdgcn_sched_barrier(0);                                                                 \
    const char* ab = smem + k2_slot(u);                                                                \
    _Pragma("unroll") for (int h = 0; h < 2; ++h) {                                                    \
      bf16x8 af[4];                                                                                    \
      _Pragma("unroll") for (int i = 0; i < 4; ++i) af[i] = *(const bf16x8*)(ab + (ard[i] ^ (128 * h))); \
      _Pragma("unroll") for (int j = 0; j < 2; ++j) {                                                  \
        const bf16x8 zf = __builtin_bit_cast(bf16x8, bst[u][2 * h + j]);                               \
        _Pragma("unroll") for (int i = 0; i < 4; ++i)                                                  \
          acc[i][j] = __builtin_amdgcn_mfma_f32_16x16x32_bf16(zf, af[i], acc[i][j], 0, 0, 0);          \
      }                                                                                                \
    }                                                                                                  \
  }
  for (int so = 0; so < 12; so += 4) {
    K2_STAGE(0, so, true) K2_STAGE(1, so + 1, true) K2_STAGE(2, so + 2, true) K2_STAGE(3, so + 3, true)
    STAMP(1, 5 + (so >> 2));
  }
  K2_STAGE(0, 12, true) K2_STAGE(1, 13, false) K2_STAGE(2, 14, false) K2_STAGE(3, 15, false)
  STAMP(1, 8);

  char* red = smem + 32768;
  char* redw = red + ((w ^ 4) < 6 ? (w ^ 4) * 4096 : XW_XB + 57344 - 32768 + ((w ^ 4) - 6) * 4096);
  const char* redr = red + (w < 6 ? w * 4096 : XW_XB + 57344 - 32768 + (w - 6) * 4096);
#pragma unroll
  for (int i = 0; i < 2; ++i)
#pragma unroll
    for (int j = 0; j < 2; ++j) *(f32x4*)(redw + (2 * i + j) * 1024 + l * 16) = acc[2 + i][j];
  BAR();
  STAMP(1, 12);
#pragma unroll
  for (int i = 0; i < 2; ++i) {
    u32x2 hv[2];
#pragma unroll
    for (int j = 0; j < 2; ++j) {
      const f32x4 a = acc[i][j] + *(const f32x4*)(redr + (2 * i + j) * 1024 + l * 16);
      const float h0 = bf_lo(z1p[i][j][0]) * bfr(bfr(a[0]) + bias[i]);
      const float h1 = bf_hi(z1p[i][j][0]) * bfr(bfr(a[1]) + bias[i]);
      const float h2 = bf_lo(z1p[i][j][1]) * bfr(bfr(a[2]) + bias[i]);
      const float h3 = bf_hi(z1p[i][j][1]) * bfr(bfr(a[3]) + bias[i]);
      hv[j] = u32x2{pack2(h0, h1), pack2(h2, h3)};
    }
    const int tt = rb * 4 + 2 * kg + i;
    u16* hp = hF + ((size_t)(tt * 28 + 7 * (cb >> 1)) * 64 + l) * 8;
    if (two) *(u32x4*)(hp + (size_t)(3 * (cb & 1) + fg) * 512) = u32x4{hv[0][0], hv[0][1], hv[1][0], hv[1][1]};
    else     *(u32x2*)(hp + 6 * 512 + 4 * (cb & 1)) = hv[0];
  }
  STAMP(1, 13); STAMPR(1, 15);
}

__global__ __launch_bounds__(768, 3) void k3_out(const u16* __restrict__ hF, const u16* __restrict__ w_out,
                                                u16* __restrict__ out) {
  __shared__ __attribute__((aligned(16))) char smem[896 * 128 + 8 * 2048];
  const int bid = blockIdx.x, xcd = bid & 7, idx = bid >> 3;
  const int rb = xcd * 4 + idx / 7, nb = idx % 7;
  const int tid = threadIdx.x, l = tid & 63, w = __builtin_amdgcn_readfirstlane(tid >> 6);
  const int n0 = nb * 64;
  if (w >= 8) {
    const int lw = w - 8, r8 = l >> 3;
    const u16* wp = w_out + (size_t)(8 * lw + r8) * D_ + n0 + (((l & 7) ^ (((r8 >> 1) & 3) << 1)) * 8);
#pragma unroll
    for (int i = 0; i < 14; ++i) glds16(wp + (size_t)i * 32 * D_, smem + (4 * i + lw) * 1024);
    WAITV(7); BAR();
#pragma unroll
    for (int i = 14; i < 21; ++i) glds16(wp + (size_t)i * 32 * D_, smem + (4 * i + lw) * 1024);
    WAITV(7); BAR();
#pragma unroll
    for (int i = 21; i < 28; ++i) glds16(wp + (size_t)i * 32 * D_, smem + (4 * i + lw) * 1024);
    WAITV(7); BAR();
    WAITV(0); BAR();
    return;
  }
  const int tq = w & 3, kg = w >> 2, g = l >> 4, c16 = l & 15, q = c16 >> 2, p = l & 3;
  const int tt = rb * 4 + tq;
  STAMPR(2, 0); STAMP(2, 1);
  bf16x8 hf[4][4];
  const u16* hp = hF + ((size_t)tt * 28 * 64 + l) * 8;
#define K3_LOAD_H(pc) \
  _Pragma("unroll") for (int i = 0; i < 4; ++i) hf[pc][i] = *(const bf16x8*)(hp + (7 * (pc) + min(2 * i + (((pc) + kg) & 1), 6)) * 512);
  K3_LOAD_H(0) K3_LOAD_H(1)
  const int rrow = 4 * g + q;
  int rb4[4];
#pragma unroll
  for (int j = 0; j < 4; ++j) rb4[j] = rrow * 128 + ((j ^ ((rrow >> 1) & 3)) * 32) + 8 * p;
  f32x4 acc[4];
#pragma unroll
  for (int j = 0; j < 4; ++j) acc[j] = f32x4{0.f, 0.f, 0.f, 0.f};
  STAMP(2, 2);
#pragma unroll
  for (int pc = 0; pc < 4; ++pc) {
    BAR();
    STAMP(2, 3 + pc);
    if (pc < 2) { K3_LOAD_H(pc + 2) }
    const int par = (pc + kg) & 1;
#pragma unroll
    for (int i = 0; i < 4; ++i) {
      if (i < 3 || par == 0) {
        const int e = 2 * i + par;
        const int tA = (e < 6) ? (14 * pc + 7 * (e / 3) + 2 * (e % 3)) : (14 * pc + 6);
        const int tB = (e < 6) ? (tA + 1) : (14 * pc + 13);
#pragma unroll
        for (int j = 0; j < 4; ++j) {
          const bf16x8 wf = tr_frag(smem + rb4[j] + tA * 2048, smem + rb4[j] + tB * 2048);
          acc[j] = __builtin_amdgcn_mfma_f32_16x16x32_bf16(wf, hf[pc][i], acc[j], 0, 0, 0);
        }
      }
    }
  }
  STAMP(2, 7);
  char* red = smem + 896 * 128;
  if (kg == 0) {
    *(f32x4*)(red + (w ^ 4) * 2048 + l * 16) = acc[2];
    *(f32x4*)(red + (w ^ 4) * 2048 + 1024 + l * 16) = acc[3];
  } else {
    *(f32x4*)(red + (w ^ 4) * 2048 + l * 16) = acc[0];
    *(f32x4*)(red + (w ^ 4) * 2048 + 1024 + l * 16) = acc[1];
  }
  BAR();
  const f32x4 a0 = (kg ? acc[2] : acc[0]) + *(const f32x4*)(red + w * 2048 + l * 16);
  const f32x4 a1 = (kg ? acc[3] : acc[1]) + *(const f32x4*)(red + w * 2048 + 1024 + l * 16);
  u16* op = out + (size_t)(tt * 16 + c16) * D_ + n0 + 32 * kg + 4 * g;
  *(u32x2*)op = u32x2{pack2(a0[0], a0[1]), pack2(a0[2], a0[3])};
  *(u32x2*)(op + 16) = u32x2{pack2(a1[0], a1[1]), pack2(a1[2], a1[3])};
  STAMP(2, 8); STAMPR(2, 15);
}

extern "C" void kernel_launch(void* const* d_in, const int* in_sizes, int n_in, void* d_out, int out_size,
                              void* d_ws, size_t ws_size, hipStream_t stream) {
  const u16* x = (const u16*)d_in[0];
  const u16* w_in = (const u16*)d_in[1];
  const u16* w_sp = (const u16*)d_in[2];
  const u16* b_sp = (const u16*)d_in[3];
  const u16* w_out = (const u16*)d_in[4];
  char* ws = (char*)d_ws;
  u16* z2R = (u16*)ws;
  u16* z2N = (u16*)(ws + (size_t)4 * 1024 * 1024);
  u16* hF = (u16*)(ws + (size_t)8 * 1024 * 1024);
  f32x2* stats = (f32x2*)(ws + (size_t)12 * 1024 * 1024);
  unsigned* flags = (unsigned*)(ws + (size_t)13 * 1024 * 1024);
  k1_z2<<<256, 768, 0, stream>>>(x, w_in, z2R, stats, flags, w_sp, w_out);
  k2_spatial<<<256, 768, 0, stream>>>(x, w_in, w_sp, z2R, stats, b_sp, z2N, flags, hF);
  k3_out<<<224, 768, 0, stream>>>(hF, w_out, (u16*)d_out);
}
```

```cpp
#include <hip/hip_runtime.h>
#include <hip/hip_bf16.h>

typedef __bf16 bf16x8 __attribute__((ext_vector_type(8)));
typedef __bf16 bf16x4 __attribute__((ext_vector_type(4)));
typedef __bf16 bf16x2 __attribute__((ext_vector_type(2)));
typedef float f32x4 __attribute__((ext_vector_type(4)));
typedef float f32x2 __attribute__((ext_vector_type(2)));
typedef unsigned u32x4 __attribute__((ext_vector_type(4)));
typedef unsigned u32x2 __attribute__((ext_vector_type(2)));
typedef unsigned short u16;
#define LAS __attribute__((address_space(3)))
#define DI __device__ __forceinline__

#define STAMP(k, i)
#define STAMPR(k, i)
constexpr int S_ = 2048, D_ = 448, F_ = 1792, FH = 896;

DI float bf_lo(unsigned u) { return __builtin_bit_cast(float, u << 16); }
DI float bf_hi(unsigned u) { return __builtin_bit_cast(float, u & 0xffff0000u); }
DI unsigned pack2(float a, float b) {
  f32x2 f = {a, b};
  return __builtin_bit_cast(unsigned, __builtin_convertvector(f, bf16x2));
}
DI float bfr(float a) { return (float)(__bf16)a; }
DI float gelu_tanh(float z) {
  const float y = z * __builtin_fmaf(z * z, -0.10294324f, -2.3022082f);
  const float e = __builtin_amdgcn_exp2f(y);
  return z * __builtin_amdgcn_rcpf(1.0f + e);
}
DI bf16x8 tr_frag(const char* a0, const char* a1) {
  bf16x4 lo = __builtin_amdgcn_ds_read_tr16_b64_v4bf16((LAS bf16x4*)a0);
  bf16x4 hi = __builtin_amdgcn_ds_read_tr16_b64_v4bf16((LAS bf16x4*)a1);
  return __builtin_shufflevector(lo, hi, 0, 1, 2, 3, 4, 5, 6, 7);
}
template <int CTRL> DI float dpp_mov(float x) {
  return __builtin_bit_cast(float, __builtin_amdgcn_update_dpp(0, __builtin_bit_cast(int, x), CTRL, 0xf, 0xf, true));
}
DI float sum16(float x) {
  x += dpp_mov<0xB1>(x);
  x += dpp_mov<0x4E>(x);
  x += dpp_mov<0x141>(x);
  x += dpp_mov<0x140>(x);
  return x;
}

#define BAR()                                              \
  do {                                                     \
    asm volatile("s_waitcnt lgkmcnt(0)" ::: "memory");     \
    __builtin_amdgcn_s_barrier();                          \
    asm volatile("" ::: "memory");                         \
  } while (0)
#define WAITV(n) asm volatile("s_waitcnt vmcnt(" #n ")" ::: "memory")
DI void glds16(const void* src, char* lds_wave_base) {
  __builtin_amdgcn_global_load_lds((const unsigned*)src, (unsigned*)lds_wave_base, 16, 0, 0);
}

constexpr int XW_XB = 7 * 8192, XW_WB = 448 * 224, XW_LDS = XW_XB + XW_WB;

template <bool Z2> DI int x_swz(int row) { return Z2 ? (((row >> 1) & 1) | (((row >> 3) & 3) << 1)) : ((row >> 1) & 7); }

template <bool Z2>
DI void xw_load_stage(const u16* x, const u16* w_in, int rb, int n0, char* smem, int lw, int l, int s) {
  const int nxp = s < 3 ? 4 : 2;
#pragma unroll
  for (int i = 0; i < nxp; ++i) {
    const int pc = 4 * i + lw, blk = 2 * s + (pc >> 3), row = 8 * (pc & 7) + (l >> 3);
    glds16(x + (size_t)(rb * 64 + row) * D_ + blk * 64 + (((l & 7) ^ x_swz<Z2>(row)) * 8), smem + blk * 8192 + (pc & 7) * 1024);
  }
  const int nwp = s < 3 ? 7 : 4;
#pragma unroll
  for (int i = 0; i < nwp; ++i) {
    const int pc = 28 * s + 4 * i + lw;
    if (s < 3 || pc < 98) {
      const int c = pc * 64 + l, row = c / 14, cc = c - row * 14;
      glds16(w_in + (size_t)row * F_ + n0 + cc * 8, smem + XW_XB + pc * 1024);
    }
  }
}
template <bool Z2> struct XwAddr {
  int xa[2], wa[2];
  DI XwAddr(int l, int fg, int mh) {
    const int g = l >> 4, c16 = l & 15, q = c16 >> 2, p = l & 3;
#pragma unroll
    for (int rt = 0; rt < 2; ++rt) {
      const int row = 32 * mh + (Z2 ? (8 * q + 4 * rt + p) : (16 * rt + c16));
      xa[rt] = row * 128 + (((g >> 1) ^ x_swz<Z2>(row)) * 16) + (g & 1) * 8;
    }
#pragma unroll
    for (int jj = 0; jj < 2; ++jj) wa[jj] = XW_XB + (4 * g + q) * 224 + min(2 * fg + jj, 6) * 32 + 8 * p;
  }
};
template <int OFF> DI u32x2 lds_read64(unsigned addr) {
  u32x2 r;
  asm volatile("ds_read_b64 %0, %1 offset:%2" : "=v"(r) : "v"(addr), "i"(OFF) : "memory");
  return r;
}
template <bool Z2, int KS0, int NKS>
DI void xw_compute(const XwAddr<Z2>& ad, const char* smem, f32x4 (&acc)[2][2]) {
  u32x2 xl[NKS][2], xh[NKS][2];
  bf16x8 wf[NKS][2];
  const unsigned sb = (unsigned)(size_t)smem;
#pragma unroll
  for (int k = 0; k < NKS; ++k) {
    constexpr int dummy = 0; (void)dummy;
#pragma unroll
    for (int rt = 0; rt < 2; ++rt) {
      const unsigned o = sb + (ad.xa[rt] ^ (64 * ((KS0 + k) & 1)));
      if (k < 2) { xl[k][rt] = lds_read64<8192 * (KS0 >> 1)>(o); xh[k][rt] = lds_read64<8192 * (KS0 >> 1)>(o ^ 32); }
      else       { xl[k][rt] = lds_read64<8192 * (KS0 >> 1) + 8192>(o); xh[k][rt] = lds_read64<8192 * (KS0 >> 1) + 8192>(o ^ 32); }
    }
#pragma unroll
    for (int jj = 0; jj < 2; ++jj) {
      const char* a = smem + ad.wa[jj] + 32 * (KS0 + k) * 224;
      wf[k][jj] = tr_frag(a, a + 16 * 224);
    }
  }
  asm volatile("s_waitcnt lgkmcnt(0)" ::: "memory");
  __builtin_amdgcn_sched_barrier(0);
#pragma unroll
  for (int k = 0; k < NKS; ++k)
#pragma unroll
    for (int rt = 0; rt < 2; ++rt) {
      const bf16x8 xf = __builtin_bit_cast(bf16x8, u32x4{xl[k][rt][0], xl[k][rt][1], xh[k][rt][0], xh[k][rt][1]});
#pragma unroll
      for (int jj = 0; jj < 2; ++jj) {
        if (Z2) acc[rt][jj] = __builtin_amdgcn_mfma_f32_16x16x32_bf16(xf, wf[k][jj], acc[rt][jj], 0, 0, 0);
        else    acc[rt][jj] = __builtin_amdgcn_mfma_f32_16x16x32_bf16(wf[k][jj], xf, acc[rt][jj], 0, 0, 0);
      }
    }
}
DI unsigned gelu_pair(float a, float b) {
  const unsigned u = pack2(a, b);
  const f32x2 z = {bf_lo(u), bf_hi(u)};
  const f32x2 y = z * __builtin_elementwise_fma(z * z, f32x2{-0.10294324f, -0.10294324f}, f32x2{-2.3022082f, -2.3022082f});
  const f32x2 d = f32x2{__builtin_amdgcn_exp2f(y[0]), __builtin_amdgcn_exp2f(y[1])} + f32x2{1.0f, 1.0f};
  const f32x2 o = z * f32x2{__builtin_amdgcn_rcpf(d[0]), __builtin_amdgcn_rcpf(d[1])};
  return pack2(o[0], o[1]);
}

DI void tile_of_block(int bid, int& rb, int& cb) {
  const int xcd = bid & 7, idx = bid >> 3;
  rb = (xcd >> 1) * 8 + (idx & 7);
  cb = (xcd & 1) * 4 + (idx >> 3);
}

__global__ __launch_bounds__(768, 3) void k1_z2(const u16* __restrict__ x, const u16* __restrict__ w_in,
                                               u16* __restrict__ z2R, f32x2* __restrict__ stats,
                                               unsigned* __restrict__ flags, const u16* __restrict__ w_sp,
                                               const u16* __restrict__ w_out) {
  __shared__ __attribute__((aligned(16))) char smem[XW_LDS + 4 * 64 * 8];
  const int bid = blockIdx.x;
  int rb, cb;
  tile_of_block(bid, rb, cb);
  const int tid = threadIdx.x, l = tid & 63, w = __builtin_amdgcn_readfirstlane(tid >> 6);
  const int n0 = FH + cb * 112;
  if (w >= 8) {
    const int lw = w - 8;
    xw_load_stage<true>(x, w_in, rb, n0, smem, lw, l, 0);
    xw_load_stage<true>(x, w_in, rb, n0, smem, lw, l, 1);
    xw_load_stage<true>(x, w_in, rb, n0, smem, lw, l, 2);
    WAITV(22); BAR();
    xw_load_stage<true>(x, w_in, rb, n0, smem, lw, l, 3);
    u32x4 pf[6];
    {
      const int c = bid * 196 + min(lw * 64 + l, 195);
      pf[0] = *(const u32x4*)(w_in + (size_t)(c / 112) * F_ + (c % 112) * 8);
      pf[1] = *(const u32x4*)(w_out + (size_t)c * 8);
    }
#pragma unroll
    for (int i = 0; i < 4; ++i) pf[2 + i] = *(const u32x4*)(w_sp + ((size_t)bid * 2048 + i * 256 + lw * 64 + l) * 8);
    WAITV(22); BAR();
    WAITV(11); BAR();
    WAITV(6); BAR();
#pragma unroll
    for (int i = 0; i < 6; ++i) asm volatile("" ::"v"(pf[i]));
    return;
  }
  const int fg = w & 3, mh = w >> 2, g = l >> 4, c16 = l & 15;
  const bool two = fg < 3;
  STAMPR(0, 0); STAMP(0, 1);
  if (tid == 0) flags[bid] = 0u;
  const XwAddr<true> ad(l, fg, mh);
  f32x4 acc[2][2];
#pragma unroll
  for (int rt = 0; rt < 2; ++rt)
#pragma unroll
    for (int jj = 0; jj < 2; ++jj) acc[rt][jj] = f32x4{0.f, 0.f, 0.f, 0.f};
  BAR(); STAMP(0, 2); xw_compute<true, 0, 4>(ad, smem, acc);
  BAR(); STAMP(0, 3); xw_compute<true, 4, 4>(ad, smem, acc);
  BAR(); STAMP(0, 4); xw_compute<true, 8, 4>(ad, smem, acc);
  BAR(); STAMP(0, 5); xw_compute<true, 12, 2>(ad, smem, acc);
  STAMP(0, 6);
  u32x4 zp[2];
#pragma unroll
  for (int jj = 0; jj < 2; ++jj)
    zp[jj] = u32x4{gelu_pair(acc[0][jj][0], acc[0][jj][1]), gelu_pair(acc[0][jj][2], acc[0][jj][3]),
                   gelu_pair(acc[1][jj][0], acc[1][jj][1]), gelu_pair(acc[1][jj][2], acc[1][jj][3])};
  STAMP(0, 7);
  const int kstep = rb * 2 + mh;
#pragma unroll
  for (int jj = 0; jj < 2; ++jj)
    if (jj == 0 || two) *(u32x4*)(z2R + ((size_t)((cb * 7 + 2 * fg + jj) * 64 + kstep) * 64 + l) * 8) = zp[jj];
  if (!two) zp[1] = u32x4{0u, 0u, 0u, 0u};
  float v[16];
#pragma unroll
  for (int e = 0; e < 4; ++e) {
    const f32x2 a0 = {bf_lo(zp[0][e]), bf_hi(zp[0][e])}, a1 = {bf_lo(zp[1][e]), bf_hi(zp[1][e])};
    const f32x2 s1 = a0 + a1, s2 = __builtin_elementwise_fma(a0, a0, a1 * a1);
    v[4 * e] = s1[0]; v[4 * e + 1] = s2[0]; v[4 * e + 2] = s1[1]; v[4 * e + 3] = s2[1];
  }
#pragma unroll
  for (int i = 0; i < 8; ++i) { const bool b = c16 & 1; v[i] = (b ? v[i + 8] : v[i]) + dpp_mov<0xB1>(b ? v[i] : v[i + 8]); }
#pragma unroll
  for (int i = 0; i < 4; ++i) { const bool b = c16 & 2; v[i] = (b ? v[i + 4] : v[i]) + dpp_mov<0x4E>(b ? v[i] : v[i + 4]); }
#pragma unroll
  for (int i = 0; i < 2; ++i) { const bool b = c16 & 4; v[i] = (b ? v[i + 2] : v[i]) + dpp_mov<0x124>(b ? v[i] : v[i + 2]); }
  { const bool b = c16 & 8; v[0] = (b ? v[1] : v[0]) + dpp_mov<0x128>(b ? v[0] : v[1]); }
  f32x2* sst = (f32x2*)(smem + XW_LDS);
  {
    const int idx = ((c16 & 1) << 3) | ((c16 & 2) << 1) | ((c16 & 4) >> 1) | (c16 >> 3);
    ((float*)(sst + fg * 64 + 32 * mh + 8 * g))[idx] = v[0];
  }
  BAR();
  if (tid < 64) {
    const f32x2 t = sst[tid] + sst[64 + tid] + sst[128 + tid] + sst[192 + tid];
    stats[(size_t)(rb * 64 + tid) * 8 + cb] = f32x2{t[0], fmaxf(t[1] - t[0] * t[0] * (1.0f / 112.0f), 0.f)};
  }
  STAMP(0, 8); STAMPR(0, 15);
}

typedef __attribute__((address_space(1))) unsigned gu32;
#define RLX_AGENT __ATOMIC_RELAXED, __HIP_MEMORY_SCOPE_AGENT
DI int k2_slot(int i) { return (i & 1) ? (XW_XB + (i >> 1) * 28672) : ((i >> 1) * 16384); }

__global__ __launch_bounds__(768, 3) void k2_spatial(const u16* __restrict__ x, const u16* __restrict__ w_in,
                                                    const u16* __restrict__ w_sp, const u16* __restrict__ z2R,
                                                    const f32x2* __restrict__ stats, const u16* __restrict__ b_sp,
                                                    u16* z2N, unsigned* flags, u16* __restrict__ hF) {
  __shared__ __attribute__((aligned(16))) char smem[XW_LDS + 8 * 512 + 1024];
  const int bid = blockIdx.x;
  int rb, cb;
  tile_of_block(bid, rb, cb);
  const int tid = threadIdx.x, l = tid & 63, w = __builtin_amdgcn_readfirstlane(tid >> 6);
  const int t0 = rb * 64, n0 = cb * 112;
  const int koff = (2 * (rb & 7) + (cb & 1)) & 15;

  if (w >= 8) {
    const int lw = w - 8;
    const u16* ap[4];
#pragma unroll
    for (int i = 0; i < 4; ++i) {
      const int row = 4 * (4 * i + lw) + (l >> 4);
      ap[i] = w_sp + (size_t)(t0 + row) * S_ + (((l & 15) ^ (row & 15)) * 8);
    }
#define K2_LOAD_W(st)                                                                          \
  {                                                                                            \
    const int sk_ = ((st) + koff) & 15;                                                        \
    char* dst_ = smem + k2_slot((st) & 3) + lw * 1024;                                         \
    _Pragma("unroll") for (int i = 0; i < 4; ++i) glds16(ap[i] + 128 * sk_, dst_ + i * 4096);  \
  }
    xw_load_stage<false>(x, w_in, rb, n0, smem, lw, l, 0);
    xw_load_stage<false>(x, w_in, rb, n0, smem, lw, l, 1);
    xw_load_stage<false>(x, w_in, rb, n0, smem, lw, l, 2);
    WAITV(22); BAR();
    xw_load_stage<false>(x, w_in, rb, n0, smem, lw, l, 3);
    WAITV(16); BAR();
    K2_LOAD_W(0) K2_LOAD_W(1)
    WAITV(13); BAR();
    K2_LOAD_W(2) K2_LOAD_W(3)
    WAITV(16); BAR();
    WAITV(12); BAR();
    WAITV(8); BAR();
    for (int st = 1; st < 13; ++st) {
      K2_LOAD_W(st + 3)
      WAITV(8); BAR();
    }
    WAITV(4); BAR();
    WAITV(0); BAR();
    return;
  }

  const int fg = w & 3, kg = w >> 2, g = l >> 4, c16 = l & 15;
  const bool two = fg < 3;
  STAMPR(1, 0); STAMP(1, 1);
  const __amdgpu_buffer_rsrc_t rsN = __builtin_amdgcn_make_buffer_rsrc((void*)z2N, 0, S_ * FH * 2, 0x00020000);
  const __amdgpu_buffer_rsrc_t rsN1 = __builtin_amdgcn_make_buffer_rsrc((void*)z2N, 0, two ? S_ * FH * 2 : 0, 0x00020000);

  if (w < 7) {
    const unsigned zoff = (unsigned)(((cb * 7 + w) * 64 + 2 * rb) * 64 + l) * 16u;
    f32x4 sp[4];
#pragma unroll
    for (int i = 0; i < 4; ++i) sp[i] = *(const f32x4*)(stats + (size_t)(t0 + l) * 8 + 2 * i);
    const u32x4 zr0 = *(const u32x4*)((const char*)z2R + zoff), zr1 = *(const u32x4*)((const char*)z2R + zoff + 1024);
    f32x2* mrs = (f32x2*)(smem + XW_LDS + w * 512);
    float sum = 0.f, m2 = 0.f;
#pragma unroll
    for (int i = 0; i < 4; ++i) { sum += sp[i][0] + sp[i][2]; m2 += sp[i][1] + sp[i][3]; }
    const float mean = sum * (1.0f / 896.0f);
#pragma unroll
    for (int i = 0; i < 4; ++i) {
      const float d0 = sp[i][0] * (1.0f / 112.0f) - mean, d1 = sp[i][2] * (1.0f / 112.0f) - mean;
      m2 += 112.0f * (d0 * d0 + d1 * d1);
    }
    const float var = bfr(m2 * (1.0f / 896.0f));
    const float den = bfr(sqrtf(bfr(var + 1e-5f)));
    mrs[l] = f32x2{bfr(mean), 1.0f / den};
#pragma unroll
    for (int hh = 0; hh < 2; ++hh) {
      const u32x4 u = hh ? zr1 : zr0;
      u32x4 v;
#pragma unroll
      for (int e = 0; e < 4; ++e) {
        const f32x4 mr = *(const f32x4*)(mrs + 32 * hh + 8 * g + 2 * e);
        v[e] = pack2(bfr(bf_lo(u[e]) - mr[0]) * mr[1], bfr(bf_hi(u[e]) - mr[2]) * mr[3]);
      }
      __builtin_amdgcn_raw_buffer_store_b128(v, rsN, zoff + 1024u * hh, 0, 16);
    }
  } else {
    const unsigned dst = __builtin_amdgcn_readfirstlane((unsigned)(size_t)(smem + XW_LDS + 8 * 512));
#pragma unroll
    for (int i = 0; i < 16; ++i) {
      unsigned keep;
      asm volatile("s_mov_b32 %0, m0\n\ts_mov_b32 m0, %2\n\ts_nop 0\n\tglobal_load_lds_dwordx4 %1, off\n\ts_mov_b32 m0, %0"
                   : "=&s"(keep) : "v"(w_sp + ((size_t)bid * 2048 + 1024 + i * 64 + l) * 8), "s"(dst) : "memory");
    }
  }
  float bias[2];
#pragma unroll
  for (int i = 0; i < 2; ++i) bias[i] = bf_lo(b_sp[(rb * 4 + 2 * kg + i) * 16 + c16]);
  STAMP(1, 2);

  const unsigned boff = (unsigned)(((cb * 7 + 2 * fg) * 64 + kg) * 64 + l) * 16u;
  u32x4 bst[4][4];
#define K2_LOAD_B(slot, st)                                                                       \
  {                                                                                               \
    const int sk_ = ((st) + koff) & 15;                                                           \
    bst[slot][0] = __builtin_amdgcn_raw_buffer_load_b128(rsN, boff, 4096 * sk_, 16);              \
    bst[slot][1] = __builtin_amdgcn_raw_buffer_load_b128(rsN1, boff + 65536, 4096 * sk_, 16);     \
    bst[slot][2] = __builtin_amdgcn_raw_buffer_load_b128(rsN, boff + 2048, 4096 * sk_, 16);       \
    bst[slot][3] = __builtin_amdgcn_raw_buffer_load_b128(rsN1, boff + 65536 + 2048, 4096 * sk_, 16); \
  }
  u32x2 z1p[2][2];
  {
    const XwAddr<false> ad(l, fg, kg);
    f32x4 za[2][2];
#pragma unroll
    for (int i = 0; i < 2; ++i)
#pragma unroll
      for (int j = 0; j < 2; ++j) za[i][j] = f32x4{0.f, 0.f, 0.f, 0.f};
    BAR(); STAMP(1, 9); xw_compute<false, 0, 4>(ad, smem, za);
    if (w < 7) {
      WAITV(0);
      if (l == 0) __hip_atomic_fetch_add((gu32*)flags + cb * 32 + rb, 1u, RLX_AGENT);
    }
    BAR(); STAMP(1, 10); xw_compute<false, 4, 4>(ad, smem, za);
    BAR(); STAMP(1, 11); xw_compute<false, 8, 4>(ad, smem, za);
    STAMP(1, 14);
    if (w == 0) {
      const gu32* fp = (const gu32*)flags + cb * 32 + (l & 31);
      for (unsigned spins = 0; spins < (1u << 22); ++spins) {
        if (__all(__hip_atomic_load(fp, RLX_AGENT) == 7u)) break;
        __builtin_amdgcn_s_sleep(1);
      }
    }
    BAR();
    STAMP(1, 3);
    K2_LOAD_B(0, 0) K2_LOAD_B(1, 1) K2_LOAD_B(2, 2)
    xw_compute<false, 12, 2>(ad, smem, za);
#pragma unroll
    for (int i = 0; i < 2; ++i)
#pragma unroll
      for (int j = 0; j < 2; ++j) z1p[i][j] = u32x2{gelu_pair(za[i][j][0], za[i][j][1]), gelu_pair(za[i][j][2], za[i][j][3])};
  }
  STAMP(1, 4);

  int ard[4];
#pragma unroll
  for (int i = 0; i < 4; ++i) ard[i] = (16 * ((i + 2 * kg) & 3) + c16) * 256 + (((4 * kg + g) ^ c16) * 16);
  f32x4 acc[4][2];
#pragma unroll
  for (int i = 0; i < 4; ++i)
#pragma unroll
    for (int j = 0; j < 2; ++j) acc[i][j] = f32x4{0.f, 0.f, 0.f, 0.f};

#define K2_STAGE(u, st, PF)                                                                            \
  {                                                                                                    \
    BAR();                                                                                             \
    if (PF) { K2_LOAD_B(((u) + 3) & 3, (st) + 3) }                                                     \
    __builtin_amdgcn_sched_barrier(0);                                                                 \
    const char* ab = smem + k2_slot(u);                                                                \
    _Pragma("unroll") for (int h = 0; h < 2; ++h) {                                                    \
      bf16x8 af[4];                                                                                    \
      _Pragma("unroll") for (int i = 0; i < 4; ++i) af[i] = *(const bf16x8*)(ab + (ard[i] ^ (128 * h))); \
      _Pragma("unroll") for (int j = 0; j < 2; ++j) {                                                  \
        const bf16x8 zf = __builtin_bit_cast(bf16x8, bst[u][2 * h + j]);                               \
        _Pragma("unroll") for (int i = 0; i < 4; ++i)                                                  \
          acc[i][j] = __builtin_amdgcn_mfma_f32_16x16x32_bf16(zf, af[i], acc[i][j], 0, 0, 0);          \
      }                                                                                                \
    }                                                                                                  \
  }
  for (int so = 0; so < 12; so += 4) {
    K2_STAGE(0, so, true) K2_STAGE(1, so + 1, true) K2_STAGE(2, so + 2, true) K2_STAGE(3, so + 3, true)
    STAMP(1, 5 + (so >> 2));
  }
  K2_STAGE(0, 12, true) K2_STAGE(1, 13, false) K2_STAGE(2, 14, false) K2_STAGE(3, 15, false)
  STAMP(1, 8);

  char* red = smem + 32768;
  char* redw = red + ((w ^ 4) < 6 ? (w ^ 4) * 4096 : XW_XB + 57344 - 32768 + ((w ^ 4) - 6) * 4096);
  const char* redr = red + (w < 6 ? w * 4096 : XW_XB + 57344 - 32768 + (w - 6) * 4096);
#pragma unroll
  for (int i = 0; i < 2; ++i)
#pragma unroll
    for (int j = 0; j < 2; ++j) *(f32x4*)(redw + (2 * i + j) * 1024 + l * 16) = acc[2 + i][j];
  BAR();
  STAMP(1, 12);
#pragma unroll
  for (int i = 0; i < 2; ++i) {
    u32x2 hv[2];
#pragma unroll
    for (int j = 0; j < 2; ++j) {
      const f32x4 a = acc[i][j] + *(const f32x4*)(redr + (2 * i + j) * 1024 + l * 16);
      const float h0 = bf_lo(z1p[i][j][0]) * bfr(bfr(a[0]) + bias[i]);
      const float h1 = bf_hi(z1p[i][j][0]) * bfr(bfr(a[1]) + bias[i]);
      const float h2 = bf_lo(z1p[i][j][1]) * bfr(bfr(a[2]) + bias[i]);
      const float h3 = bf_hi(z1p[i][j][1]) * bfr(bfr(a[3]) + bias[i]);
      hv[j] = u32x2{pack2(h0, h1), pack2(h2, h3)};
    }
    const int tt = rb * 4 + 2 * kg + i;
    u16* hp = hF + ((size_t)(tt * 28 + 7 * (cb >> 1)) * 64 + l) * 8;
    if (two) *(u32x4*)(hp + (size_t)(3 * (cb & 1) + fg) * 512) = u32x4{hv[0][0], hv[0][1], hv[1][0], hv[1][1]};
    else     *(u32x2*)(hp + 6 * 512 + 4 * (cb & 1)) = hv[0];
  }
  STAMP(1, 13); STAMPR(1, 15);
}

__global__ __launch_bounds__(768, 3) void k3_out(const u16* __restrict__ hF, const u16* __restrict__ w_out,
                                                u16* __restrict__ out) {
  __shared__ __attribute__((aligned(16))) char smem[896 * 128 + 8 * 2048];
  const int bid = blockIdx.x, xcd = bid & 7, idx = bid >> 3;
  const int rb = xcd * 4 + idx / 7, nb = idx % 7;
  const int tid = threadIdx.x, l = tid & 63, w = __builtin_amdgcn_readfirstlane(tid >> 6);
  const int n0 = nb * 64;
  if (w >= 8) {
    const int lw = w - 8, r8 = l >> 3;
    const u16* wp = w_out + (size_t)(8 * lw + r8) * D_ + n0 + (((l & 7) ^ (((r8 >> 1) & 3) << 1)) * 8);
#pragma unroll
    for (int i = 0; i < 14; ++i) glds16(wp + (size_t)i * 32 * D_, smem + (4 * i + lw) * 1024);
    WAITV(7); BAR();
#pragma unroll
    for (int i = 14; i < 21; ++i) glds16(wp + (size_t)i * 32 * D_, smem + (4 * i + lw) * 1024);
    WAITV(7); BAR();
#pragma unroll
    for (int i = 21; i < 28; ++i) glds16(wp + (size_t)i * 32 * D_, smem + (4 * i + lw) * 1024);
    WAITV(7); BAR();
    WAITV(0); BAR();
    return;
  }
  const int tq = w & 3, kg = w >> 2, g = l >> 4, c16 = l & 15, q = c16 >> 2, p = l & 3;
  const int tt = rb * 4 + tq;
  STAMPR(2, 0); STAMP(2, 1);
  bf16x8 hf[4][4];
  const u16* hp = hF + ((size_t)tt * 28 * 64 + l) * 8;
#define K3_LOAD_H(pc) \
  _Pragma("unroll") for (int i = 0; i < 4; ++i) hf[pc][i] = *(const bf16x8*)(hp + (7 * (pc) + min(2 * i + (((pc) + kg) & 1), 6)) * 512);
  K3_LOAD_H(0) K3_LOAD_H(1)
  const int rrow = 4 * g + q;
  int rb4[4];
#pragma unroll
  for (int j = 0; j < 4; ++j) rb4[j] = rrow * 128 + ((j ^ ((rrow >> 1) & 3)) * 32) + 8 * p;
  f32x4 acc[4];
#pragma unroll
  for (int j = 0; j < 4; ++j) acc[j] = f32x4{0.f, 0.f, 0.f, 0.f};
  STAMP(2, 2);
#pragma unroll
  for (int pc = 0; pc < 4; ++pc) {
    BAR();
    STAMP(2, 3 + pc);
    if (pc < 2) { K3_LOAD_H(pc + 2) }
    const int par = (pc + kg) & 1;
#pragma unroll
    for (int i = 0; i < 4; ++i) {
      if (i < 3 || par == 0) {
        const int e = 2 * i + par;
        const int tA = (e < 6) ? (14 * pc + 7 * (e / 3) + 2 * (e % 3)) : (14 * pc + 6);
        const int tB = (e < 6) ? (tA + 1) : (14 * pc + 13);
#pragma unroll
        for (int j = 0; j < 4; ++j) {
          const bf16x8 wf = tr_frag(smem + rb4[j] + tA * 2048, smem + rb4[j] + tB * 2048);
          acc[j] = __builtin_amdgcn_mfma_f32_16x16x32_bf16(wf, hf[pc][i], acc[j], 0, 0, 0);
        }
      }
    }
  }
  STAMP(2, 7);
  char* red = smem + 896 * 128;
  if (kg == 0) {
    *(f32x4*)(red + (w ^ 4) * 2048 + l * 16) = acc[2];
    *(f32x4*)(red + (w ^ 4) * 2048 + 1024 + l * 16) = acc[3];
  } else {
    *(f32x4*)(red + (w ^ 4) * 2048 + l * 16) = acc[0];
    *(f32x4*)(red + (w ^ 4) * 2048 + 1024 + l * 16) = acc[1];
  }
  BAR();
  const f32x4 a0 = (kg ? acc[2] : acc[0]) + *(const f32x4*)(red + w * 2048 + l * 16);
  const f32x4 a1 = (kg ? acc[3] : acc[1]) + *(const f32x4*)(red + w * 2048 + 1024 + l * 16);
  u16* op = out + (size_t)(tt * 16 + c16) * D_ + n0 + 32 * kg + 4 * g;
  *(u32x2*)op = u32x2{pack2(a0[0], a0[1]), pack2(a0[2], a0[3])};
  *(u32x2*)(op + 16) = u32x2{pack2(a1[0], a1[1]), pack2(a1[2], a1[3])};
  STAMP(2, 8); STAMPR(2, 15);
}

extern "C" void kernel_launch(void* const* d_in, const int* in_sizes, int n_in, void* d_out, int out_size,
                              void* d_ws, size_t ws_size, hipStream_t stream) {
  const u16* x = (const u16*)d_in[0];
  const u16* w_in = (const u16*)d_in[1];
  const u16* w_sp = (const u16*)d_in[2];
  const u16* b_sp = (const u16*)d_in[3];
  const u16* w_out = (const u16*)d_in[4];
  char* ws = (char*)d_ws;
  u16* z2R = (u16*)ws;
  u16* z2N = (u16*)(ws + (size_t)4 * 1024 * 1024);
  u16* hF = (u16*)(ws + (size_t)8 * 1024 * 1024);
  f32x2* stats = (f32x2*)(ws + (size_t)12 * 1024 * 1024);
  unsigned* flags = (unsigned*)(ws + (size_t)13 * 1024 * 1024);
  k1_z2<<<256, 768, 0, stream>>>(x, w_in, z2R, stats, flags, w_sp, w_out);
  k2_spatial<<<256, 768, 0, stream>>>(x, w_in, w_sp, z2R, stats, b_sp, z2N, flags, hF);
  k3_out<<<224, 768, 0, stream>>>(hF, w_out, (u16*)d_out);
}
```

```cpp
#include <hip/hip_runtime.h>
#include <hip/hip_bf16.h>

typedef __bf16 bf16x8 __attribute__((ext_vector_type(8)));
typedef __bf16 bf16x4 __attribute__((ext_vector_type(4)));
typedef __bf16 bf16x2 __attribute__((ext_vector_type(2)));
typedef float f32x4 __attribute__((ext_vector_type(4)));
typedef float f32x2 __attribute__((ext_vector_type(2)));
typedef unsigned u32x4 __attribute__((ext_vector_type(4)));
typedef unsigned u32x2 __attribute__((ext_vector_type(2)));
typedef unsigned short u16;
#define LAS __attribute__((address_space(3)))
#define DI __device__ __forceinline__

#define STAMP(k, i)
#define STAMPR(k, i)
constexpr int S_ = 2048, D_ = 448, F_ = 1792, FH = 896;

DI float bf_lo(unsigned u) { return __builtin_bit_cast(float, u << 16); }
DI float bf_hi(unsigned u) { return __builtin_bit_cast(float, u & 0xffff0000u); }
DI unsigned pack2(float a, float b) {
  f32x2 f = {a, b};
  return __builtin_bit_cast(unsigned, __builtin_convertvector(f, bf16x2));
}
DI float bfr(float a) { return (float)(__bf16)a; }
DI float gelu_tanh(float z) {
  const float y = z * __builtin_fmaf(z * z, -0.10294324f, -2.3022082f);
  const float e = __builtin_amdgcn_exp2f(y);
  return z * __builtin_amdgcn_rcpf(1.0f + e);
}
DI bf16x8 tr_frag(const char* a0, const char* a1) {
  bf16x4 lo = __builtin_amdgcn_ds_read_tr16_b64_v4bf16((LAS bf16x4*)a0);
  bf16x4 hi = __builtin_amdgcn_ds_read_tr16_b64_v4bf16((LAS bf16x4*)a1);
  return __builtin_shufflevector(lo, hi, 0, 1, 2, 3, 4, 5, 6, 7);
}
template <int CTRL> DI float dpp_mov(float x) {
  return __builtin_bit_cast(float, __builtin_amdgcn_update_dpp(0, __builtin_bit_cast(int, x), CTRL, 0xf, 0xf, true));
}
DI float sum16(float x) {
  x += dpp_mov<0xB1>(x);
  x += dpp_mov<0x4E>(x);
  x += dpp_mov<0x141>(x);
  x += dpp_mov<0x140>(x);
  return x;
}

#define BAR()                                              \
  do {                                                     \
    asm volatile("s_waitcnt lgkmcnt(0)" ::: "memory");     \
    __builtin_amdgcn_s_barrier();                          \
    asm volatile("" ::: "memory");                         \
  } while (0)
#define WAITV(n) asm volatile("s_waitcnt vmcnt(" #n ")" ::: "memory")
typedef __amdgpu_buffer_rsrc_t rsrc_t;
DI rsrc_t make_rsrc(const void* p, unsigned bytes) { return __builtin_amdgcn_make_buffer_rsrc((void*)p, 0, bytes, 0x00020000); }
DI void blds16(rsrc_t rs, char* lds_wave_base, unsigned voff, unsigned soff) {
  __builtin_amdgcn_raw_ptr_buffer_load_lds(rs, (LAS void*)lds_wave_base, 16, voff, soff, 0, 0);
}

constexpr int XW_XB = 7 * 8192, XW_WB = 448 * 224, XW_LDS = XW_XB + XW_WB;

template <bool Z2> DI int x_swz(int row) { return Z2 ? (((row >> 1) & 1) | (((row >> 3) & 3) << 1)) : ((row >> 1) & 7); }

template <bool Z2> struct XwLoader {
  rsrc_t rx, rw;
  unsigned xo[4], wo[7];
  int lw;
  DI XwLoader(const u16* x, const u16* w_in, int rb, int n0, int lw_, int l) : lw(lw_) {
    rx = make_rsrc(x, S_ * D_ * 2);
    rw = make_rsrc(w_in, D_ * F_ * 2);
#pragma unroll
    for (int i = 0; i < 4; ++i) {
      const int pc = 4 * i + lw, row = 8 * (pc & 7) + (l >> 3);
      xo[i] = (unsigned)((rb * 64 + row) * D_ + (pc >> 3) * 64 + (((l & 7) ^ x_swz<Z2>(row)) * 8)) * 2u;
    }
#pragma unroll
    for (int i = 0; i < 7; ++i) {
      const int c = (4 * i + lw) * 64 + l, row = c / 14, cc = c - row * 14;
      wo[i] = (unsigned)(row * F_ + n0 + cc * 8) * 2u;
    }
  }
  DI void stage(char* smem, int s) const {
#pragma unroll
    for (int i = 0; i < (s < 3 ? 4 : 2); ++i) blds16(rx, smem + 2 * s * 8192 + (4 * i + lw) * 1024, xo[i], 256 * s);
#pragma unroll
    for (int i = 0; i < (s < 3 ? 7 : 4); ++i)
      if (s < 3 || 4 * i + lw < 14) blds16(rw, smem + XW_XB + (28 * s + 4 * i + lw) * 1024, wo[i], 128 * F_ * 2 * s);
  }
};
template <bool Z2> struct XwAddr {
  int xa[2], wa[2];
  DI XwAddr(int l, int fg, int mh) {
    const int g = l >> 4, c16 = l & 15, q = c16 >> 2, p = l & 3;
#pragma unroll
    for (int rt = 0; rt < 2; ++rt) {
      const int row = 32 * mh + (Z2 ? (8 * q + 4 * rt + p) : (16 * rt + c16));
      xa[rt] = row * 128 + (((g >> 1) ^ x_swz<Z2>(row)) * 16) + (g & 1) * 8;
    }
#pragma unroll
    for (int jj = 0; jj < 2; ++jj) wa[jj] = XW_XB + (4 * g + q) * 224 + min(2 * fg + jj, 6) * 32 + 8 * p;
  }
};
template <int OFF> DI u32x2 lds_read64(unsigned addr) {
  u32x2 r;
  asm volatile("ds_read_b64 %0, %1 offset:%2" : "=v"(r) : "v"(addr), "i"(OFF) : "memory");
  return r;
}
template <bool Z2, int KS0, int NKS>
DI void xw_compute(const XwAddr<Z2>& ad, const char* smem, f32x4 (&acc)[2][2]) {
  u32x2 xl[NKS][2], xh[NKS][2];
  bf16x8 wf[NKS][2];
  const unsigned sb = (unsigned)(size_t)smem;
#pragma unroll
  for (int k = 0; k < NKS; ++k) {
    constexpr int dummy = 0; (void)dummy;
#pragma unroll
    for (int rt = 0; rt < 2; ++rt) {
      const unsigned o = sb + (ad.xa[rt] ^ (64 * ((KS0 + k) & 1)));
      if (k < 2) { xl[k][rt] = lds_read64<8192 * (KS0 >> 1)>(o); xh[k][rt] = lds_read64<8192 * (KS0 >> 1)>(o ^ 32); }
      else       { xl[k][rt] = lds_read64<8192 * (KS0 >> 1) + 8192>(o); xh[k][rt] = lds_read64<8192 * (KS0 >> 1) + 8192>(o ^ 32); }
    }
#pragma unroll
    for (int jj = 0; jj < 2; ++jj) {
      const char* a = smem + ad.wa[jj] + 32 * (KS0 + k) * 224;
      wf[k][jj] = tr_frag(a, a + 16 * 224);
    }
  }
  asm volatile("s_waitcnt lgkmcnt(0)" ::: "memory");
  __builtin_amdgcn_sched_barrier(0);
#pragma unroll
  for (int k = 0; k < NKS; ++k)
#pragma unroll
    for (int rt = 0; rt < 2; ++rt) {
      const bf16x8 xf = __builtin_bit_cast(bf16x8, u32x4{xl[k][rt][0], xl[k][rt][1], xh[k][rt][0], xh[k][rt][1]});
#pragma unroll
      for (int jj = 0; jj < 2; ++jj) {
        if (Z2) acc[rt][jj] = __builtin_amdgcn_mfma_f32_16x16x32_bf16(xf, wf[k][jj], acc[rt][jj], 0, 0, 0);
        else    acc[rt][jj] = __builtin_amdgcn_mfma_f32_16x16x32_bf16(wf[k][jj], xf, acc[rt][jj], 0, 0, 0);
      }
    }
}
DI unsigned gelu_pair(float a, float b) {
  const unsigned u = pack2(a, b);
  const f32x2 z = {bf_lo(u), bf_hi(u)};
  const f32x2 y = z * __builtin_elementwise_fma(z * z, f32x2{-0.10294324f, -0.10294324f}, f32x2{-2.3022082f, -2.3022082f});
  const f32x2 d = f32x2{__builtin_amdgcn_exp2f(y[0]), __builtin_amdgcn_exp2f(y[1])} + f32x2{1.0f, 1.0f};
  const f32x2 o = z * f32x2{__builtin_amdgcn_rcpf(d[0]), __builtin_amdgcn_rcpf(d[1])};
  return pack2(o[0], o[1]);
}

DI void tile_of_block(int bid, int& rb, int& cb) {
  const int xcd = bid & 7, idx = bid >> 3;
  rb = (xcd >> 1) * 8 + (idx & 7);
  cb = (xcd & 1) * 4 + (idx >> 3);
}

__global__ __launch_bounds__(768, 3) void k1_z2(const u16* __restrict__ x, const u16* __restrict__ w_in,
                                               u16* __restrict__ z2R, f32x2* __restrict__ stats,
                                               unsigned* __restrict__ flags, const u16* __restrict__ w_sp,
                                               const u16* __restrict__ w_out) {
  __shared__ __attribute__((aligned(16))) char smem[XW_LDS + 4 * 64 * 8];
  const int bid = blockIdx.x;
  int rb, cb;
  tile_of_block(bid, rb, cb);
  const int tid = threadIdx.x, l = tid & 63, w = __builtin_amdgcn_readfirstlane(tid >> 6);
  const int n0 = FH + cb * 112;
  if (w >= 8) {
    const int lw = w - 8;
    const XwLoader<true> ld(x, w_in, rb, n0, lw, l);
    ld.stage(smem, 0);
    ld.stage(smem, 1);
    ld.stage(smem, 2);
    WAITV(22); BAR();
    ld.stage(smem, 3);
    u32x4 pf[6];
    {
      const int c = bid * 196 + min(lw * 64 + l, 195);
      pf[0] = *(const u32x4*)(w_in + (size_t)(c / 112) * F_ + (c % 112) * 8);
      pf[1] = *(const u32x4*)(w_out + (size_t)c * 8);
    }
#pragma unroll
    for (int i = 0; i < 4; ++i) pf[2 + i] = *(const u32x4*)(w_sp + ((size_t)bid * 2048 + i * 256 + lw * 64 + l) * 8);
    WAITV(22); BAR();
    WAITV(11); BAR();
    WAITV(6); BAR();
#pragma unroll
    for (int i = 0; i < 6; ++i) asm volatile("" ::"v"(pf[i]));
    return;
  }
  const int fg = w & 3, mh = w >> 2, g = l >> 4, c16 = l & 15;
  const bool two = fg < 3;
  STAMPR(0, 0); STAMP(0, 1);
  if (tid == 0) flags[bid * 64] = 0u;
  const XwAddr<true> ad(l, fg, mh);
  f32x4 acc[2][2];
#pragma unroll
  for (int rt = 0; rt < 2; ++rt)
#pragma unroll
    for (int jj = 0; jj < 2; ++jj) acc[rt][jj] = f32x4{0.f, 0.f, 0.f, 0.f};
  BAR(); STAMP(0, 2); xw_compute<true, 0, 4>(ad, smem, acc);
  BAR(); STAMP(0, 3); xw_compute<true, 4, 4>(ad, smem, acc);
  BAR(); STAMP(0, 4); xw_compute<true, 8, 4>(ad, smem, acc);
  BAR(); STAMP(0, 5); xw_compute<true, 12, 2>(ad, smem, acc);
  STAMP(0, 6);
  u32x4 zp[2];
#pragma unroll
  for (int jj = 0; jj < 2; ++jj)
    zp[jj] = u32x4{gelu_pair(acc[0][jj][0], acc[0][jj][1]), gelu_pair(acc[0][jj][2], acc[0][jj][3]),
                   gelu_pair(acc[1][jj][0], acc[1][jj][1]), gelu_pair(acc[1][jj][2], acc[1][jj][3])};
  STAMP(0, 7);
  const int kstep = rb * 2 + mh;
#pragma unroll
  for (int jj = 0; jj < 2; ++jj)
    if (jj == 0 || two) *(u32x4*)(z2R + ((size_t)((cb * 7 + 2 * fg + jj) * 64 + kstep) * 64 + l) * 8) = zp[jj];
  if (!two) zp[1] = u32x4{0u, 0u, 0u, 0u};
  float v[16];
#pragma unroll
  for (int e = 0; e < 4; ++e) {
    const f32x2 a0 = {bf_lo(zp[0][e]), bf_hi(zp[0][e])}, a1 = {bf_lo(zp[1][e]), bf_hi(zp[1][e])};
    const f32x2 s1 = a0 + a1, s2 = __builtin_elementwise_fma(a0, a0, a1 * a1);
    v[4 * e] = s1[0]; v[4 * e + 1] = s2[0]; v[4 * e + 2] = s1[1]; v[4 * e + 3] = s2[1];
  }
#pragma unroll
  for (int i = 0; i < 8; ++i) { const bool b = c16 & 1; v[i] = (b ? v[i + 8] : v[i]) + dpp_mov<0xB1>(b ? v[i] : v[i + 8]); }
#pragma unroll
  for (int i = 0; i < 4; ++i) { const bool b = c16 & 2; v[i] = (b ? v[i + 4] : v[i]) + dpp_mov<0x4E>(b ? v[i] : v[i + 4]); }
#pragma unroll
  for (int i = 0; i < 2; ++i) { const bool b = c16 & 4; v[i] = (b ? v[i + 2] : v[i]) + dpp_mov<0x124>(b ? v[i] : v[i + 2]); }
  { const bool b = c16 & 8; v[0] = (b ? v[1] : v[0]) + dpp_mov<0x128>(b ? v[0] : v[1]); }
  f32x2* sst = (f32x2*)(smem + XW_LDS);
  {
    const int idx = ((c16 & 1) << 3) | ((c16 & 2) << 1) | ((c16 & 4) >> 1) | (c16 >> 3);
    ((float*)(sst + fg * 64 + 32 * mh + 8 * g))[idx] = v[0];
  }
  BAR();
  if (tid < 64) {
    const f32x2 t = sst[tid] + sst[64 + tid] + sst[128 + tid] + sst[192 + tid];
    stats[(size_t)(rb * 64 + tid) * 8 + cb] = f32x2{t[0], fmaxf(t[1] - t[0] * t[0] * (1.0f / 112.0f), 0.f)};
  }
  STAMP(0, 8); STAMPR(0, 15);
}

typedef __attribute__((address_space(1))) unsigned gu32;
#define RLX_AGENT __ATOMIC_RELAXED, __HIP_MEMORY_SCOPE_AGENT
DI int k2_slot(int i) { return (i & 1) ? (XW_XB + (i >> 1) * 28672) : ((i >> 1) * 16384); }

__global__ __launch_bounds__(768, 3) void k2_spatial(const u16* __restrict__ x, const u16* __restrict__ w_in,
                                                    const u16* __restrict__ w_sp, const u16* __restrict__ z2R,
                                                    const f32x2* __restrict__ stats, const u16* __restrict__ b_sp,
                                                    u16* z2N, unsigned* flags, u16* __restrict__ hF) {
  __shared__ __attribute__((aligned(16))) char smem[XW_LDS + 8 * 512 + 1024];
  const int bid = blockIdx.x;
  int rb, cb;
  tile_of_block(bid, rb, cb);
  const int tid = threadIdx.x, l = tid & 63, w = __builtin_amdgcn_readfirstlane(tid >> 6);
  const int t0 = rb * 64, n0 = cb * 112;
  const int koff = (2 * (rb & 7) + (cb & 1)) & 15;

  if (w >= 8) {
    const int lw = w - 8;
    const XwLoader<false> ld(x, w_in, rb, n0, lw, l);
    const rsrc_t rsA = make_rsrc(w_sp, S_ * S_ * 2);
    unsigned ap[4];
#pragma unroll
    for (int i = 0; i < 4; ++i) {
      const int row = 4 * (4 * i + lw) + (l >> 4);
      ap[i] = (unsigned)((t0 + row) * S_ + (((l & 15) ^ (row & 15)) * 8)) * 2u;
    }
#define K2_LOAD_W(st)                                                                             \
  {                                                                                               \
    const int sk_ = ((st) + koff) & 15;                                                           \
    char* dst_ = smem + k2_slot((st) & 3) + lw * 1024;                                            \
    _Pragma("unroll") for (int i = 0; i < 4; ++i) blds16(rsA, dst_ + i * 4096, ap[i], 256 * sk_); \
  }
    ld.stage(smem, 0);
    ld.stage(smem, 1);
    WAITV(11); BAR();
    ld.stage(smem, 2);
    ld.stage(smem, 3);
    WAITV(16); BAR();
    K2_LOAD_W(0) K2_LOAD_W(1)
    WAITV(13); BAR();
    K2_LOAD_W(2) K2_LOAD_W(3)
    WAITV(16); BAR();
    WAITV(12); BAR();
    WAITV(8); BAR();
    for (int st = 1; st < 13; ++st) {
      K2_LOAD_W(st + 3)
      WAITV(8); BAR();
    }
    WAITV(4); BAR();
    WAITV(0); BAR();
    return;
  }

  const int fg = w & 3, kg = w >> 2, g = l >> 4, c16 = l & 15;
  const bool two = fg < 3;
  STAMPR(1, 0); STAMP(1, 1);
  const rsrc_t rsN = make_rsrc(z2N, S_ * FH * 2);
  const rsrc_t rsN1 = make_rsrc(z2N, two ? S_ * FH * 2 : 0);

  if (w < 7) {
    const unsigned zoff = (unsigned)(((cb * 7 + w) * 64 + 2 * rb) * 64 + l) * 16u;
    f32x4 sp[4];
#pragma unroll
    for (int i = 0; i < 4; ++i) sp[i] = *(const f32x4*)(stats + (size_t)(t0 + l) * 8 + 2 * i);
    const u32x4 zr0 = *(const u32x4*)((const char*)z2R + zoff), zr1 = *(const u32x4*)((const char*)z2R + zoff + 1024);
    f32x2* mrs = (f32x2*)(smem + XW_LDS + w * 512);
    float sum = 0.f, m2 = 0.f;
#pragma unroll
    for (int i = 0; i < 4; ++i) { sum += sp[i][0] + sp[i][2]; m2 += sp[i][1] + sp[i][3]; }
    const float mean = sum * (1.0f / 896.0f);
#pragma unroll
    for (int i = 0; i < 4; ++i) {
      const float d0 = sp[i][0] * (1.0f / 112.0f) - mean, d1 = sp[i][2] * (1.0f / 112.0f) - mean;
      m2 += 112.0f * (d0 * d0 + d1 * d1);
    }
    const float var = bfr(m2 * (1.0f / 896.0f));
    const float den = bfr(sqrtf(bfr(var + 1e-5f)));
    mrs[l] = f32x2{bfr(mean), 1.0f / den};
#pragma unroll
    for (int hh = 0; hh < 2; ++hh) {
      const u32x4 u = hh ? zr1 : zr0;
      u32x4 v;
#pragma unroll
      for (int e = 0; e < 4; ++e) {
        const f32x4 mr = *(const f32x4*)(mrs + 32 * hh + 8 * g + 2 * e);
        v[e] = pack2(bfr(bf_lo(u[e]) - mr[0]) * mr[1], bfr(bf_hi(u[e]) - mr[2]) * mr[3]);
      }
      __builtin_amdgcn_raw_buffer_store_b128(v, rsN, zoff + 1024u * hh, 0, 16);
    }
  } else {
    const unsigned dst = __builtin_amdgcn_readfirstlane((unsigned)(size_t)(smem + XW_LDS + 8 * 512));
#pragma unroll
    for (int i = 0; i < 16; ++i) {
      unsigned keep;
      asm volatile("s_mov_b32 %0, m0\n\ts_mov_b32 m0, %2\n\ts_nop 0\n\tglobal_load_lds_dwordx4 %1, off\n\ts_mov_b32 m0, %0"
                   : "=&s"(keep) : "v"(w_sp + ((size_t)bid * 2048 + 1024 + i * 64 + l) * 8), "s"(dst) : "memory");
    }
  }
  STAMP(1, 2);

  const unsigned boff = (unsigned)(((cb * 7 + 2 * fg) * 64 + kg) * 64 + l) * 16u;
  u32x4 bst[4][4];
#define K2_LOAD_B(slot, st)                                                                       \
  {                                                                                               \
    const int sk_ = ((st) + koff) & 15;                                                           \
    bst[slot][0] = __builtin_amdgcn_raw_buffer_load_b128(rsN, boff, 4096 * sk_, 16);              \
    bst[slot][1] = __builtin_amdgcn_raw_buffer_load_b128(rsN1, boff + 65536, 4096 * sk_, 16);     \
    bst[slot][2] = __builtin_amdgcn_raw_buffer_load_b128(rsN, boff + 2048, 4096 * sk_, 16);       \
    bst[slot][3] = __builtin_amdgcn_raw_buffer_load_b128(rsN1, boff + 65536 + 2048, 4096 * sk_, 16); \
  }
  u32x2 z1p[2][2];
  {
    const XwAddr<false> ad(l, fg, kg);
    f32x4 za[2][2];
#pragma unroll
    for (int i = 0; i < 2; ++i)
#pragma unroll
      for (int j = 0; j < 2; ++j) za[i][j] = f32x4{0.f, 0.f, 0.f, 0.f};
    BAR(); STAMP(1, 9); xw_compute<false, 0, 4>(ad, smem, za);
    if (w < 7) {
      WAITV(0);
      if (l == 0) __hip_atomic_fetch_add((gu32*)flags + (cb * 32 + rb) * 64, 1u, RLX_AGENT);
    }
    BAR(); STAMP(1, 10); xw_compute<false, 4, 4>(ad, smem, za);
    if (w == 0) {
      const gu32* fp = (const gu32*)flags + (cb * 32 + (l & 31)) * 64;
      for (unsigned spins = 0; spins < (1u << 22); ++spins) {
        if (__all(__hip_atomic_load(fp, RLX_AGENT) == 7u)) break;
        __builtin_amdgcn_s_sleep(1);
      }
    }
    BAR();
    STAMP(1, 11);
    K2_LOAD_B(0, 0) K2_LOAD_B(1, 1)
    xw_compute<false, 8, 4>(ad, smem, za);
    BAR();
    STAMP(1, 3);
    K2_LOAD_B(2, 2)
    xw_compute<false, 12, 2>(ad, smem, za);
#pragma unroll
    for (int i = 0; i < 2; ++i)
#pragma unroll
      for (int j = 0; j < 2; ++j) z1p[i][j] = u32x2{gelu_pair(za[i][j][0], za[i][j][1]), gelu_pair(za[i][j][2], za[i][j][3])};
  }
  STAMP(1, 4);

  int ard[4];
#pragma unroll
  for (int i = 0; i < 4; ++i) ard[i] = (16 * ((i + 2 * kg) & 3) + c16) * 256 + (((4 * kg + g) ^ c16) * 16);
  f32x4 acc[4][2];
#pragma unroll
  for (int i = 0; i < 4; ++i)
#pragma unroll
    for (int j = 0; j < 2; ++j) acc[i][j] = f32x4{0.f, 0.f, 0.f, 0.f};

  bf16x8 af[2][4];
#define K2_READ_A(u, h)                                                                                  \
  {                                                                                                      \
    const char* ab = smem + k2_slot(u);                                                                  \
    _Pragma("unroll") for (int i = 0; i < 4; ++i) af[h][i] = *(const bf16x8*)(ab + (ard[i] ^ (128 * (h)))); \
    __builtin_amdgcn_sched_barrier(0);                                                                   \
  }
#define K2_MMA(u, h)                                                                                     \
  {                                                                                                      \
    _Pragma("unroll") for (int j = 0; j < 2; ++j) {                                                      \
      const bf16x8 zf = __builtin_bit_cast(bf16x8, bst[u][2 * (h) + j]);                                 \
      _Pragma("unroll") for (int i = 0; i < 4; ++i)                                                      \
        acc[i][j] = __builtin_amdgcn_mfma_f32_16x16x32_bf16(zf, af[h][i], acc[i][j], 0, 0, 0);           \
    }                                                                                                    \
    __builtin_amdgcn_sched_barrier(0);                                                                   \
  }
#define K2_ITER(u, it, PF)                           \
  {                                                  \
    BAR();                                           \
    K2_READ_A(u, 0)                                  \
    K2_MMA(((u) + 3) & 3, 1)                         \
    K2_READ_A(u, 1)                                  \
    if (PF) { K2_LOAD_B(((u) + 3) & 3, (it) + 3) }   \
    K2_MMA(u, 0)                                     \
  }
  BAR();
  K2_READ_A(0, 0)
  K2_READ_A(0, 1)
  K2_LOAD_B(3, 3)
  K2_MMA(0, 0)
#pragma unroll 1
  for (int it = 1; it < 13; it += 4) {
    K2_ITER(1, it, true) K2_ITER(2, it + 1, true) K2_ITER(3, it + 2, true) K2_ITER(0, it + 3, true)
    STAMP(1, 5 + (it >> 2));
  }
  K2_ITER(1, 13, false)
  int lz = threadIdx.x & 63;
  asm volatile("" : "+v"(lz));
  float bias[2];
#pragma unroll
  for (int i = 0; i < 2; ++i) bias[i] = bf_lo(b_sp[(rb * 4 + 2 * kg + i) * 16 + (lz & 15)]);
  K2_ITER(2, 14, false) K2_ITER(3, 15, false)
  K2_MMA(3, 1)
  STAMP(1, 8);

  char* red = smem + 32768;
  char* redw = red + ((w ^ 4) < 6 ? (w ^ 4) * 4096 : XW_XB + 57344 - 32768 + ((w ^ 4) - 6) * 4096);
  const char* redr = red + (w < 6 ? w * 4096 : XW_XB + 57344 - 32768 + (w - 6) * 4096);
#pragma unroll
  for (int i = 0; i < 2; ++i)
#pragma unroll
    for (int j = 0; j < 2; ++j) *(f32x4*)(redw + (2 * i + j) * 1024 + lz * 16) = acc[2 + i][j];
  BAR();
  STAMP(1, 12);
#pragma unroll
  for (int i = 0; i < 2; ++i) {
    u32x2 hv[2];
#pragma unroll
    for (int j = 0; j < 2; ++j) {
      const f32x4 a = acc[i][j] + *(const f32x4*)(redr + (2 * i + j) * 1024 + lz * 16);
      const float h0 = bf_lo(z1p[i][j][0]) * bfr(bfr(a[0]) + bias[i]);
      const float h1 = bf_hi(z1p[i][j][0]) * bfr(bfr(a[1]) + bias[i]);
      const float h2 = bf_lo(z1p[i][j][1]) * bfr(bfr(a[2]) + bias[i]);
      const float h3 = bf_hi(z1p[i][j][1]) * bfr(bfr(a[3]) + bias[i]);
      hv[j] = u32x2{pack2(h0, h1), pack2(h2, h3)};
    }
    const int tt = rb * 4 + 2 * kg + i;
    u16* hp = hF + ((size_t)(tt * 28 + 7 * (cb >> 1)) * 64 + lz) * 8;
    if (two) *(u32x4*)(hp + (size_t)(3 * (cb & 1) + fg) * 512) = u32x4{hv[0][0], hv[0][1], hv[1][0], hv[1][1]};
    else     *(u32x2*)(hp + 6 * 512 + 4 * (cb & 1)) = hv[0];
  }
  STAMP(1, 13); STAMPR(1, 15);
}

__global__ __launch_bounds__(768, 3) void k3_out(const u16* __restrict__ hF, const u16* __restrict__ w_out,
                                                u16* __restrict__ out) {
  __shared__ __attribute__((aligned(16))) char smem[896 * 128 + 8 * 2048];
  const int bid = blockIdx.x, xcd = bid & 7, idx = bid >> 3;
  const int rb = xcd * 4 + idx / 7, nb = idx % 7;
  const int tid = threadIdx.x, l = tid & 63, w = __builtin_amdgcn_readfirstlane(tid >> 6);
  const int n0 = nb * 64;
  if (w >= 8) {
    const int lw = w - 8, r8 = l >> 3;
    const rsrc_t rw = make_rsrc(w_out, FH * D_ * 2);
    const unsigned wo = (unsigned)((8 * lw + r8) * D_ + n0 + (((l & 7) ^ (((r8 >> 1) & 3) << 1)) * 8)) * 2u;
#pragma unroll
    for (int i = 0; i < 14; ++i) blds16(rw, smem + (4 * i + lw) * 1024, wo, i * 32 * D_ * 2);
    WAITV(7); BAR();
#pragma unroll
    for (int i = 14; i < 21; ++i) blds16(rw, smem + (4 * i + lw) * 1024, wo, i * 32 * D_ * 2);
    WAITV(7); BAR();
#pragma unroll
    for (int i = 21; i < 28; ++i) blds16(rw, smem + (4 * i + lw) * 1024, wo, i * 32 * D_ * 2);
    WAITV(7); BAR();
    WAITV(0); BAR();
    return;
  }
  const int tq = w & 3, kg = w >> 2, g = l >> 4, c16 = l & 15, q = c16 >> 2, p = l & 3;
  const int tt = rb * 4 + tq;
  STAMPR(2, 0); STAMP(2, 1);
  bf16x8 hf[4][4];
  const u16* hp = hF + ((size_t)tt * 28 * 64 + l) * 8;
#define K3_LOAD_H(pc) \
  _Pragma("unroll") for (int i = 0; i < 4; ++i) hf[pc][i] = *(const bf16x8*)(hp + (7 * (pc) + min(2 * i + (((pc) + kg) & 1), 6)) * 512);
  K3_LOAD_H(0) K3_LOAD_H(1)
  const int rrow = 4 * g + q;
  int rb4[4];
#pragma unroll
  for (int j = 0; j < 4; ++j) rb4[j] = rrow * 128 + ((j ^ ((rrow >> 1) & 3)) * 32) + 8 * p;
  f32x4 acc[4];
#pragma unroll
  for (int j = 0; j < 4; ++j) acc[j] = f32x4{0.f, 0.f, 0.f, 0.f};
  STAMP(2, 2);
#pragma unroll
  for (int pc = 0; pc < 4; ++pc) {
    BAR();
    STAMP(2, 3 + pc);
    if (pc < 2) { K3_LOAD_H(pc + 2) }
    const int par = (pc + kg) & 1;
#pragma unroll
    for (int i = 0; i < 4; ++i) {
      if (i < 3 || par == 0) {
        const int e = 2 * i + par;
        const int tA = (e < 6) ? (14 * pc + 7 * (e / 3) + 2 * (e % 3)) : (14 * pc + 6);
        const int tB = (e < 6) ? (tA + 1) : (14 * pc + 13);
#pragma unroll
        for (int j = 0; j < 4; ++j) {
          const bf16x8 wf = tr_frag(smem + rb4[j] + tA * 2048, smem + rb4[j] + tB * 2048);
          acc[j] = __builtin_amdgcn_mfma_f32_16x16x32_bf16(wf, hf[pc][i], acc[j], 0, 0, 0);
        }
      }
    }
  }
  STAMP(2, 7);
  char* red = smem + 896 * 128;
  if (kg == 0) {
    *(f32x4*)(red + (w ^ 4) * 2048 + l * 16) = acc[2];
    *(f32x4*)(red + (w ^ 4) * 2048 + 1024 + l * 16) = acc[3];
  } else {
    *(f32x4*)(red + (w ^ 4) * 2048 + l * 16) = acc[0];
    *(f32x4*)(red + (w ^ 4) * 2048 + 1024 + l * 16) = acc[1];
  }
  BAR();
  const f32x4 a0 = (kg ? acc[2] : acc[0]) + *(const f32x4*)(red + w * 2048 + l * 16);
  const f32x4 a1 = (kg ? acc[3] : acc[1]) + *(const f32x4*)(red + w * 2048 + 1024 + l * 16);
  u16* op = out + (size_t)(tt * 16 + c16) * D_ + n0 + 32 * kg + 4 * g;
  *(u32x2*)op = u32x2{pack2(a0[0], a0[1]), pack2(a0[2], a0[3])};
  *(u32x2*)(op + 16) = u32x2{pack2(a1[0], a1[1]), pack2(a1[2], a1[3])};
  STAMP(2, 8); STAMPR(2, 15);
}

extern "C" void kernel_launch(void* const* d_in, const int* in_sizes, int n_in, void* d_out, int out_size,
                              void* d_ws, size_t ws_size, hipStream_t stream) {
  const u16* x = (const u16*)d_in[0];
  const u16* w_in = (const u16*)d_in[1];
  const u16* w_sp = (const u16*)d_in[2];
  const u16* b_sp = (const u16*)d_in[3];
  const u16* w_out = (const u16*)d_in[4];
  char* ws = (char*)d_ws;
  u16* z2R = (u16*)ws;
  u16* z2N = (u16*)(ws + (size_t)4 * 1024 * 1024);
  u16* hF = (u16*)(ws + (size_t)8 * 1024 * 1024);
  f32x2* stats = (f32x2*)(ws + (size_t)12 * 1024 * 1024);
  unsigned* flags = (unsigned*)(ws + (size_t)13 * 1024 * 1024);
  k1_z2<<<256, 768, 0, stream>>>(x, w_in, z2R, stats, flags, w_sp, w_out);
  k2_spatial<<<256, 768, 0, stream>>>(x, w_in, w_sp, z2R, stats, b_sp, z2N, flags, hF);
  k3_out<<<224, 768, 0, stream>>>(hF, w_out, (u16*)d_out);
}
```

```cpp
#include <hip/hip_runtime.h>
#include <hip/hip_bf16.h>

typedef __bf16 bf16x8 __attribute__((ext_vector_type(8)));
typedef __bf16 bf16x4 __attribute__((ext_vector_type(4)));
typedef __bf16 bf16x2 __attribute__((ext_vector_type(2)));
typedef float f32x4 __attribute__((ext_vector_type(4)));
typedef float f32x2 __attribute__((ext_vector_type(2)));
typedef unsigned u32x4 __attribute__((ext_vector_type(4)));
typedef unsigned u32x2 __attribute__((ext_vector_type(2)));
typedef unsigned short u16;
#define LAS __attribute__((address_space(3)))
#define DI __device__ __forceinline__

#define STAMP(k, i)
#define STAMPR(k, i)
constexpr int S_ = 2048, D_ = 448, F_ = 1792, FH = 896;

DI float bf_lo(unsigned u) { return __builtin_bit_cast(float, u << 16); }
DI float bf_hi(unsigned u) { return __builtin_bit_cast(float, u & 0xffff0000u); }
DI unsigned pack2(float a, float b) {
  f32x2 f = {a, b};
  return __builtin_bit_cast(unsigned, __builtin_convertvector(f, bf16x2));
}
DI float bfr(float a) { return (float)(__bf16)a; }
DI float gelu_tanh(float z) {
  const float y = z * __builtin_fmaf(z * z, -0.10294324f, -2.3022082f);
  const float e = __builtin_amdgcn_exp2f(y);
  return z * __builtin_amdgcn_rcpf(1.0f + e);
}
DI bf16x8 tr_frag(const char* a0, const char* a1) {
  bf16x4 lo = __builtin_amdgcn_ds_read_tr16_b64_v4bf16((LAS bf16x4*)a0);
  bf16x4 hi = __builtin_amdgcn_ds_read_tr16_b64_v4bf16((LAS bf16x4*)a1);
  return __builtin_shufflevector(lo, hi, 0, 1, 2, 3, 4, 5, 6, 7);
}
template <int CTRL> DI float dpp_mov(float x) {
  return __builtin_bit_cast(float, __builtin_amdgcn_update_dpp(0, __builtin_bit_cast(int, x), CTRL, 0xf, 0xf, true));
}
DI float sum16(float x) {
  x += dpp_mov<0xB1>(x);
  x += dpp_mov<0x4E>(x);
  x += dpp_mov<0x141>(x);
  x += dpp_mov<0x140>(x);
  return x;
}

#define BAR()                                              \
  do {                                                     \
    asm volatile("s_waitcnt lgkmcnt(0)" ::: "memory");     \
    __builtin_amdgcn_s_barrier();                          \
    asm volatile("" ::: "memory");                         \
  } while (0)
#define WAITV(n) asm volatile("s_waitcnt vmcnt(" #n ")" ::: "memory")
typedef __amdgpu_buffer_rsrc_t rsrc_t;
DI rsrc_t make_rsrc(const void* p, unsigned bytes) { return __builtin_amdgcn_make_buffer_rsrc((void*)p, 0, bytes, 0x00020000); }
DI void blds16(rsrc_t rs, char* lds_wave_base, unsigned voff, unsigned soff) {
  __builtin_amdgcn_raw_ptr_buffer_load_lds(rs, (LAS void*)lds_wave_base, 16, voff, soff, 0, 0);
}

constexpr int XW_XB = 7 * 8192, XW_WB = 448 * 224, XW_LDS = XW_XB + XW_WB;

DI int x_swz(int row) { return ((row >> 1) & 1) | (((row >> 3) & 1) << 1) | ((((row >> 2) ^ (row >> 4)) & 1) << 2); }

struct XwLoader {
  rsrc_t rx, rw;
  unsigned xo[4], wo[7];
  int lw;
  DI XwLoader(const u16* x, const u16* w_in, int rb, int n0, int lw_, int l) : lw(lw_) {
    rx = make_rsrc(x, S_ * D_ * 2);
    rw = make_rsrc(w_in, D_ * F_ * 2);
#pragma unroll
    for (int i = 0; i < 4; ++i) {
      const int pc = 4 * i + lw, row = 8 * (pc & 7) + (l >> 3);
      xo[i] = (unsigned)((rb * 64 + row) * D_ + (pc >> 3) * 64 + (((l & 7) ^ x_swz(row)) * 8)) * 2u;
    }
#pragma unroll
    for (int i = 0; i < 7; ++i) {
      const int c = (4 * i + lw) * 64 + l, row = c / 14, cc = c - row * 14;
      wo[i] = (unsigned)(row * F_ + n0 + cc * 8) * 2u;
    }
  }
  DI void stage_x(char* smem, int s) const {
#pragma unroll
    for (int i = 0; i < (s < 3 ? 4 : 2); ++i) blds16(rx, smem + 2 * s * 8192 + (4 * i + lw) * 1024, xo[i], 256 * s);
  }
  DI void stage_w(char* smem, int s, int col0) const {
#pragma unroll
    for (int i = 0; i < (s < 3 ? 7 : 4); ++i)
      if (s < 3 || 4 * i + lw < 14) blds16(rw, smem + XW_XB + (28 * s + 4 * i + lw) * 1024, wo[i], 128 * F_ * 2 * s + col0 * 2);
  }
};
template <bool Z2> struct XwAddr {
  int xa[2], wa[2];
  DI XwAddr(int l, int fg, int mh) {
    const int g = l >> 4, c16 = l & 15, q = c16 >> 2, p = l & 3;
#pragma unroll
    for (int rt = 0; rt < 2; ++rt) {
      const int row = 32 * mh + (Z2 ? (8 * q + 4 * rt + p) : (16 * rt + c16));
      xa[rt] = row * 128 + (((g >> 1) ^ x_swz(row)) * 16) + (g & 1) * 8;
    }
#pragma unroll
    for (int jj = 0; jj < 2; ++jj) wa[jj] = XW_XB + (4 * g + q) * 224 + min(2 * fg + jj, 6) * 32 + 8 * p;
  }
};
template <int OFF> DI u32x2 lds_read64(unsigned addr) {
  u32x2 r;
  asm volatile("ds_read_b64 %0, %1 offset:%2" : "=v"(r) : "v"(addr), "i"(OFF) : "memory");
  return r;
}
template <bool Z2, int KS0, int NKS>
DI void xw_compute(const XwAddr<Z2>& ad, const char* smem, f32x4 (&acc)[2][2]) {
  u32x2 xl[NKS][2], xh[NKS][2];
  bf16x8 wf[NKS][2];
  const unsigned sb = (unsigned)(size_t)smem;
#pragma unroll
  for (int k = 0; k < NKS; ++k) {
    constexpr int dummy = 0; (void)dummy;
#pragma unroll
    for (int rt = 0; rt < 2; ++rt) {
      const unsigned o = sb + (ad.xa[rt] ^ (64 * ((KS0 + k) & 1)));
      if (k < 2) { xl[k][rt] = lds_read64<8192 * (KS0 >> 1)>(o); xh[k][rt] = lds_read64<8192 * (KS0 >> 1)>(o ^ 32); }
      else       { xl[k][rt] = lds_read64<8192 * (KS0 >> 1) + 8192>(o); xh[k][rt] = lds_read64<8192 * (KS0 >> 1) + 8192>(o ^ 32); }
    }
#pragma unroll
    for (int jj = 0; jj < 2; ++jj) {
      const char* a = smem + ad.wa[jj] + 32 * (KS0 + k) * 224;
      wf[k][jj] = tr_frag(a, a + 16 * 224);
    }
  }
  asm volatile("s_waitcnt lgkmcnt(0)" ::: "memory");
  __builtin_amdgcn_sched_barrier(0);
#pragma unroll
  for (int k = 0; k < NKS; ++k)
#pragma unroll
    for (int rt = 0; rt < 2; ++rt) {
      const bf16x8 xf = __builtin_bit_cast(bf16x8, u32x4{xl[k][rt][0], xl[k][rt][1], xh[k][rt][0], xh[k][rt][1]});
#pragma unroll
      for (int jj = 0; jj < 2; ++jj) {
        if (Z2) acc[rt][jj] = __builtin_amdgcn_mfma_f32_16x16x32_bf16(xf, wf[k][jj], acc[rt][jj], 0, 0, 0);
        else    acc[rt][jj] = __builtin_amdgcn_mfma_f32_16x16x32_bf16(wf[k][jj], xf, acc[rt][jj], 0, 0, 0);
      }
    }
}
DI unsigned gelu_pair(float a, float b) {
  const unsigned u = pack2(a, b);
  const f32x2 z = {bf_lo(u), bf_hi(u)};
  const f32x2 y = z * __builtin_elementwise_fma(z * z, f32x2{-0.10294324f, -0.10294324f}, f32x2{-2.3022082f, -2.3022082f});
  const f32x2 d = f32x2{__builtin_amdgcn_exp2f(y[0]), __builtin_amdgcn_exp2f(y[1])} + f32x2{1.0f, 1.0f};
  const f32x2 o = z * f32x2{__builtin_amdgcn_rcpf(d[0]), __builtin_amdgcn_rcpf(d[1])};
  return pack2(o[0], o[1]);
}

DI void tile_of_block(int bid, int& rb, int& cb) {
  const int xcd = bid & 7, idx = bid >> 3;
  rb = (xcd >> 1) * 8 + (idx & 7);
  cb = (xcd & 1) * 4 + (idx >> 3);
}

__global__ __launch_bounds__(768, 3) void k1_gemm(const u16* __restrict__ x, const u16* __restrict__ w_in,
                                                 u16* __restrict__ z2R, u16* __restrict__ z1F, f32x2* __restrict__ stats,
                                                 unsigned* __restrict__ flags, const u16* __restrict__ w_sp,
                                                 const u16* __restrict__ w_out) {
  __shared__ __attribute__((aligned(16))) char smem[XW_LDS + 4 * 64 * 8];
  const int bid = blockIdx.x;
  int rb, cb;
  tile_of_block(bid, rb, cb);
  const int tid = threadIdx.x, l = tid & 63, w = __builtin_amdgcn_readfirstlane(tid >> 6);
  if (w >= 8) {
    const int lw = w - 8;
    const XwLoader ld(x, w_in, rb, cb * 112, lw, l);
    u32x4 pf[9];
    ld.stage_x(smem, 0); ld.stage_w(smem, 0, FH);
    ld.stage_x(smem, 1); ld.stage_w(smem, 1, FH);
    WAITV(11); BAR();
    ld.stage_x(smem, 2); ld.stage_w(smem, 2, FH);
    ld.stage_x(smem, 3); ld.stage_w(smem, 3, FH);
    WAITV(16); BAR();
    ld.stage_w(smem, 0, 0);
    WAITV(12); BAR();
    ld.stage_w(smem, 1, 0);
    asm volatile("" ::: "memory");
#pragma unroll
    for (int i = 0; i < 4; ++i) pf[i] = *(const u32x4*)(w_sp + ((size_t)bid * 2048 + i * 256 + lw * 64 + l) * 8);
    WAITV(18); BAR();
    ld.stage_w(smem, 2, 0);
    WAITV(18); BAR();
    ld.stage_w(smem, 3, 0);
    asm volatile("" ::: "memory");
#pragma unroll
    for (int i = 4; i < 8; ++i) pf[i] = *(const u32x4*)(w_sp + ((size_t)bid * 2048 + i * 256 + lw * 64 + l) * 8);
    pf[8] = *(const u32x4*)(w_out + (size_t)(bid * 196 + min(lw * 64 + l, 195)) * 8);
    WAITV(19); BAR();
    WAITV(8); BAR();
    WAITV(5); BAR();
#pragma unroll
    for (int i = 0; i < 9; ++i) asm volatile("" ::"v"(pf[i]));
    return;
  }
  const int fg = w & 3, mh = w >> 2, g = l >> 4, c16 = l & 15;
  const bool two = fg < 3;
  STAMPR(0, 0); STAMP(0, 1);
  if (tid == 0) flags[bid * 64] = 0u;
  f32x2* sst = (f32x2*)(smem + XW_LDS);
  {
    const XwAddr<true> ad(l, fg, mh);
    f32x4 acc[2][2];
#pragma unroll
    for (int rt = 0; rt < 2; ++rt)
#pragma unroll
      for (int jj = 0; jj < 2; ++jj) acc[rt][jj] = f32x4{0.f, 0.f, 0.f, 0.f};
    BAR(); STAMP(0, 2); xw_compute<true, 0, 4>(ad, smem, acc);
    BAR(); STAMP(0, 3); xw_compute<true, 4, 4>(ad, smem, acc);
    BAR(); STAMP(0, 4); xw_compute<true, 8, 4>(ad, smem, acc);
    BAR(); STAMP(0, 5); xw_compute<true, 12, 2>(ad, smem, acc);
    u32x4 zp[2];
#pragma unroll
    for (int jj = 0; jj < 2; ++jj)
      zp[jj] = u32x4{gelu_pair(acc[0][jj][0], acc[0][jj][1]), gelu_pair(acc[0][jj][2], acc[0][jj][3]),
                     gelu_pair(acc[1][jj][0], acc[1][jj][1]), gelu_pair(acc[1][jj][2], acc[1][jj][3])};
    const int kstep = rb * 2 + mh;
#pragma unroll
    for (int jj = 0; jj < 2; ++jj)
      if (jj == 0 || two) *(u32x4*)(z2R + ((size_t)((cb * 7 + 2 * fg + jj) * 64 + kstep) * 64 + l) * 8) = zp[jj];
    if (!two) zp[1] = u32x4{0u, 0u, 0u, 0u};
    float v[16];
#pragma unroll
    for (int e = 0; e < 4; ++e) {
      const f32x2 a0 = {bf_lo(zp[0][e]), bf_hi(zp[0][e])}, a1 = {bf_lo(zp[1][e]), bf_hi(zp[1][e])};
      const f32x2 s1 = a0 + a1, s2 = __builtin_elementwise_fma(a0, a0, a1 * a1);
      v[4 * e] = s1[0]; v[4 * e + 1] = s2[0]; v[4 * e + 2] = s1[1]; v[4 * e + 3] = s2[1];
    }
#pragma unroll
    for (int i = 0; i < 8; ++i) { const bool b = c16 & 1; v[i] = (b ? v[i + 8] : v[i]) + dpp_mov<0xB1>(b ? v[i] : v[i + 8]); }
#pragma unroll
    for (int i = 0; i < 4; ++i) { const bool b = c16 & 2; v[i] = (b ? v[i + 4] : v[i]) + dpp_mov<0x4E>(b ? v[i] : v[i + 4]); }
#pragma unroll
    for (int i = 0; i < 2; ++i) { const bool b = c16 & 4; v[i] = (b ? v[i + 2] : v[i]) + dpp_mov<0x124>(b ? v[i] : v[i + 2]); }
    { const bool b = c16 & 8; v[0] = (b ? v[1] : v[0]) + dpp_mov<0x128>(b ? v[0] : v[1]); }
    const int idx = ((c16 & 1) << 3) | ((c16 & 2) << 1) | ((c16 & 4) >> 1) | (c16 >> 3);
    ((float*)(sst + fg * 64 + 32 * mh + 8 * g))[idx] = v[0];
  }
  STAMP(0, 6);
  {
    const XwAddr<false> ad(l, fg, mh);
    f32x4 acc[2][2];
#pragma unroll
    for (int rt = 0; rt < 2; ++rt)
#pragma unroll
      for (int jj = 0; jj < 2; ++jj) acc[rt][jj] = f32x4{0.f, 0.f, 0.f, 0.f};
    BAR(); STAMP(0, 7);
    if (w == 7) {
      const f32x2 t = sst[l] + sst[64 + l] + sst[128 + l] + sst[192 + l];
      stats[(size_t)(rb * 64 + l) * 8 + cb] = f32x2{t[0], fmaxf(t[1] - t[0] * t[0] * (1.0f / 112.0f), 0.f)};
    }
    xw_compute<false, 0, 4>(ad, smem, acc);
    BAR(); STAMP(0, 8); xw_compute<false, 4, 4>(ad, smem, acc);
    BAR(); STAMP(0, 9); xw_compute<false, 8, 4>(ad, smem, acc);
    BAR(); STAMP(0, 10); xw_compute<false, 12, 2>(ad, smem, acc);
#pragma unroll
    for (int rt = 0; rt < 2; ++rt)
#pragma unroll
      for (int jj = 0; jj < 2; ++jj)
        if (jj == 0 || two) {
          const u32x2 v = {gelu_pair(acc[rt][jj][0], acc[rt][jj][1]), gelu_pair(acc[rt][jj][2], acc[rt][jj][3])};
          *(u32x2*)(z1F + ((size_t)((rb * 4 + 2 * mh + rt) * 56 + cb * 7 + 2 * fg + jj) * 64 + l) * 4) = v;
        }
  }
  STAMP(0, 11); STAMPR(0, 15);
}

typedef __attribute__((address_space(1))) unsigned gu32;
#define RLX_AGENT __ATOMIC_RELAXED, __HIP_MEMORY_SCOPE_AGENT
constexpr int K2_RING = 9;

__global__ __launch_bounds__(768, 3) void k2_spatial(const u16* __restrict__ w_sp, const u16* __restrict__ z2R,
                                                    const f32x2* __restrict__ stats, const u16* __restrict__ b_sp,
                                                    const u16* __restrict__ z1F, u16* z2N, unsigned* flags,
                                                    u16* __restrict__ hF) {
  __shared__ __attribute__((aligned(16))) char smem[K2_RING * 16384 + 8 * 512];
  const int bid = blockIdx.x;
  int rb, cb;
  tile_of_block(bid, rb, cb);
  const int tid = threadIdx.x, l = tid & 63, w = __builtin_amdgcn_readfirstlane(tid >> 6);
  const int t0 = rb * 64;
  const int koff = (2 * (rb & 7) + (cb & 1)) & 15;

  if (w >= 8) {
    const int lw = w - 8;
    const rsrc_t rsA = make_rsrc(w_sp, S_ * S_ * 2);
    unsigned ap[4];
#pragma unroll
    for (int i = 0; i < 4; ++i) {
      const int row = 4 * (4 * i + lw) + (l >> 4);
      ap[i] = (unsigned)((t0 + row) * S_ + (((l & 15) ^ (row & 15)) * 8)) * 2u;
    }
#define K2_LOAD_W(st)                                                                             \
  {                                                                                               \
    const int sk_ = ((st) + koff) & 15;                                                           \
    char* dst_ = smem + ((st) % K2_RING) * 16384 + lw * 1024;                                     \
    _Pragma("unroll") for (int i = 0; i < 4; ++i) blds16(rsA, dst_ + i * 4096, ap[i], 256 * sk_); \
  }
#pragma unroll
    for (int st = 0; st < 9; ++st) K2_LOAD_W(st)
    WAITV(32); BAR();
    WAITV(28); BAR();
#pragma unroll
    for (int st = 1; st < 8; ++st) {
      K2_LOAD_W(st + 8)
      WAITV(28); BAR();
    }
    WAITV(24); BAR(); WAITV(20); BAR(); WAITV(16); BAR(); WAITV(12); BAR();
    WAITV(8); BAR(); WAITV(4); BAR(); WAITV(0); BAR();
    return;
  }

  const int fg = w & 3, kg = w >> 2, g = l >> 4, c16 = l & 15;
  const bool two = fg < 3;
  STAMPR(1, 0); STAMP(1, 1);
  const rsrc_t rsN = make_rsrc(z2N, S_ * FH * 2);
  const rsrc_t rsN1 = make_rsrc(z2N, two ? S_ * FH * 2 : 0);

  if (w < 7) {
    const unsigned zoff = (unsigned)(((cb * 7 + w) * 64 + 2 * rb) * 64 + l) * 16u;
    f32x4 sp[4];
#pragma unroll
    for (int i = 0; i < 4; ++i) sp[i] = *(const f32x4*)(stats + (size_t)(t0 + l) * 8 + 2 * i);
    const u32x4 zr0 = *(const u32x4*)((const char*)z2R + zoff), zr1 = *(const u32x4*)((const char*)z2R + zoff + 1024);
    f32x2* mrs = (f32x2*)(smem + K2_RING * 16384 + w * 512);
    float sum = 0.f, m2 = 0.f;
#pragma unroll
    for (int i = 0; i < 4; ++i) { sum += sp[i][0] + sp[i][2]; m2 += sp[i][1] + sp[i][3]; }
    const float mean = sum * (1.0f / 896.0f);
#pragma unroll
    for (int i = 0; i < 4; ++i) {
      const float d0 = sp[i][0] * (1.0f / 112.0f) - mean, d1 = sp[i][2] * (1.0f / 112.0f) - mean;
      m2 += 112.0f * (d0 * d0 + d1 * d1);
    }
    const float var = bfr(m2 * (1.0f / 896.0f));
    const float den = bfr(sqrtf(bfr(var + 1e-5f)));
    mrs[l] = f32x2{bfr(mean), 1.0f / den};
#pragma unroll
    for (int hh = 0; hh < 2; ++hh) {
      const u32x4 u = hh ? zr1 : zr0;
      u32x4 v;
#pragma unroll
      for (int e = 0; e < 4; ++e) {
        const f32x4 mr = *(const f32x4*)(mrs + 32 * hh + 8 * g + 2 * e);
        v[e] = pack2(bfr(bf_lo(u[e]) - mr[0]) * mr[1], bfr(bf_hi(u[e]) - mr[2]) * mr[3]);
      }
      __builtin_amdgcn_raw_buffer_store_b128(v, rsN, zoff + 1024u * hh, 0, 16);
    }
    WAITV(0);
    if (l == 0) __hip_atomic_fetch_add((gu32*)flags + (cb * 32 + rb) * 64, 1u, RLX_AGENT);
  } else {
    const gu32* fp = (const gu32*)flags + (cb * 32 + (l & 31)) * 64;
    for (unsigned spins = 0; spins < (1u << 22); ++spins) {
      if (__all(__hip_atomic_load(fp, RLX_AGENT) == 7u)) break;
      __builtin_amdgcn_s_sleep(2);
    }
  }
  STAMP(1, 2);
  u32x2 z1p[2][2];
  float bias[2];
#pragma unroll
  for (int i = 0; i < 2; ++i) {
    const int tt = rb * 4 + 2 * kg + i;
#pragma unroll
    for (int j = 0; j < 2; ++j) z1p[i][j] = *(const u32x2*)(z1F + ((size_t)(tt * 56 + cb * 7 + min(2 * fg + j, 6)) * 64 + l) * 4);
    bias[i] = bf_lo(b_sp[tt * 16 + c16]);
  }

  const unsigned boff = (unsigned)(((cb * 7 + 2 * fg) * 64 + kg) * 64 + l) * 16u;
  u32x4 bst[4][4];
#define K2_LOAD_B(slot, st)                                                                       \
  {                                                                                               \
    const int sk_ = ((st) + koff) & 15;                                                           \
    bst[slot][0] = __builtin_amdgcn_raw_buffer_load_b128(rsN, boff, 4096 * sk_, 16);              \
    bst[slot][1] = __builtin_amdgcn_raw_buffer_load_b128(rsN1, boff + 65536, 4096 * sk_, 16);     \
    bst[slot][2] = __builtin_amdgcn_raw_buffer_load_b128(rsN, boff + 2048, 4096 * sk_, 16);       \
    bst[slot][3] = __builtin_amdgcn_raw_buffer_load_b128(rsN1, boff + 65536 + 2048, 4096 * sk_, 16); \
  }
  int ard[4];
#pragma unroll
  for (int i = 0; i < 4; ++i) ard[i] = (16 * ((i + 2 * kg) & 3) + c16) * 256 + (((4 * kg + g) ^ c16) * 16);
  f32x4 acc[4][2];
#pragma unroll
  for (int i = 0; i < 4; ++i)
#pragma unroll
    for (int j = 0; j < 2; ++j) acc[i][j] = f32x4{0.f, 0.f, 0.f, 0.f};
  bf16x8 af[2][4];
#define K2_READ_A(st, h)                                                                                 \
  {                                                                                                      \
    const char* ab = smem + ((st) % K2_RING) * 16384;                                                    \
    _Pragma("unroll") for (int i = 0; i < 4; ++i) af[h][i] = *(const bf16x8*)(ab + (ard[i] ^ (128 * (h)))); \
    __builtin_amdgcn_sched_barrier(0);                                                                   \
  }
#define K2_MMA(u, h)                                                                                     \
  {                                                                                                      \
    _Pragma("unroll") for (int j = 0; j < 2; ++j) {                                                      \
      const bf16x8 zf = __builtin_bit_cast(bf16x8, bst[u][2 * (h) + j]);                                 \
      _Pragma("unroll") for (int i = 0; i < 4; ++i)                                                      \
        acc[i][j] = __builtin_amdgcn_mfma_f32_16x16x32_bf16(zf, af[h][i], acc[i][j], 0, 0, 0);           \
    }                                                                                                    \
    __builtin_amdgcn_sched_barrier(0);                                                                   \
  }
#define K2_ITER(it)                                                \
  {                                                                \
    BAR();                                                         \
    K2_READ_A(it, 0)                                               \
    K2_MMA(((it) + 3) & 3, 1)                                      \
    K2_READ_A(it, 1)                                               \
    if ((it) + 3 < 16) { K2_LOAD_B(((it) + 3) & 3, (it) + 3) }     \
    K2_MMA((it) & 3, 0)                                            \
  }
  BAR();
  STAMP(1, 3);
  K2_LOAD_B(0, 0) K2_LOAD_B(1, 1) K2_LOAD_B(2, 2) K2_LOAD_B(3, 3)
  K2_READ_A(0, 0)
  K2_READ_A(0, 1)
  K2_MMA(0, 0)
  STAMP(1, 4);
  K2_ITER(1) K2_ITER(2) K2_ITER(3) K2_ITER(4)
  STAMP(1, 5);
  K2_ITER(5) K2_ITER(6) K2_ITER(7) K2_ITER(8)
  STAMP(1, 6);
  K2_ITER(9) K2_ITER(10) K2_ITER(11) K2_ITER(12)
  STAMP(1, 7);
  K2_ITER(13) K2_ITER(14) K2_ITER(15)
  K2_MMA(3, 1)
  STAMP(1, 8);

  char* red = smem;
#pragma unroll
  for (int i = 0; i < 2; ++i)
#pragma unroll
    for (int j = 0; j < 2; ++j) *(f32x4*)(red + ((w ^ 4) * 4 + 2 * i + j) * 1024 + l * 16) = acc[2 + i][j];
  BAR();
  STAMP(1, 12);
#pragma unroll
  for (int i = 0; i < 2; ++i) {
    u32x2 hv[2];
#pragma unroll
    for (int j = 0; j < 2; ++j) {
      const f32x4 a = acc[i][j] + *(const f32x4*)(red + (w * 4 + 2 * i + j) * 1024 + l * 16);
      const unsigned g01 = pack2(a[0], a[1]), g23 = pack2(a[2], a[3]);
      const unsigned s01 = pack2(bf_lo(g01) + bias[i], bf_hi(g01) + bias[i]);
      const unsigned s23 = pack2(bf_lo(g23) + bias[i], bf_hi(g23) + bias[i]);
      hv[j] = u32x2{pack2(bf_lo(z1p[i][j][0]) * bf_lo(s01), bf_hi(z1p[i][j][0]) * bf_hi(s01)),
                    pack2(bf_lo(z1p[i][j][1]) * bf_lo(s23), bf_hi(z1p[i][j][1]) * bf_hi(s23))};
    }
    const int tt = rb * 4 + 2 * kg + i;
    u16* hp = hF + ((size_t)(tt * 28 + 7 * (cb >> 1)) * 64 + l) * 8;
    if (two) *(u32x4*)(hp + (size_t)(3 * (cb & 1) + fg) * 512) = u32x4{hv[0][0], hv[0][1], hv[1][0], hv[1][1]};
    else     *(u32x2*)(hp + 6 * 512 + 4 * (cb & 1)) = hv[0];
  }
  STAMP(1, 13); STAMPR(1, 15);
}

__global__ __launch_bounds__(768, 3) void k3_out(const u16* __restrict__ hF, const u16* __restrict__ w_out,
                                                u16* __restrict__ out) {
  __shared__ __attribute__((aligned(16))) char smem[896 * 128 + 8 * 2048];
  const int bid = blockIdx.x, xcd = bid & 7, idx = bid >> 3;
  const int rb = xcd * 4 + idx / 7, nb = idx % 7;
  const int tid = threadIdx.x, l = tid & 63, w = __builtin_amdgcn_readfirstlane(tid >> 6);
  const int n0 = nb * 64;
  if (w >= 8) {
    const int lw = w - 8, r8 = l >> 3;
    const rsrc_t rw = make_rsrc(w_out, FH * D_ * 2);
    const unsigned wo = (unsigned)((8 * lw + r8) * D_ + n0 + (((l & 7) ^ (((r8 >> 1) & 3) << 1)) * 8)) * 2u;
#pragma unroll
    for (int i = 0; i < 14; ++i) blds16(rw, smem + (4 * i + lw) * 1024, wo, i * 32 * D_ * 2);
    WAITV(7); BAR();
#pragma unroll
    for (int i = 14; i < 21; ++i) blds16(rw, smem + (4 * i + lw) * 1024, wo, i * 32 * D_ * 2);
    WAITV(7); BAR();
#pragma unroll
    for (int i = 21; i < 28; ++i) blds16(rw, smem + (4 * i + lw) * 1024, wo, i * 32 * D_ * 2);
    WAITV(7); BAR();
    WAITV(0); BAR();
    return;
  }
  const int tq = w & 3, kg = w >> 2, g = l >> 4, c16 = l & 15, q = c16 >> 2, p = l & 3;
  const int tt = rb * 4 + tq;
  STAMPR(2, 0); STAMP(2, 1);
  bf16x8 hf[4][4];
  const u16* hp = hF + ((size_t)tt * 28 * 64 + l) * 8;
#define K3_LOAD_H(pc) \
  _Pragma("unroll") for (int i = 0; i < 4; ++i) hf[pc][i] = *(const bf16x8*)(hp + (7 * (pc) + min(2 * i + (((pc) + kg) & 1), 6)) * 512);
  K3_LOAD_H(0) K3_LOAD_H(1)
  const int rrow = 4 * g + q;
  int rb4[4];
#pragma unroll
  for (int j = 0; j < 4; ++j) rb4[j] = rrow * 128 + ((j ^ ((rrow >> 1) & 3)) * 32) + 8 * p;
  f32x4 acc[4];
#pragma unroll
  for (int j = 0; j < 4; ++j) acc[j] = f32x4{0.f, 0.f, 0.f, 0.f};
  STAMP(2, 2);
#pragma unroll
  for (int pc = 0; pc < 4; ++pc) {
    BAR();
    STAMP(2, 3 + pc);
    if (pc < 2) { K3_LOAD_H(pc + 2) }
    const int par = (pc + kg) & 1;
#pragma unroll
    for (int i = 0; i < 4; ++i) {
      if (i < 3 || par == 0) {
        const int e = 2 * i + par;
        const int tA = (e < 6) ? (14 * pc + 7 * (e / 3) + 2 * (e % 3)) : (14 * pc + 6);
        const int tB = (e < 6) ? (tA + 1) : (14 * pc + 13);
#pragma unroll
        for (int j = 0; j < 4; ++j) {
          const bf16x8 wf = tr_frag(smem + rb4[j] + tA * 2048, smem + rb4[j] + tB * 2048);
          acc[j] = __builtin_amdgcn_mfma_f32_16x16x32_bf16(wf, hf[pc][i], acc[j], 0, 0, 0);
        }
      }
    }
  }
  STAMP(2, 7);
  char* red = smem + 896 * 128;
  if (kg == 0) {
    *(f32x4*)(red + (w ^ 4) * 2048 + l * 16) = acc[2];
    *(f32x4*)(red + (w ^ 4) * 2048 + 1024 + l * 16) = acc[3];
  } else {
    *(f32x4*)(red + (w ^ 4) * 2048 + l * 16) = acc[0];
    *(f32x4*)(red + (w ^ 4) * 2048 + 1024 + l * 16) = acc[1];
  }
  BAR();
  const f32x4 a0 = (kg ? acc[2] : acc[0]) + *(const f32x4*)(red + w * 2048 + l * 16);
  const f32x4 a1 = (kg ? acc[3] : acc[1]) + *(const f32x4*)(red + w * 2048 + 1024 + l * 16);
  u16* op = out + (size_t)(tt * 16 + c16) * D_ + n0 + 32 * kg + 4 * g;
  *(u32x2*)op = u32x2{pack2(a0[0], a0[1]), pack2(a0[2], a0[3])};
  *(u32x2*)(op + 16) = u32x2{pack2(a1[0], a1[1]), pack2(a1[2], a1[3])};
  STAMP(2, 8); STAMPR(2, 15);
}

extern "C" void kernel_launch(void* const* d_in, const int* in_sizes, int n_in, void* d_out, int out_size,
                              void* d_ws, size_t ws_size, hipStream_t stream) {
  const u16* x = (const u16*)d_in[0];
  const u16* w_in = (const u16*)d_in[1];
  const u16* w_sp = (const u16*)d_in[2];
  const u16* b_sp = (const u16*)d_in[3];
  const u16* w_out = (const u16*)d_in[4];
  char* ws = (char*)d_ws;
  u16* z2R = (u16*)ws;
  u16* z2N = (u16*)(ws + (size_t)4 * 1024 * 1024);
  u16* hF = (u16*)(ws + (size_t)8 * 1024 * 1024);
  u16* z1F = (u16*)(ws + (size_t)12 * 1024 * 1024);
  f32x2* stats = (f32x2*)(ws + (size_t)16 * 1024 * 1024);
  unsigned* flags = (unsigned*)(ws + (size_t)17 * 1024 * 1024);
  k1_gemm<<<256, 768, 0, stream>>>(x, w_in, z2R, z1F, stats, flags, w_sp, w_out);
  k2_spatial<<<256, 768, 0, stream>>>(w_sp, z2R, stats, b_sp, z1F, z2N, flags, hF);
  k3_out<<<224, 768, 0, stream>>>(hF, w_out, (u16*)d_out);
}
```

```cpp
#include <hip/hip_runtime.h>
#include <hip/hip_bf16.h>

typedef __bf16 bf16x8 __attribute__((ext_vector_type(8)));
typedef __bf16 bf16x4 __attribute__((ext_vector_type(4)));
typedef __bf16 bf16x2 __attribute__((ext_vector_type(2)));
typedef float f32x4 __attribute__((ext_vector_type(4)));
typedef float f32x2 __attribute__((ext_vector_type(2)));
typedef unsigned u32x4 __attribute__((ext_vector_type(4)));
typedef unsigned u32x2 __attribute__((ext_vector_type(2)));
typedef unsigned short u16;
#define LAS __attribute__((address_space(3)))
#define DI __device__ __forceinline__

#define STAMP(k, i)
#define STAMPR(k, i)
constexpr int S_ = 2048, D_ = 448, F_ = 1792, FH = 896;

DI float bf_lo(unsigned u) { return __builtin_bit_cast(float, u << 16); }
DI float bf_hi(unsigned u) { return __builtin_bit_cast(float, u & 0xffff0000u); }
DI unsigned pack2(float a, float b) {
  f32x2 f = {a, b};
  return __builtin_bit_cast(unsigned, __builtin_convertvector(f, bf16x2));
}
DI float bfr(float a) { return (float)(__bf16)a; }
DI float gelu_tanh(float z) {
  const float y = z * __builtin_fmaf(z * z, -0.10294324f, -2.3022082f);
  const float e = __builtin_amdgcn_exp2f(y);
  return z * __builtin_amdgcn_rcpf(1.0f + e);
}
DI bf16x8 tr_frag(const char* a0, const char* a1) {
  bf16x4 lo = __builtin_amdgcn_ds_read_tr16_b64_v4bf16((LAS bf16x4*)a0);
  bf16x4 hi = __builtin_amdgcn_ds_read_tr16_b64_v4bf16((LAS bf16x4*)a1);
  return __builtin_shufflevector(lo, hi, 0, 1, 2, 3, 4, 5, 6, 7);
}
template <int CTRL> DI float dpp_mov(float x) {
  return __builtin_bit_cast(float, __builtin_amdgcn_update_dpp(0, __builtin_bit_cast(int, x), CTRL, 0xf, 0xf, true));
}
DI float sum16(float x) {
  x += dpp_mov<0xB1>(x);
  x += dpp_mov<0x4E>(x);
  x += dpp_mov<0x141>(x);
  x += dpp_mov<0x140>(x);
  return x;
}

#define BAR()                                              \
  do {                                                     \
    asm volatile("s_waitcnt lgkmcnt(0)" ::: "memory");     \
    __builtin_amdgcn_s_barrier();                          \
    asm volatile("" ::: "memory");                         \
  } while (0)
#define WAITV(n) asm volatile("s_waitcnt vmcnt(" #n ")" ::: "memory")
typedef __amdgpu_buffer_rsrc_t rsrc_t;
DI rsrc_t make_rsrc(const void* p, unsigned bytes) { return __builtin_amdgcn_make_buffer_rsrc((void*)p, 0, bytes, 0x00020000); }
DI void blds16(rsrc_t rs, char* lds_wave_base, unsigned voff, unsigned soff) {
  __builtin_amdgcn_raw_ptr_buffer_load_lds(rs, (LAS void*)lds_wave_base, 16, voff, soff, 0, 0);
}

constexpr int XW_XB = 7 * 8192, XW_WB = 448 * 224, XW_LDS = XW_XB + XW_WB;

DI int x_swz(int row) { return ((row >> 1) & 1) | (((row >> 3) & 1) << 1) | ((((row >> 2) ^ (row >> 4)) & 1) << 2); }

struct XwLoader {
  rsrc_t rx, rw;
  unsigned xo[4], wo[7];
  int lw;
  DI XwLoader(const u16* x, const u16* w_in, int rb, int n0, int lw_, int l) : lw(lw_) {
    rx = make_rsrc(x, S_ * D_ * 2);
    rw = make_rsrc(w_in, D_ * F_ * 2);
#pragma unroll
    for (int i = 0; i < 4; ++i) {
      const int pc = 4 * i + lw, row = 8 * (pc & 7) + (l >> 3);
      xo[i] = (unsigned)((rb * 64 + row) * D_ + (pc >> 3) * 64 + (((l & 7) ^ x_swz(row)) * 8)) * 2u;
    }
#pragma unroll
    for (int i = 0; i < 7; ++i) {
      const int c = (4 * i + lw) * 64 + l, row = c / 14, cc = c - row * 14;
      wo[i] = (unsigned)(row * F_ + n0 + cc * 8) * 2u;
    }
  }
  DI void stage_x(char* smem, int s) const {
#pragma unroll
    for (int i = 0; i < (s < 3 ? 4 : 2); ++i) blds16(rx, smem + 2 * s * 8192 + (4 * i + lw) * 1024, xo[i], 256 * s);
  }
  DI void stage_w(char* smem, int s, int col0) const {
#pragma unroll
    for (int i = 0; i < (s < 3 ? 7 : 4); ++i)
      if (s < 3 || 4 * i + lw < 14) blds16(rw, smem + XW_XB + (28 * s + 4 * i + lw) * 1024, wo[i], 128 * F_ * 2 * s + col0 * 2);
  }
};
template <bool Z2> struct XwAddr {
  int xa[2], wa[2];
  DI XwAddr(int l, int fg, int mh) {
    const int g = l >> 4, c16 = l & 15, q = c16 >> 2, p = l & 3;
#pragma unroll
    for (int rt = 0; rt < 2; ++rt) {
      const int row = 32 * mh + (Z2 ? (8 * q + 4 * rt + p) : (16 * rt + c16));
      xa[rt] = row * 128 + (((g >> 1) ^ x_swz(row)) * 16) + (g & 1) * 8;
    }
#pragma unroll
    for (int jj = 0; jj < 2; ++jj) wa[jj] = XW_XB + (4 * g + q) * 224 + min(2 * fg + jj, 6) * 32 + 8 * p;
  }
};
template <int OFF> DI u32x2 lds_read64(unsigned addr) {
  u32x2 r;
  asm volatile("ds_read_b64 %0, %1 offset:%2" : "=v"(r) : "v"(addr), "i"(OFF) : "memory");
  return r;
}
template <bool Z2, int KS0, int NKS>
DI void xw_compute(const XwAddr<Z2>& ad, const char* smem, f32x4 (&acc)[2][2]) {
  u32x2 xl[NKS][2], xh[NKS][2];
  bf16x8 wf[NKS][2];
  const unsigned sb = (unsigned)(size_t)smem;
#pragma unroll
  for (int k = 0; k < NKS; ++k) {
    constexpr int dummy = 0; (void)dummy;
#pragma unroll
    for (int rt = 0; rt < 2; ++rt) {
      const unsigned o = sb + (ad.xa[rt] ^ (64 * ((KS0 + k) & 1)));
      if (k < 2) { xl[k][rt] = lds_read64<8192 * (KS0 >> 1)>(o); xh[k][rt] = lds_read64<8192 * (KS0 >> 1)>(o ^ 32); }
      else       { xl[k][rt] = lds_read64<8192 * (KS0 >> 1) + 8192>(o); xh[k][rt] = lds_read64<8192 * (KS0 >> 1) + 8192>(o ^ 32); }
    }
#pragma unroll
    for (int jj = 0; jj < 2; ++jj) {
      const char* a = smem + ad.wa[jj] + 32 * (KS0 + k) * 224;
      wf[k][jj] = tr_frag(a, a + 16 * 224);
    }
  }
  asm volatile("s_waitcnt lgkmcnt(0)" ::: "memory");
  __builtin_amdgcn_sched_barrier(0);
#pragma unroll
  for (int k = 0; k < NKS; ++k)
#pragma unroll
    for (int rt = 0; rt < 2; ++rt) {
      const bf16x8 xf = __builtin_bit_cast(bf16x8, u32x4{xl[k][rt][0], xl[k][rt][1], xh[k][rt][0], xh[k][rt][1]});
#pragma unroll
      for (int jj = 0; jj < 2; ++jj) {
        if (Z2) acc[rt][jj] = __builtin_amdgcn_mfma_f32_16x16x32_bf16(xf, wf[k][jj], acc[rt][jj], 0, 0, 0);
        else    acc[rt][jj] = __builtin_amdgcn_mfma_f32_16x16x32_bf16(wf[k][jj], xf, acc[rt][jj], 0, 0, 0);
      }
    }
}
DI unsigned gelu_pair(float a, float b) {
  const unsigned u = pack2(a, b);
  const f32x2 z = {bf_lo(u), bf_hi(u)};
  const f32x2 y = z * __builtin_elementwise_fma(z * z, f32x2{-0.10294324f, -0.10294324f}, f32x2{-2.3022082f, -2.3022082f});
  const f32x2 d = f32x2{__builtin_amdgcn_exp2f(y[0]), __builtin_amdgcn_exp2f(y[1])} + f32x2{1.0f, 1.0f};
  const f32x2 o = z * f32x2{__builtin_amdgcn_rcpf(d[0]), __builtin_amdgcn_rcpf(d[1])};
  return pack2(o[0], o[1]);
}

DI void tile_of_block(int bid, int& rb, int& cb) {
  const int xcd = bid & 7, idx = bid >> 3;
  rb = (xcd >> 1) * 8 + (idx & 7);
  cb = (xcd & 1) * 4 + (idx >> 3);
}

__global__ __launch_bounds__(768, 3) void k1_z2(const u16* __restrict__ x, const u16* __restrict__ w_in,
                                               u16* __restrict__ z2R, f32x2* __restrict__ stats,
                                               unsigned* __restrict__ flags, const u16* __restrict__ w_sp,
                                               const u16* __restrict__ w_out) {
  __shared__ __attribute__((aligned(16))) char smem[XW_LDS + 4 * 64 * 8];
  const int bid = blockIdx.x;
  int rb, cb;
  tile_of_block(bid, rb, cb);
  const int tid = threadIdx.x, l = tid & 63, w = __builtin_amdgcn_readfirstlane(tid >> 6);
  if (w >= 8) {
    const int lw = w - 8;
    const XwLoader ld(x, w_in, rb, FH + cb * 112, lw, l);
    ld.stage_x(smem, 0); ld.stage_w(smem, 0, 0);
    ld.stage_x(smem, 1); ld.stage_w(smem, 1, 0);
    WAITV(11); BAR();
    ld.stage_x(smem, 2); ld.stage_w(smem, 2, 0);
    ld.stage_x(smem, 3); ld.stage_w(smem, 3, 0);
    asm volatile("" ::: "memory");
    u32x4 pf[6];
    {
      const int c = bid * 196 + min(lw * 64 + l, 195);
      pf[0] = *(const u32x4*)(w_in + (size_t)(c / 112) * F_ + (c % 112) * 8);
      pf[1] = *(const u32x4*)(w_out + (size_t)c * 8);
    }
#pragma unroll
    for (int i = 0; i < 4; ++i) pf[2 + i] = *(const u32x4*)(w_sp + ((size_t)bid * 2048 + i * 256 + lw * 64 + l) * 8);
    WAITV(22); BAR();
    WAITV(11); BAR();
    WAITV(6); BAR();
#pragma unroll
    for (int i = 0; i < 6; ++i) asm volatile("" ::"v"(pf[i]));
    return;
  }
  const int fg = w & 3, mh = w >> 2, g = l >> 4, c16 = l & 15;
  const bool two = fg < 3;
  STAMPR(0, 0); STAMP(0, 1);
  if (tid == 0) flags[bid * 64] = 0u;
  const XwAddr<true> ad(l, fg, mh);
  f32x4 acc[2][2];
#pragma unroll
  for (int rt = 0; rt < 2; ++rt)
#pragma unroll
    for (int jj = 0; jj < 2; ++jj) acc[rt][jj] = f32x4{0.f, 0.f, 0.f, 0.f};
  BAR(); STAMP(0, 2); xw_compute<true, 0, 4>(ad, smem, acc);
  BAR(); STAMP(0, 3); xw_compute<true, 4, 4>(ad, smem, acc);
  BAR(); STAMP(0, 4); xw_compute<true, 8, 4>(ad, smem, acc);
  BAR(); STAMP(0, 5); xw_compute<true, 12, 2>(ad, smem, acc);
  u32x4 zp[2];
#pragma unroll
  for (int jj = 0; jj < 2; ++jj)
    zp[jj] = u32x4{gelu_pair(acc[0][jj][0], acc[0][jj][1]), gelu_pair(acc[0][jj][2], acc[0][jj][3]),
                   gelu_pair(acc[1][jj][0], acc[1][jj][1]), gelu_pair(acc[1][jj][2], acc[1][jj][3])};
#pragma unroll
  for (int jj = 0; jj < 2; ++jj)
    if (jj == 0 || two) *(u32x4*)(z2R + ((size_t)((cb * 7 + 2 * fg + jj) * 64 + rb * 2 + mh) * 64 + l) * 8) = zp[jj];
  if (!two) zp[1] = u32x4{0u, 0u, 0u, 0u};
  float v[16];
#pragma unroll
  for (int e = 0; e < 4; ++e) {
    const f32x2 a0 = {bf_lo(zp[0][e]), bf_hi(zp[0][e])}, a1 = {bf_lo(zp[1][e]), bf_hi(zp[1][e])};
    const f32x2 s1 = a0 + a1, s2 = __builtin_elementwise_fma(a0, a0, a1 * a1);
    v[4 * e] = s1[0]; v[4 * e + 1] = s2[0]; v[4 * e + 2] = s1[1]; v[4 * e + 3] = s2[1];
  }
#pragma unroll
  for (int i = 0; i < 8; ++i) { const bool b = c16 & 1; v[i] = (b ? v[i + 8] : v[i]) + dpp_mov<0xB1>(b ? v[i] : v[i + 8]); }
#pragma unroll
  for (int i = 0; i < 4; ++i) { const bool b = c16 & 2; v[i] = (b ? v[i + 4] : v[i]) + dpp_mov<0x4E>(b ? v[i] : v[i + 4]); }
#pragma unroll
  for (int i = 0; i < 2; ++i) { const bool b = c16 & 4; v[i] = (b ? v[i + 2] : v[i]) + dpp_mov<0x124>(b ? v[i] : v[i + 2]); }
  { const bool b = c16 & 8; v[0] = (b ? v[1] : v[0]) + dpp_mov<0x128>(b ? v[0] : v[1]); }
  f32x2* sst = (f32x2*)(smem + XW_LDS);
  const int idx = ((c16 & 1) << 3) | ((c16 & 2) << 1) | ((c16 & 4) >> 1) | (c16 >> 3);
  ((float*)(sst + fg * 64 + 32 * mh + 8 * g))[idx] = v[0];
  BAR();
  if (w == 0) {
    const f32x2 t = sst[l] + sst[64 + l] + sst[128 + l] + sst[192 + l];
    stats[(size_t)(rb * 64 + l) * 8 + cb] = f32x2{t[0], fmaxf(t[1] - t[0] * t[0] * (1.0f / 112.0f), 0.f)};
  }
  STAMP(0, 8); STAMPR(0, 15);
}

typedef __attribute__((address_space(1))) unsigned gu32;
#define RLX_AGENT __ATOMIC_RELAXED, __HIP_MEMORY_SCOPE_AGENT
DI int k2_slot(int i) { return (i & 1) ? (XW_XB + ((i >> 1) & 1) * 28672) : (((i >> 1) & 1) * 16384); }

__global__ __launch_bounds__(768, 3) void k2_spatial(const u16* __restrict__ x, const u16* __restrict__ w_in,
                                                    const u16* __restrict__ w_sp, const u16* __restrict__ z2R,
                                                    const f32x2* __restrict__ stats, const u16* __restrict__ b_sp,
                                                    u16* z2N, unsigned* flags, u16* __restrict__ hF) {
  __shared__ __attribute__((aligned(16))) char smem[XW_LDS + 8 * 512 + 1024];
  const int bid = blockIdx.x;
  int rb, cb;
  tile_of_block(bid, rb, cb);
  const int tid = threadIdx.x, l = tid & 63, w = __builtin_amdgcn_readfirstlane(tid >> 6);
  const int t0 = rb * 64;
  const int koff = (2 * (rb & 7) + (cb & 1)) & 15;

  if (w >= 8) {
    const int lw = w - 8;
    const XwLoader ld(x, w_in, rb, cb * 112, lw, l);
    const rsrc_t rsA = make_rsrc(w_sp, S_ * S_ * 2);
    unsigned ap[4];
#pragma unroll
    for (int i = 0; i < 4; ++i) {
      const int row = 4 * (4 * i + lw) + (l >> 4);
      ap[i] = (unsigned)((t0 + row) * S_ + (((l & 15) ^ (row & 15)) * 8)) * 2u;
    }
#define K2_LOAD_W(st)                                                                             \
  {                                                                                               \
    const int sk_ = ((st) + koff) & 15;                                                           \
    char* dst_ = smem + k2_slot(st) + lw * 1024;                                                  \
    _Pragma("unroll") for (int i = 0; i < 4; ++i) blds16(rsA, dst_ + i * 4096, ap[i], 256 * sk_); \
  }
    ld.stage_x(smem, 0); ld.stage_w(smem, 0, 0);
    ld.stage_x(smem, 1); ld.stage_w(smem, 1, 0);
    WAITV(11); BAR();
    ld.stage_x(smem, 2); ld.stage_w(smem, 2, 0);
    ld.stage_x(smem, 3); ld.stage_w(smem, 3, 0);
    WAITV(16); BAR();
    K2_LOAD_W(0) K2_LOAD_W(1)
    WAITV(13); BAR();
    K2_LOAD_W(2) K2_LOAD_W(3)
    WAITV(16); BAR();
    WAITV(12); BAR();
    WAITV(8); BAR();
#pragma unroll
    for (int st = 1; st < 13; ++st) {
      K2_LOAD_W(st + 3)
      WAITV(8); BAR();
    }
    WAITV(4); BAR();
    WAITV(0); BAR();
    return;
  }

  const int fg = w & 3, kg = w >> 2, g = l >> 4, c16 = l & 15;
  const bool two = fg < 3;
  STAMPR(1, 0); STAMP(1, 1);
  const rsrc_t rsN = make_rsrc(z2N, S_ * FH * 2);
  const rsrc_t rsN1 = make_rsrc(z2N, two ? S_ * FH * 2 : 0);

  if (w < 7) {
    const unsigned zoff = (unsigned)(((cb * 7 + w) * 64 + 2 * rb) * 64 + l) * 16u;
    f32x4 sp[4];
#pragma unroll
    for (int i = 0; i < 4; ++i) sp[i] = *(const f32x4*)(stats + (size_t)(t0 + l) * 8 + 2 * i);
    const u32x4 zr0 = *(const u32x4*)((const char*)z2R + zoff), zr1 = *(const u32x4*)((const char*)z2R + zoff + 1024);
    f32x2* mrs = (f32x2*)(smem + XW_LDS + w * 512);
    float sum = 0.f, m2 = 0.f;
#pragma unroll
    for (int i = 0; i < 4; ++i) { sum += sp[i][0] + sp[i][2]; m2 += sp[i][1] + sp[i][3]; }
    const float mean = sum * (1.0f / 896.0f);
#pragma unroll
    for (int i = 0; i < 4; ++i) {
      const float d0 = sp[i][0] * (1.0f / 112.0f) - mean, d1 = sp[i][2] * (1.0f / 112.0f) - mean;
      m2 += 112.0f * (d0 * d0 + d1 * d1);
    }
    const float var = bfr(m2 * (1.0f / 896.0f));
    const float den = bfr(sqrtf(bfr(var + 1e-5f)));
    mrs[l] = f32x2{bfr(mean), 1.0f / den};
#pragma unroll
    for (int hh = 0; hh < 2; ++hh) {
      const u32x4 u = hh ? zr1 : zr0;
      u32x4 v;
#pragma unroll
      for (int e = 0; e < 4; ++e) {
        const f32x4 mr = *(const f32x4*)(mrs + 32 * hh + 8 * g + 2 * e);
        v[e] = pack2(bfr(bf_lo(u[e]) - mr[0]) * mr[1], bfr(bf_hi(u[e]) - mr[2]) * mr[3]);
      }
      __builtin_amdgcn_raw_buffer_store_b128(v, rsN, zoff + 1024u * hh, 0, 16);
    }
  }
  STAMP(1, 2);

  const unsigned boff = (unsigned)(((cb * 7 + 2 * fg) * 64 + kg) * 64 + l) * 16u;
  u32x4 bst[4][4];
#define K2_LOAD_B(slot, st)                                                                       \
  {                                                                                               \
    const int sk_ = ((st) + koff) & 15;                                                           \
    bst[slot][0] = __builtin_amdgcn_raw_buffer_load_b128(rsN, boff, 4096 * sk_, 16);              \
    bst[slot][1] = __builtin_amdgcn_raw_buffer_load_b128(rsN1, boff + 65536, 4096 * sk_, 16);     \
    bst[slot][2] = __builtin_amdgcn_raw_buffer_load_b128(rsN, boff + 2048, 4096 * sk_, 16);       \
    bst[slot][3] = __builtin_amdgcn_raw_buffer_load_b128(rsN1, boff + 65536 + 2048, 4096 * sk_, 16); \
  }
  u32x2 z1p[2][2];
  {
    const XwAddr<false> ad(l, fg, kg);
    f32x4 za[2][2];
#pragma unroll
    for (int i = 0; i < 2; ++i)
#pragma unroll
      for (int j = 0; j < 2; ++j) za[i][j] = f32x4{0.f, 0.f, 0.f, 0.f};
    BAR(); STAMP(1, 9); xw_compute<false, 0, 4>(ad, smem, za);
    if (w < 7) {
      WAITV(0);
      if (l == 0) __hip_atomic_fetch_add((gu32*)flags + (cb * 32 + rb) * 64, 1u, RLX_AGENT);
    } else {
      const unsigned dst = __builtin_amdgcn_readfirstlane((unsigned)(size_t)(smem + XW_LDS + 8 * 512));
#pragma unroll
      for (int i = 0; i < 16; ++i) {
        unsigned keep;
        asm volatile("s_mov_b32 %0, m0\n\ts_mov_b32 m0, %2\n\ts_nop 0\n\tglobal_load_lds_dwordx4 %1, off\n\ts_mov_b32 m0, %0"
                     : "=&s"(keep) : "v"(w_sp + ((size_t)bid * 2048 + 1024 + i * 64 + l) * 8), "s"(dst) : "memory");
      }
    }
    BAR(); STAMP(1, 10); xw_compute<false, 4, 4>(ad, smem, za);
    if (w == 0) {
      const gu32* fp = (const gu32*)flags + (cb * 32 + (l & 31)) * 64;
      for (unsigned spins = 0; spins < (1u << 20); ++spins) {
        if (__all(__hip_atomic_load(fp, RLX_AGENT) == 7u)) break;
        __builtin_amdgcn_s_sleep(4);
      }
    }
    BAR();
    STAMP(1, 11);
    K2_LOAD_B(0, 0) K2_LOAD_B(1, 1)
    xw_compute<false, 8, 4>(ad, smem, za);
    BAR();
    STAMP(1, 3);
    K2_LOAD_B(2, 2)
    xw_compute<false, 12, 2>(ad, smem, za);
#pragma unroll
    for (int i = 0; i < 2; ++i)
#pragma unroll
      for (int j = 0; j < 2; ++j) {
        z1p[i][j] = u32x2{gelu_pair(za[i][j][0], za[i][j][1]), gelu_pair(za[i][j][2], za[i][j][3])};
        asm volatile("" : "+v"(z1p[i][j]));
      }
  }
  STAMP(1, 4);

  int ard[4];
#pragma unroll
  for (int i = 0; i < 4; ++i) ard[i] = (16 * ((i + 2 * kg) & 3) + c16) * 256 + (((4 * kg + g) ^ c16) * 16);
  f32x4 acc[4][2];
#pragma unroll
  for (int i = 0; i < 4; ++i)
#pragma unroll
    for (int j = 0; j < 2; ++j) acc[i][j] = f32x4{0.f, 0.f, 0.f, 0.f};
  bf16x8 af[2][4];
#define K2_READ_A(u, h)                                                                                  \
  {                                                                                                      \
    const char* ab = smem + k2_slot(u);                                                                  \
    _Pragma("unroll") for (int i = 0; i < 4; ++i) af[h][i] = *(const bf16x8*)(ab + (ard[i] ^ (128 * (h)))); \
    __builtin_amdgcn_sched_barrier(0);                                                                   \
  }
#define K2_MMA(u, h)                                                                                     \
  {                                                                                                      \
    _Pragma("unroll") for (int j = 0; j < 2; ++j) {                                                      \
      const bf16x8 zf = __builtin_bit_cast(bf16x8, bst[u][2 * (h) + j]);                                 \
      _Pragma("unroll") for (int i = 0; i < 4; ++i)                                                      \
        acc[i][j] = __builtin_amdgcn_mfma_f32_16x16x32_bf16(zf, af[h][i], acc[i][j], 0, 0, 0);           \
    }                                                                                                    \
    __builtin_amdgcn_sched_barrier(0);                                                                   \
  }
#define K2_ITER(u, it, PF)                           \
  {                                                  \
    BAR();                                           \
    K2_READ_A(u, 0)                                  \
    K2_MMA(((u) + 3) & 3, 1)                         \
    K2_READ_A(u, 1)                                  \
    if (PF) { K2_LOAD_B(((u) + 3) & 3, (it) + 3) }   \
    K2_MMA(u, 0)                                     \
  }
  BAR();
  K2_READ_A(0, 0)
  K2_READ_A(0, 1)
  K2_LOAD_B(3, 3)
  K2_MMA(0, 0)
#pragma unroll 1
  for (int it = 1; it < 13; it += 4) {
    K2_ITER(1, it, true) K2_ITER(2, it + 1, true) K2_ITER(3, it + 2, true) K2_ITER(0, it + 3, true)
    STAMP(1, 5 + (it >> 2));
  }
  K2_ITER(1, 13, false)
  int lz = threadIdx.x & 63;
  asm volatile("" : "+v"(lz));
  float bias[2];
#pragma unroll
  for (int i = 0; i < 2; ++i) bias[i] = bf_lo(b_sp[(rb * 4 + 2 * kg + i) * 16 + (lz & 15)]);
  K2_ITER(2, 14, false) K2_ITER(3, 15, false)
  K2_MMA(3, 1)
  STAMP(1, 8);

  char* red = smem + XW_XB + 57344;
#pragma unroll
  for (int i = 0; i < 2; ++i)
#pragma unroll
    for (int j = 0; j < 2; ++j) *(f32x4*)(red + ((w ^ 4) * 4 + 2 * i + j) * 1024 + lz * 16) = acc[2 + i][j];
  BAR();
  STAMP(1, 12);
#pragma unroll
  for (int i = 0; i < 2; ++i) {
    u32x2 hv[2];
#pragma unroll
    for (int j = 0; j < 2; ++j) {
      const f32x4 a = acc[i][j] + *(const f32x4*)(red + (w * 4 + 2 * i + j) * 1024 + lz * 16);
      const unsigned g01 = pack2(a[0], a[1]), g23 = pack2(a[2], a[3]);
      const unsigned s01 = pack2(bf_lo(g01) + bias[i], bf_hi(g01) + bias[i]);
      const unsigned s23 = pack2(bf_lo(g23) + bias[i], bf_hi(g23) + bias[i]);
      hv[j] = u32x2{pack2(bf_lo(z1p[i][j][0]) * bf_lo(s01), bf_hi(z1p[i][j][0]) * bf_hi(s01)),
                    pack2(bf_lo(z1p[i][j][1]) * bf_lo(s23), bf_hi(z1p[i][j][1]) * bf_hi(s23))};
    }
    const int tt = rb * 4 + 2 * kg + i;
    u16* hp = hF + ((size_t)(tt * 28 + 7 * (cb >> 1)) * 64 + lz) * 8;
    if (two) *(u32x4*)(hp + (size_t)(3 * (cb & 1) + fg) * 512) = u32x4{hv[0][0], hv[0][1], hv[1][0], hv[1][1]};
    else     *(u32x2*)(hp + 6 * 512 + 4 * (cb & 1)) = hv[0];
  }
  STAMP(1, 13); STAMPR(1, 15);
}

__global__ __launch_bounds__(768, 3) void k3_out(const u16* __restrict__ hF, const u16* __restrict__ w_out,
                                                u16* __restrict__ out) {
  __shared__ __attribute__((aligned(16))) char smem[896 * 128 + 8 * 2048];
  const int bid = blockIdx.x, xcd = bid & 7, idx = bid >> 3;
  const int rb = xcd * 4 + idx / 7, nb = idx % 7;
  const int tid = threadIdx.x, l = tid & 63, w = __builtin_amdgcn_readfirstlane(tid >> 6);
  const int n0 = nb * 64;
  if (w >= 8) {
    const int lw = w - 8, r8 = l >> 3;
    const rsrc_t rw = make_rsrc(w_out, FH * D_ * 2);
    const unsigned wo = (unsigned)((8 * lw + r8) * D_ + n0 + (((l & 7) ^ (((r8 >> 1) & 3) << 1)) * 8)) * 2u;
#pragma unroll
    for (int i = 0; i < 14; ++i) blds16(rw, smem + (4 * i + lw) * 1024, wo, i * 32 * D_ * 2);
    WAITV(7); BAR();
#pragma unroll
    for (int i = 14; i < 21; ++i) blds16(rw, smem + (4 * i + lw) * 1024, wo, i * 32 * D_ * 2);
    WAITV(7); BAR();
#pragma unroll
    for (int i = 21; i < 28; ++i) blds16(rw, smem + (4 * i + lw) * 1024, wo, i * 32 * D_ * 2);
    WAITV(7); BAR();
    WAITV(0); BAR();
    return;
  }
  const int tq = w & 3, kg = w >> 2, g = l >> 4, c16 = l & 15, q = c16 >> 2, p = l & 3;
  const int tt = rb * 4 + tq;
  STAMPR(2, 0); STAMP(2, 1);
  bf16x8 hf[4][4];
  const u16* hp = hF + ((size_t)tt * 28 * 64 + l) * 8;
#define K3_LOAD_H(pc) \
  _Pragma("unroll") for (int i = 0; i < 4; ++i) hf[pc][i] = *(const bf16x8*)(hp + (7 * (pc) + min(2 * i + (((pc) + kg) & 1), 6)) * 512);
  K3_LOAD_H(0) K3_LOAD_H(1)
  const int rrow = 4 * g + q;
  int rb4[4];
#pragma unroll
  for (int j = 0; j < 4; ++j) rb4[j] = rrow * 128 + ((j ^ ((rrow >> 1) & 3)) * 32) + 8 * p;
  f32x4 acc[4];
#pragma unroll
  for (int j = 0; j < 4; ++j) acc[j] = f32x4{0.f, 0.f, 0.f, 0.f};
  STAMP(2, 2);
#pragma unroll
  for (int pc = 0; pc < 4; ++pc) {
    BAR();
    STAMP(2, 3 + pc);
    if (pc < 2) { K3_LOAD_H(pc + 2) }
    const int par = (pc + kg) & 1;
#pragma unroll
    for (int i = 0; i < 4; ++i) {
      if (i < 3 || par == 0) {
        const int e = 2 * i + par;
        const int tA = (e < 6) ? (14 * pc + 7 * (e / 3) + 2 * (e % 3)) : (14 * pc + 6);
        const int tB = (e < 6) ? (tA + 1) : (14 * pc + 13);
#pragma unroll
        for (int j = 0; j < 4; ++j) {
          const bf16x8 wf = tr_frag(smem + rb4[j] + tA * 2048, smem + rb4[j] + tB * 2048);
          acc[j] = __builtin_amdgcn_mfma_f32_16x16x32_bf16(wf, hf[pc][i], acc[j], 0, 0, 0);
        }
      }
    }
  }
  STAMP(2, 7);
  char* red = smem + 896 * 128;
  if (kg == 0) {
    *(f32x4*)(red + (w ^ 4) * 2048 + l * 16) = acc[2];
    *(f32x4*)(red + (w ^ 4) * 2048 + 1024 + l * 16) = acc[3];
  } else {
    *(f32x4*)(red + (w ^ 4) * 2048 + l * 16) = acc[0];
    *(f32x4*)(red + (w ^ 4) * 2048 + 1024 + l * 16) = acc[1];
  }
  BAR();
  const f32x4 a0 = (kg ? acc[2] : acc[0]) + *(const f32x4*)(red + w * 2048 + l * 16);
  const f32x4 a1 = (kg ? acc[3] : acc[1]) + *(const f32x4*)(red + w * 2048 + 1024 + l * 16);
  u16* op = out + (size_t)(tt * 16 + c16) * D_ + n0 + 32 * kg + 4 * g;
  *(u32x2*)op = u32x2{pack2(a0[0], a0[1]), pack2(a0[2], a0[3])};
  *(u32x2*)(op + 16) = u32x2{pack2(a1[0], a1[1]), pack2(a1[2], a1[3])};
  STAMP(2, 8); STAMPR(2, 15);
}

extern "C" void kernel_launch(void* const* d_in, const int* in_sizes, int n_in, void* d_out, int out_size,
                              void* d_ws, size_t ws_size, hipStream_t stream) {
  const u16* x = (const u16*)d_in[0];
  const u16* w_in = (const u16*)d_in[1];
  const u16* w_sp = (const u16*)d_in[2];
  const u16* b_sp = (const u16*)d_in[3];
  const u16* w_out = (const u16*)d_in[4];
  char* ws = (char*)d_ws;
  u16* z2R = (u16*)ws;
  u16* z2N = (u16*)(ws + (size_t)4 * 1024 * 1024);
  u16* hF = (u16*)(ws + (size_t)8 * 1024 * 1024);
  f32x2* stats = (f32x2*)(ws + (size_t)12 * 1024 * 1024);
  unsigned* flags = (unsigned*)(ws + (size_t)13 * 1024 * 1024);
  k1_z2<<<256, 768, 0, stream>>>(x, w_in, z2R, stats, flags, w_sp, w_out);
  k2_spatial<<<256, 768, 0, stream>>>(x, w_in, w_sp, z2R, stats, b_sp, z2N, flags, hF);
  k3_out<<<224, 768, 0, stream>>>(hF, w_out, (u16*)d_out);
}
```

```cpp
#include <hip/hip_runtime.h>
#include <hip/hip_bf16.h>

typedef __bf16 bf16x8 __attribute__((ext_vector_type(8)));
typedef __bf16 bf16x4 __attribute__((ext_vector_type(4)));
typedef __bf16 bf16x2 __attribute__((ext_vector_type(2)));
typedef float f32x4 __attribute__((ext_vector_type(4)));
typedef float f32x2 __attribute__((ext_vector_type(2)));
typedef unsigned u32x4 __attribute__((ext_vector_type(4)));
typedef unsigned u32x2 __attribute__((ext_vector_type(2)));
typedef unsigned short u16;
#define LAS __attribute__((address_space(3)))
#define DI __device__ __forceinline__

#define STAMP(k, i)
#define STAMPR(k, i)
constexpr int S_ = 2048, D_ = 448, F_ = 1792, FH = 896;

DI float bf_lo(unsigned u) { return __builtin_bit_cast(float, u << 16); }
DI float bf_hi(unsigned u) { return __builtin_bit_cast(float, u & 0xffff0000u); }
DI unsigned pack2(float a, float b) {
  f32x2 f = {a, b};
  return __builtin_bit_cast(unsigned, __builtin_convertvector(f, bf16x2));
}
DI float bfr(float a) { return (float)(__bf16)a; }
DI float gelu_tanh(float z) {
  const float y = z * __builtin_fmaf(z * z, -0.10294324f, -2.3022082f);
  const float e = __builtin_amdgcn_exp2f(y);
  return z * __builtin_amdgcn_rcpf(1.0f + e);
}
DI bf16x8 tr_frag(const char* a0, const char* a1) {
  bf16x4 lo = __builtin_amdgcn_ds_read_tr16_b64_v4bf16((LAS bf16x4*)a0);
  bf16x4 hi = __builtin_amdgcn_ds_read_tr16_b64_v4bf16((LAS bf16x4*)a1);
  return __builtin_shufflevector(lo, hi, 0, 1, 2, 3, 4, 5, 6, 7);
}
template <int CTRL> DI float dpp_mov(float x) {
  return __builtin_bit_cast(float, __builtin_amdgcn_update_dpp(0, __builtin_bit_cast(int, x), CTRL, 0xf, 0xf, true));
}
DI float sum16(float x) {
  x += dpp_mov<0xB1>(x);
  x += dpp_mov<0x4E>(x);
  x += dpp_mov<0x141>(x);
  x += dpp_mov<0x140>(x);
  return x;
}

#define BAR()                                              \
  do {                                                     \
    asm volatile("s_waitcnt lgkmcnt(0)" ::: "memory");     \
    __builtin_amdgcn_s_barrier();                          \
    asm volatile("" ::: "memory");                         \
  } while (0)
#define WAITV(n) asm volatile("s_waitcnt vmcnt(" #n ")" ::: "memory")
typedef __amdgpu_buffer_rsrc_t rsrc_t;
DI rsrc_t make_rsrc(const void* p, unsigned bytes) { return __builtin_amdgcn_make_buffer_rsrc((void*)p, 0, bytes, 0x00020000); }
DI void blds16(rsrc_t rs, char* lds_wave_base, unsigned voff, unsigned soff) {
  __builtin_amdgcn_raw_ptr_buffer_load_lds(rs, (LAS void*)lds_wave_base, 16, voff, soff, 0, 0);
}

constexpr int XW_XB = 7 * 8192, XW_WB = 448 * 224, XW_LDS = XW_XB + XW_WB;

DI int x_swz(int row) { return ((row >> 1) & 1) | (((row >> 3) & 1) << 1) | ((((row >> 2) ^ (row >> 4)) & 1) << 2); }

struct XwLoader {
  rsrc_t rx, rw;
  unsigned xo[4], wo[7];
  int lw;
  DI XwLoader(const u16* x, const u16* w_in, int rb, int n0, int lw_, int l) : lw(lw_) {
    rx = make_rsrc(x, S_ * D_ * 2);
    rw = make_rsrc(w_in, D_ * F_ * 2);
#pragma unroll
    for (int i = 0; i < 4; ++i) {
      const int pc = 4 * i + lw, row = 8 * (pc & 7) + (l >> 3);
      xo[i] = (unsigned)((rb * 64 + row) * D_ + (pc >> 3) * 64 + (((l & 7) ^ x_swz(row)) * 8)) * 2u;
    }
#pragma unroll
    for (int i = 0; i < 7; ++i) {
      const int c = (4 * i + lw) * 64 + l, row = c / 14, cc = c - row * 14;
      wo[i] = (unsigned)(row * F_ + n0 + cc * 8) * 2u;
    }
  }
  DI void stage_x(char* smem, int s) const {
#pragma unroll
    for (int i = 0; i < (s < 3 ? 4 : 2); ++i) blds16(rx, smem + 2 * s * 8192 + (4 * i + lw) * 1024, xo[i], 256 * s);
  }
  DI void stage_w(char* smem, int s, int col0) const {
#pragma unroll
    for (int i = 0; i < (s < 3 ? 7 : 4); ++i)
      if (s < 3 || 4 * i + lw < 14) blds16(rw, smem + XW_XB + (28 * s + 4 * i + lw) * 1024, wo[i], 128 * F_ * 2 * s + col0 * 2);
  }
};
template <bool Z2> struct XwAddr {
  int xa[2], wa[2];
  DI XwAddr(int l, int fg, int mh) {
    const int g = l >> 4, c16 = l & 15, q = c16 >> 2, p = l & 3;
#pragma unroll
    for (int rt = 0; rt < 2; ++rt) {
      const int row = 32 * mh + (Z2 ? (8 * q + 4 * rt + p) : (16 * rt + c16));
      xa[rt] = row * 128 + (((g >> 1) ^ x_swz(row)) * 16) + (g & 1) * 8;
    }
#pragma unroll
    for (int jj = 0; jj < 2; ++jj) wa[jj] = XW_XB + (4 * g + q) * 224 + min(2 * fg + jj, 6) * 32 + 8 * p;
  }
};
template <int OFF> DI u32x2 lds_read64(unsigned addr) {
  u32x2 r;
  asm volatile("ds_read_b64 %0, %1 offset:%2" : "=v"(r) : "v"(addr), "i"(OFF) : "memory");
  return r;
}
template <bool Z2, int KS0, int NKS>
DI void xw_compute(const XwAddr<Z2>& ad, const char* smem, f32x4 (&acc)[2][2]) {
  u32x2 xl[NKS][2], xh[NKS][2];
  bf16x8 wf[NKS][2];
  const unsigned sb = (unsigned)(size_t)smem;
#pragma unroll
  for (int k = 0; k < NKS; ++k) {
    constexpr int dummy = 0; (void)dummy;
#pragma unroll
    for (int rt = 0; rt < 2; ++rt) {
      const unsigned o = sb + (ad.xa[rt] ^ (64 * ((KS0 + k) & 1)));
      if (k < 2) { xl[k][rt] = lds_read64<8192 * (KS0 >> 1)>(o); xh[k][rt] = lds_read64<8192 * (KS0 >> 1)>(o ^ 32); }
      else       { xl[k][rt] = lds_read64<8192 * (KS0 >> 1) + 8192>(o); xh[k][rt] = lds_read64<8192 * (KS0 >> 1) + 8192>(o ^ 32); }
    }
#pragma unroll
    for (int jj = 0; jj < 2; ++jj) {
      const char* a = smem + ad.wa[jj] + 32 * (KS0 + k) * 224;
      wf[k][jj] = tr_frag(a, a + 16 * 224);
    }
  }
  asm volatile("s_waitcnt lgkmcnt(0)" ::: "memory");
  __builtin_amdgcn_sched_barrier(0);
#pragma unroll
  for (int k = 0; k < NKS; ++k)
#pragma unroll
    for (int rt = 0; rt < 2; ++rt) {
      const bf16x8 xf = __builtin_bit_cast(bf16x8, u32x4{xl[k][rt][0], xl[k][rt][1], xh[k][rt][0], xh[k][rt][1]});
#pragma unroll
      for (int jj = 0; jj < 2; ++jj) {
        if (Z2) acc[rt][jj] = __builtin_amdgcn_mfma_f32_16x16x32_bf16(xf, wf[k][jj], acc[rt][jj], 0, 0, 0);
        else    acc[rt][jj] = __builtin_amdgcn_mfma_f32_16x16x32_bf16(wf[k][jj], xf, acc[rt][jj], 0, 0, 0);
      }
    }
}
DI unsigned gelu_pair(float a, float b) {
  const unsigned u = pack2(a, b);
  const f32x2 z = {bf_lo(u), bf_hi(u)};
  const f32x2 y = z * __builtin_elementwise_fma(z * z, f32x2{-0.10294324f, -0.10294324f}, f32x2{-2.3022082f, -2.3022082f});
  const f32x2 d = f32x2{__builtin_amdgcn_exp2f(y[0]), __builtin_amdgcn_exp2f(y[1])} + f32x2{1.0f, 1.0f};
  const f32x2 o = z * f32x2{__builtin_amdgcn_rcpf(d[0]), __builtin_amdgcn_rcpf(d[1])};
  return pack2(o[0], o[1]);
}

DI void tile_of_block(int bid, int& rb, int& cb) {
  const int xcd = bid & 7, idx = bid >> 3;
  rb = (xcd >> 1) * 8 + (idx & 7);
  cb = (xcd & 1) * 4 + (idx >> 3);
}

typedef unsigned long long u64;
typedef __attribute__((address_space(1))) u64 gu64;
#define RLX_AGENT __ATOMIC_RELAXED, __HIP_MEMORY_SCOPE_AGENT
DI constexpr int ring_slot(int st) {
  return 16384 * ((st % 9) == 0 ? 0 : (st % 9) == 1 ? 4 : (st % 9) == 2 ? 1 : (st % 9) == 3 ? 5 : (st % 9) == 4 ? 6 :
                  (st % 9) == 5 ? 2 : (st % 9) == 6 ? 7 : (st % 9) == 7 ? 3 : 8);
}
DI void wait_flags(const u64* f, int n, int l, u64 tag) {
  const gu64* fp = (const gu64*)f + (l < n ? l : 0) * 32;
  for (unsigned spins = 0; spins < (1u << 20); ++spins) {
    if (__all(__hip_atomic_load(fp, RLX_AGENT) == tag)) break;
    __builtin_amdgcn_s_sleep(4);
  }
}

__global__ __launch_bounds__(768, 3) void gmlp_fused(const u16* __restrict__ x, const u16* __restrict__ w_in,
                                                    const u16* __restrict__ w_sp, const u16* __restrict__ b_sp,
                                                    const u16* __restrict__ w_out, u16* __restrict__ out,
                                                    float* stats, u16* z2N, u16* hF, u64* sync) {
  __shared__ __attribute__((aligned(16))) char smem[XW_LDS + 2048 + 8 * 256];
  const int bid = blockIdx.x;
  int rb, cb;
  tile_of_block(bid, rb, cb);
  const int tid = threadIdx.x, l = tid & 63, w = __builtin_amdgcn_readfirstlane(tid >> 6);
  const int t0 = rb * 64;
  const int koff = (2 * (rb & 7) + (cb & 1)) & 15;

  if (w >= 8) {
    const int lw = w - 8;
    const XwLoader ld(x, w_in, rb, cb * 112, lw, l);
    const rsrc_t rsA = make_rsrc(w_sp, S_ * S_ * 2);
    const rsrc_t rsO = make_rsrc(w_out, cb < 7 ? FH * D_ * 2 : 0);
    unsigned ap[4];
#pragma unroll
    for (int i = 0; i < 4; ++i) {
      const int row = 4 * (4 * i + lw) + (l >> 4);
      ap[i] = (unsigned)((t0 + row) * S_ + (((l & 15) ^ (row & 15)) * 8)) * 2u;
    }
    const unsigned oo = (unsigned)((8 * lw + (l >> 3)) * D_ + cb * 64 + (((l & 7) ^ (((l >> 4) & 3) << 1)) * 8)) * 2u;
#define LOAD_W(st)                                                                                \
  {                                                                                               \
    const int sk_ = ((st) + koff) & 15;                                                           \
    char* dst_ = smem + ring_slot(st) + lw * 1024;                                                \
    _Pragma("unroll") for (int i = 0; i < 4; ++i) blds16(rsA, dst_ + i * 4096, ap[i], 256 * sk_); \
  }
#define LOAD_O(c)        \
  {                                                                                                           \
    char* dst_ = smem + ring_slot((c) + 7) + lw * 1024;                                                       \
    _Pragma("unroll") for (int i = 0; i < 4; ++i) blds16(rsO, dst_ + i * 4096, oo, (128 * (c) + 32 * i) * D_ * 2); \
  }
    ld.stage_x(smem, 0); ld.stage_w(smem, 0, FH);
    ld.stage_x(smem, 1); ld.stage_w(smem, 1, FH);
    WAITV(11); BAR();
    ld.stage_x(smem, 2); ld.stage_w(smem, 2, FH);
    ld.stage_x(smem, 3); ld.stage_w(smem, 3, FH);
    WAITV(16); BAR();
    ld.stage_w(smem, 0, 0);
    WAITV(12); BAR();
    ld.stage_w(smem, 1, 0);
    WAITV(14); BAR();
    ld.stage_w(smem, 2, 0);
    WAITV(14); BAR();
    ld.stage_w(smem, 3, 0);
    WAITV(10); BAR();
    LOAD_W(0) LOAD_W(1)
    WAITV(11); BAR();
    LOAD_W(2) LOAD_W(3) LOAD_W(4)
    WAITV(20); BAR();
    LOAD_W(5) LOAD_W(6)
    BAR();
    LOAD_W(7) LOAD_W(8)
    WAITV(32); BAR();
    WAITV(28); BAR();
#pragma unroll
    for (int st = 1; st < 8; ++st) {
      LOAD_W(st + 8)
      WAITV(28); BAR();
    }
#pragma unroll
    for (int c = 0; c < 7; ++c) {
      LOAD_O(c)
      WAITV(28); BAR();
    }
    BAR(); BAR(); BAR();
    WAITV(0); BAR();
    return;
  }

  const int fg = w & 3, kg = w >> 2, g = l >> 4, c16 = l & 15;
  const bool two = fg < 3;
  STAMPR(0, 0); STAMP(0, 1);
  const u64 tag = __hip_atomic_load((const gu64*)sync, RLX_AGENT) + 1;
  u64* sflag = sync + 32;
  u64* zflag = sflag + 256 * 32;
  u64* hflag = zflag + 256 * 32;
  const rsrc_t rsS = make_rsrc(stats, S_ * 8 * 8);
  const rsrc_t rsN = make_rsrc(z2N, S_ * FH * 2);
  const rsrc_t rsN1 = make_rsrc(z2N, two ? S_ * FH * 2 : 0);
  f32x2* sst = (f32x2*)(smem + XW_LDS);

  u32x4 zp[2];
  {
    const XwAddr<true> ad(l, fg, kg);
    f32x4 acc[2][2];
#pragma unroll
    for (int rt = 0; rt < 2; ++rt)
#pragma unroll
      for (int jj = 0; jj < 2; ++jj) acc[rt][jj] = f32x4{0.f, 0.f, 0.f, 0.f};
    BAR(); STAMP(0, 2); xw_compute<true, 0, 4>(ad, smem, acc);
    BAR(); xw_compute<true, 4, 4>(ad, smem, acc);
    BAR(); xw_compute<true, 8, 4>(ad, smem, acc);
    BAR(); xw_compute<true, 12, 2>(ad, smem, acc);
#pragma unroll
    for (int jj = 0; jj < 2; ++jj)
      zp[jj] = u32x4{gelu_pair(acc[0][jj][0], acc[0][jj][1]), gelu_pair(acc[0][jj][2], acc[0][jj][3]),
                     gelu_pair(acc[1][jj][0], acc[1][jj][1]), gelu_pair(acc[1][jj][2], acc[1][jj][3])};
    if (!two) zp[1] = u32x4{0u, 0u, 0u, 0u};
    float v[16];
#pragma unroll
    for (int e = 0; e < 4; ++e) {
      const f32x2 a0 = {bf_lo(zp[0][e]), bf_hi(zp[0][e])}, a1 = {bf_lo(zp[1][e]), bf_hi(zp[1][e])};
      const f32x2 s1 = a0 + a1, s2 = __builtin_elementwise_fma(a0, a0, a1 * a1);
      v[4 * e] = s1[0]; v[4 * e + 1] = s2[0]; v[4 * e + 2] = s1[1]; v[4 * e + 3] = s2[1];
    }
#pragma unroll
    for (int i = 0; i < 8; ++i) { const bool b = c16 & 1; v[i] = (b ? v[i + 8] : v[i]) + dpp_mov<0xB1>(b ? v[i] : v[i + 8]); }
#pragma unroll
    for (int i = 0; i < 4; ++i) { const bool b = c16 & 2; v[i] = (b ? v[i + 4] : v[i]) + dpp_mov<0x4E>(b ? v[i] : v[i + 4]); }
#pragma unroll
    for (int i = 0; i < 2; ++i) { const bool b = c16 & 4; v[i] = (b ? v[i + 2] : v[i]) + dpp_mov<0x124>(b ? v[i] : v[i + 2]); }
    { const bool b = c16 & 8; v[0] = (b ? v[1] : v[0]) + dpp_mov<0x128>(b ? v[0] : v[1]); }
    const int idx = ((c16 & 1) << 3) | ((c16 & 2) << 1) | ((c16 & 4) >> 1) | (c16 >> 3);
    ((float*)(sst + fg * 64 + 32 * kg + 8 * g))[idx] = v[0];
  }
  STAMP(0, 3);

  u32x2 z1p[2][2];
  {
    const XwAddr<false> ad(l, fg, kg);
    f32x4 za[2][2];
#pragma unroll
    for (int i = 0; i < 2; ++i)
#pragma unroll
      for (int j = 0; j < 2; ++j) za[i][j] = f32x4{0.f, 0.f, 0.f, 0.f};
    BAR();
    if (w == 7) {
      const f32x2 t = sst[l] + sst[64 + l] + sst[128 + l] + sst[192 + l];
      const u32x2 sv = __builtin_bit_cast(u32x2, f32x2{t[0], fmaxf(t[1] - t[0] * t[0] * (1.0f / 112.0f), 0.f)});
      __builtin_amdgcn_raw_buffer_store_b64(sv, rsS, (unsigned)((t0 + l) * 8 + cb) * 8u, 0, 16);
    }
    xw_compute<false, 0, 4>(ad, smem, za);
    if (w == 7) {
      WAITV(0);
      if (l == 0) __hip_atomic_store((gu64*)sflag + (rb * 8 + cb) * 32, tag, RLX_AGENT);
    }
    BAR(); xw_compute<false, 4, 4>(ad, smem, za);
    BAR(); xw_compute<false, 8, 4>(ad, smem, za);
    if (w == 0) wait_flags(sflag + rb * 8 * 32, 8, l, tag);
    BAR();
    STAMP(0, 4);
    u32x4 sp[4];
#pragma unroll
    for (int i = 0; i < 4; ++i)
      sp[i] = __builtin_amdgcn_raw_buffer_load_b128(rsS, (unsigned)(t0 + 32 * kg + (l & 31)) * 64u + 16u * i, 0, 16);
    xw_compute<false, 12, 2>(ad, smem, za);
#pragma unroll
    for (int i = 0; i < 2; ++i)
#pragma unroll
      for (int j = 0; j < 2; ++j) {
        z1p[i][j] = u32x2{gelu_pair(za[i][j][0], za[i][j][1]), gelu_pair(za[i][j][2], za[i][j][3])};
        asm volatile("" : "+v"(z1p[i][j]));
      }
    f32x2* mrs = (f32x2*)(smem + XW_LDS + 2048 + w * 256);
    float sum = 0.f, m2 = 0.f;
#pragma unroll
    for (int i = 0; i < 4; ++i) {
      const f32x4 q = __builtin_bit_cast(f32x4, sp[i]);
      sum += q[0] + q[2]; m2 += q[1] + q[3];
    }
    const float mean = sum * (1.0f / 896.0f);
#pragma unroll
    for (int i = 0; i < 4; ++i) {
      const f32x4 q = __builtin_bit_cast(f32x4, sp[i]);
      const float d0 = q[0] * (1.0f / 112.0f) - mean, d1 = q[2] * (1.0f / 112.0f) - mean;
      m2 += 112.0f * (d0 * d0 + d1 * d1);
    }
    const float var = bfr(m2 * (1.0f / 896.0f));
    const float den = bfr(sqrtf(bfr(var + 1e-5f)));
    mrs[l & 31] = f32x2{bfr(mean), 1.0f / den};
    const unsigned zoff = (unsigned)(((cb * 7 + 2 * fg) * 64 + 2 * rb + kg) * 64 + l) * 16u;
#pragma unroll
    for (int jj = 0; jj < 2; ++jj) {
      u32x4 v;
#pragma unroll
      for (int e = 0; e < 4; ++e) {
        const f32x4 mr = *(const f32x4*)(mrs + 8 * g + 2 * e);
        v[e] = pack2(bfr(bf_lo(zp[jj][e]) - mr[0]) * mr[1], bfr(bf_hi(zp[jj][e]) - mr[2]) * mr[3]);
      }
      __builtin_amdgcn_raw_buffer_store_b128(v, jj ? rsN1 : rsN, zoff + 65536u * jj, 0, 16);
    }
    WAITV(0);
    BAR();
    if (w == 0) {
      if (l == 0) __hip_atomic_store((gu64*)zflag + (cb * 32 + rb) * 32, tag, RLX_AGENT);
      wait_flags(zflag + cb * 32 * 32, 32, l, tag);
    }
  }
  STAMP(0, 5);

  const unsigned boff = (unsigned)(((cb * 7 + 2 * fg) * 64 + kg) * 64 + l) * 16u;
  u32x4 bst[4][4];
#define LOAD_B(slot, st)                                                                          \
  {                                                                                               \
    const int sk_ = ((st) + koff) & 15;                                                           \
    bst[slot][0] = __builtin_amdgcn_raw_buffer_load_b128(rsN, boff, 4096 * sk_, 16);              \
    bst[slot][1] = __builtin_amdgcn_raw_buffer_load_b128(rsN1, boff + 65536, 4096 * sk_, 16);     \
    bst[slot][2] = __builtin_amdgcn_raw_buffer_load_b128(rsN, boff + 2048, 4096 * sk_, 16);       \
    bst[slot][3] = __builtin_amdgcn_raw_buffer_load_b128(rsN1, boff + 65536 + 2048, 4096 * sk_, 16); \
  }
  int ard[4];
#pragma unroll
  for (int i = 0; i < 4; ++i) ard[i] = (16 * ((i + 2 * kg) & 3) + c16) * 256 + (((4 * kg + g) ^ c16) * 16);
  f32x4 acc[4][2];
#pragma unroll
  for (int i = 0; i < 4; ++i)
#pragma unroll
    for (int j = 0; j < 2; ++j) acc[i][j] = f32x4{0.f, 0.f, 0.f, 0.f};
  bf16x8 af[2][4];
#define READ_A(st, h)                                                                                    \
  {                                                                                                      \
    const char* ab = smem + ring_slot(st);                                                               \
    _Pragma("unroll") for (int i = 0; i < 4; ++i) af[h][i] = *(const bf16x8*)(ab + (ard[i] ^ (128 * (h)))); \
    __builtin_amdgcn_sched_barrier(0);                                                                   \
  }
#define MMA(u, h)                                                                                        \
  {                                                                                                      \
    _Pragma("unroll") for (int j = 0; j < 2; ++j) {                                                      \
      const bf16x8 zf = __builtin_bit_cast(bf16x8, bst[u][2 * (h) + j]);                                 \
      _Pragma("unroll") for (int i = 0; i < 4; ++i)                                                      \
        acc[i][j] = __builtin_amdgcn_mfma_f32_16x16x32_bf16(zf, af[h][i], acc[i][j], 0, 0, 0);           \
    }                                                                                                    \
    __builtin_amdgcn_sched_barrier(0);                                                                   \
  }
#define ITER(it)                                                \
  {                                                             \
    BAR();                                                      \
    READ_A(it, 0)                                               \
    MMA(((it) + 3) & 3, 1)                                      \
    READ_A(it, 1)                                               \
    if ((it) + 3 < 16) { LOAD_B(((it) + 3) & 3, (it) + 3) }     \
    MMA((it) & 3, 0)                                            \
  }
  BAR();
  STAMP(0, 6);
  LOAD_B(0, 0) LOAD_B(1, 1) LOAD_B(2, 2) LOAD_B(3, 3)
  READ_A(0, 0)
  READ_A(0, 1)
  MMA(0, 0)
  STAMP(0, 7);
  ITER(1) ITER(2) ITER(3) ITER(4) ITER(5) ITER(6) ITER(7) ITER(8)
  STAMP(0, 8);
  ITER(9) ITER(10) ITER(11) ITER(12) ITER(13)
  int lz = threadIdx.x & 63;
  asm volatile("" : "+v"(lz));
  float bias[2];
#pragma unroll
  for (int i = 0; i < 2; ++i) bias[i] = bf_lo(b_sp[(rb * 4 + 2 * kg + i) * 16 + (lz & 15)]);
  ITER(14) ITER(15)
  MMA(3, 1)
  STAMP(0, 9);
  BAR();
  char* redw = smem + ring_slot(14 + (w >> 2 ^ 1)) + (w & 3) * 4096 + lz * 16;
  const char* redr = smem + ring_slot(14 + (w >> 2)) + (w & 3) * 4096 + lz * 16;
#pragma unroll
  for (int i = 0; i < 2; ++i)
#pragma unroll
    for (int j = 0; j < 2; ++j) *(f32x4*)(redw + (2 * i + j) * 1024) = acc[2 + i][j];
  BAR();
  const rsrc_t rsH = make_rsrc(hF, S_ * FH * 2);
#pragma unroll
  for (int i = 0; i < 2; ++i) {
    u32x2 hv[2];
#pragma unroll
    for (int j = 0; j < 2; ++j) {
      const f32x4 a = acc[i][j] + *(const f32x4*)(redr + (2 * i + j) * 1024);
      const unsigned g01 = pack2(a[0], a[1]), g23 = pack2(a[2], a[3]);
      const unsigned s01 = pack2(bf_lo(g01) + bias[i], bf_hi(g01) + bias[i]);
      const unsigned s23 = pack2(bf_lo(g23) + bias[i], bf_hi(g23) + bias[i]);
      hv[j] = u32x2{pack2(bf_lo(z1p[i][j][0]) * bf_lo(s01), bf_hi(z1p[i][j][0]) * bf_hi(s01)),
                    pack2(bf_lo(z1p[i][j][1]) * bf_lo(s23), bf_hi(z1p[i][j][1]) * bf_hi(s23))};
    }
    const unsigned ho = (unsigned)(((rb * 4 + 2 * kg + i) * 28 + 7 * (cb >> 1)) * 64 + lz) * 16u;
    if (two) __builtin_amdgcn_raw_buffer_store_b128(u32x4{hv[0][0], hv[0][1], hv[1][0], hv[1][1]}, rsH, ho + (3 * (cb & 1) + fg) * 1024u, 0, 16);
    else     __builtin_amdgcn_raw_buffer_store_b64(hv[0], rsH, ho + 6 * 1024u + 8u * (cb & 1), 0, 16);
  }
  WAITV(0);
  BAR();
  STAMP(0, 10);
  if (w == 0) {
    if (lz == 0) __hip_atomic_store((gu64*)hflag + (rb * 8 + cb) * 32, tag, RLX_AGENT);
    if (cb < 7) wait_flags(hflag + rb * 8 * 32, 8, lz, tag);
  }
  BAR();
  STAMP(0, 11);
  if (cb == 7) return;

  {
    const int tq = w & 3, gz = lz >> 4, cz = lz & 15;
    const unsigned ho = (unsigned)(((rb * 4 + tq) * 28 + kg) * 64 + lz) * 16u;
    u32x4 hf[14];
#pragma unroll
    for (int i = 0; i < 14; ++i) hf[i] = __builtin_amdgcn_raw_buffer_load_b128(rsH, ho, 2048 * i, 16);
    const int rrow = 4 * gz + (cz >> 2);
    int rb4[4];
#pragma unroll
    for (int j = 0; j < 4; ++j) rb4[j] = rrow * 128 + ((j ^ ((rrow >> 1) & 3)) * 32) + 8 * (lz & 3);
    f32x4 oc[4];
#pragma unroll
    for (int j = 0; j < 4; ++j) oc[j] = f32x4{0.f, 0.f, 0.f, 0.f};
#define OUT_TILE(t) (ring_slot(((t) >> 3) + 7) + ((t) & 7) * 2048)
#define OUT_STEP(i, ks)                                                                                   \
  {                                                                                                       \
    constexpr int pc_ = (ks) / 7, e_ = (ks) % 7;                                                          \
    constexpr int tA_ = (e_ < 6) ? (14 * pc_ + 7 * (e_ / 3) + 2 * (e_ % 3)) : (14 * pc_ + 6);             \
    constexpr int tB_ = (e_ < 6) ? (tA_ + 1) : (14 * pc_ + 13);                                           \
    _Pragma("unroll") for (int j = 0; j < 4; ++j) {                                                       \
      const bf16x8 wf = tr_frag(smem + rb4[j] + OUT_TILE(tA_), smem + rb4[j] + OUT_TILE(tB_));            \
      oc[j] = __builtin_amdgcn_mfma_f32_16x16x32_bf16(wf, __builtin_bit_cast(bf16x8, hf[i]), oc[j], 0, 0, 0); \
    }                                                                                                     \
  }
#define OUT_ALL(par)                                                                                       \
  OUT_STEP(0, par) OUT_STEP(1, 2 + par) OUT_STEP(2, 4 + par) OUT_STEP(3, 6 + par) OUT_STEP(4, 8 + par)     \
  OUT_STEP(5, 10 + par) OUT_STEP(6, 12 + par) OUT_STEP(7, 14 + par) OUT_STEP(8, 16 + par) OUT_STEP(9, 18 + par) \
  OUT_STEP(10, 20 + par) OUT_STEP(11, 22 + par) OUT_STEP(12, 24 + par) OUT_STEP(13, 26 + par)
    if (kg == 0) { OUT_ALL(0) } else { OUT_ALL(1) }
    char* red = smem + ring_slot(14);
    *(f32x4*)(red + (w ^ 4) * 2048 + lz * 16) = kg ? oc[0] : oc[2];
    *(f32x4*)(red + (w ^ 4) * 2048 + 1024 + lz * 16) = kg ? oc[1] : oc[3];
    BAR();
    const f32x4 a0 = (kg ? oc[2] : oc[0]) + *(const f32x4*)(red + w * 2048 + lz * 16);
    const f32x4 a1 = (kg ? oc[3] : oc[1]) + *(const f32x4*)(red + w * 2048 + 1024 + lz * 16);
    u16* op = out + (size_t)((rb * 4 + tq) * 16 + cz) * D_ + cb * 64 + 32 * kg + 4 * gz;
    *(u32x2*)op = u32x2{pack2(a0[0], a0[1]), pack2(a0[2], a0[3])};
    *(u32x2*)(op + 16) = u32x2{pack2(a1[0], a1[1]), pack2(a1[2], a1[3])};
  }
  if (bid == 0 && tid == 0) __hip_atomic_store((gu64*)sync, tag, RLX_AGENT);
  STAMP(0, 12); STAMPR(0, 15);
}

extern "C" void kernel_launch(void* const* d_in, const int* in_sizes, int n_in, void* d_out, int out_size,
                              void* d_ws, size_t ws_size, hipStream_t stream) {
  const u16* x = (const u16*)d_in[0];
  const u16* w_in = (const u16*)d_in[1];
  const u16* w_sp = (const u16*)d_in[2];
  const u16* b_sp = (const u16*)d_in[3];
  const u16* w_out = (const u16*)d_in[4];
  char* ws = (char*)d_ws;
  u16* z2N = (u16*)ws;
  u16* hF = (u16*)(ws + (size_t)4 * 1024 * 1024);
  float* stats = (float*)(ws + (size_t)8 * 1024 * 1024);
  u64* sync = (u64*)(ws + (size_t)9 * 1024 * 1024);
  gmlp_fused<<<256, 768, 0, stream>>>(x, w_in, w_sp, b_sp, w_out, (u16*)d_out, stats, z2N, hF, sync);
}
```

```cpp
#include <hip/hip_runtime.h>
#include <hip/hip_bf16.h>

typedef __bf16 bf16x8 __attribute__((ext_vector_type(8)));
typedef __bf16 bf16x4 __attribute__((ext_vector_type(4)));
typedef __bf16 bf16x2 __attribute__((ext_vector_type(2)));
typedef float f32x4 __attribute__((ext_vector_type(4)));
typedef float f32x2 __attribute__((ext_vector_type(2)));
typedef unsigned u32x4 __attribute__((ext_vector_type(4)));
typedef unsigned u32x2 __attribute__((ext_vector_type(2)));
typedef unsigned short u16;
#define LAS __attribute__((address_space(3)))
#define DI __device__ __forceinline__

#define STAMP(k, i)
#define STAMPR(k, i)
constexpr int S_ = 2048, D_ = 448, F_ = 1792, FH = 896;

DI float bf_lo(unsigned u) { return __builtin_bit_cast(float, u << 16); }
DI float bf_hi(unsigned u) { return __builtin_bit_cast(float, u & 0xffff0000u); }
DI unsigned pack2(float a, float b) {
  f32x2 f = {a, b};
  return __builtin_bit_cast(unsigned, __builtin_convertvector(f, bf16x2));
}
DI float bfr(float a) { return (float)(__bf16)a; }
DI float gelu_tanh(float z) {
  const float y = z * __builtin_fmaf(z * z, -0.10294324f, -2.3022082f);
  const float e = __builtin_amdgcn_exp2f(y);
  return z * __builtin_amdgcn_rcpf(1.0f + e);
}
DI bf16x8 tr_frag(const char* a0, const char* a1) {
  bf16x4 lo = __builtin_amdgcn_ds_read_tr16_b64_v4bf16((LAS bf16x4*)a0);
  bf16x4 hi = __builtin_amdgcn_ds_read_tr16_b64_v4bf16((LAS bf16x4*)a1);
  return __builtin_shufflevector(lo, hi, 0, 1, 2, 3, 4, 5, 6, 7);
}
template <int CTRL> DI float dpp_mov(float x) {
  return __builtin_bit_cast(float, __builtin_amdgcn_update_dpp(0, __builtin_bit_cast(int, x), CTRL, 0xf, 0xf, true));
}
DI float sum16(float x) {
  x += dpp_mov<0xB1>(x);
  x += dpp_mov<0x4E>(x);
  x += dpp_mov<0x141>(x);
  x += dpp_mov<0x140>(x);
  return x;
}

#define BAR()                                              \
  do {                                                     \
    asm volatile("s_waitcnt lgkmcnt(0)" ::: "memory");     \
    __builtin_amdgcn_s_barrier();                          \
    asm volatile("" ::: "memory");                         \
  } while (0)
#define WAITV(n) asm volatile("s_waitcnt vmcnt(" #n ")" ::: "memory")
typedef __amdgpu_buffer_rsrc_t rsrc_t;
DI rsrc_t make_rsrc(const void* p, unsigned bytes) { return __builtin_amdgcn_make_buffer_rsrc((void*)p, 0, bytes, 0x00020000); }
DI void blds16(rsrc_t rs, char* lds_wave_base, unsigned voff, unsigned soff) {
  __builtin_amdgcn_raw_ptr_buffer_load_lds(rs, (LAS void*)lds_wave_base, 16, voff, soff, 0, 0);
}

constexpr int XW_XB = 7 * 8192, XW_WB = 448 * 224, XW_LDS = XW_XB + XW_WB;

DI int x_swz(int row) { return ((row >> 1) & 1) | (((row >> 3) & 1) << 1) | ((((row >> 2) ^ (row >> 4)) & 1) << 2); }

struct XwLoader {
  rsrc_t rx, rw;
  unsigned xo[4], wo[7];
  int lw;
  DI XwLoader(const u16* x, const u16* w_in, int rb, int n0, int lw_, int l) : lw(lw_) {
    rx = make_rsrc(x, S_ * D_ * 2);
    rw = make_rsrc(w_in, D_ * F_ * 2);
#pragma unroll
    for (int i = 0; i < 4; ++i) {
      const int pc = 4 * i + lw, row = 8 * (pc & 7) + (l >> 3);
      xo[i] = (unsigned)((rb * 64 + row) * D_ + (pc >> 3) * 64 + (((l & 7) ^ x_swz(row)) * 8)) * 2u;
    }
#pragma unroll
    for (int i = 0; i < 7; ++i) {
      const int c = (4 * i + lw) * 64 + l, row = c / 14, cc = c - row * 14;
      wo[i] = (unsigned)(row * F_ + n0 + cc * 8) * 2u;
    }
  }
  DI void stage_x(char* smem, int s) const {
#pragma unroll
    for (int i = 0; i < (s < 3 ? 4 : 2); ++i) blds16(rx, smem + 2 * s * 8192 + (4 * i + lw) * 1024, xo[i], 256 * s);
  }
  DI void stage_w(char* smem, int s, int col0) const {
#pragma unroll
    for (int i = 0; i < (s < 3 ? 7 : 4); ++i)
      if (s < 3 || 4 * i + lw < 14) blds16(rw, smem + XW_XB + (28 * s + 4 * i + lw) * 1024, wo[i], 128 * F_ * 2 * s + col0 * 2);
  }
};
template <bool Z2> struct XwAddr {
  int xa[2], wa[2];
  DI XwAddr(int l, int fg, int mh) {
    const int g = l >> 4, c16 = l & 15, q = c16 >> 2, p = l & 3;
#pragma unroll
    for (int rt = 0; rt < 2; ++rt) {
      const int row = 32 * mh + (Z2 ? (8 * q + 4 * rt + p) : (16 * rt + c16));
      xa[rt] = row * 128 + (((g >> 1) ^ x_swz(row)) * 16) + (g & 1) * 8;
    }
#pragma unroll
    for (int jj = 0; jj < 2; ++jj) wa[jj] = XW_XB + (4 * g + q) * 224 + min(2 * fg + jj, 6) * 32 + 8 * p;
  }
};
template <int OFF> DI u32x2 lds_read64(unsigned addr) {
  u32x2 r;
  asm volatile("ds_read_b64 %0, %1 offset:%2" : "=v"(r) : "v"(addr), "i"(OFF) : "memory");
  return r;
}
template <bool Z2, int KS0, int NKS>
DI void xw_compute(const XwAddr<Z2>& ad, const char* smem, f32x4 (&acc)[2][2]) {
  u32x2 xl[NKS][2], xh[NKS][2];
  bf16x8 wf[NKS][2];
  const unsigned sb = (unsigned)(size_t)smem;
#pragma unroll
  for (int k = 0; k < NKS; ++k) {
    constexpr int dummy = 0; (void)dummy;
#pragma unroll
    for (int rt = 0; rt < 2; ++rt) {
      const unsigned o = sb + (ad.xa[rt] ^ (64 * ((KS0 + k) & 1)));
      if (k < 2) { xl[k][rt] = lds_read64<8192 * (KS0 >> 1)>(o); xh[k][rt] = lds_read64<8192 * (KS0 >> 1)>(o ^ 32); }
      else       { xl[k][rt] = lds_read64<8192 * (KS0 >> 1) + 8192>(o); xh[k][rt] = lds_read64<8192 * (KS0 >> 1) + 8192>(o ^ 32); }
    }
#pragma unroll
    for (int jj = 0; jj < 2; ++jj) {
      const char* a = smem + ad.wa[jj] + 32 * (KS0 + k) * 224;
      wf[k][jj] = tr_frag(a, a + 16 * 224);
    }
  }
  asm volatile("s_waitcnt lgkmcnt(0)" ::: "memory");
  __builtin_amdgcn_sched_barrier(0);
#pragma unroll
  for (int k = 0; k < NKS; ++k)
#pragma unroll
    for (int rt = 0; rt < 2; ++rt) {
      const bf16x8 xf = __builtin_bit_cast(bf16x8, u32x4{xl[k][rt][0], xl[k][rt][1], xh[k][rt][0], xh[k][rt][1]});
#pragma unroll
      for (int jj = 0; jj < 2; ++jj) {
        if (Z2) acc[rt][jj] = __builtin_amdgcn_mfma_f32_16x16x32_bf16(xf, wf[k][jj], acc[rt][jj], 0, 0, 0);
        else    acc[rt][jj] = __builtin_amdgcn_mfma_f32_16x16x32_bf16(wf[k][jj], xf, acc[rt][jj], 0, 0, 0);
      }
    }
}
DI unsigned gelu_pair(float a, float b) {
  const unsigned u = pack2(a, b);
  const f32x2 z = {bf_lo(u), bf_hi(u)};
  const f32x2 y = z * __builtin_elementwise_fma(z * z, f32x2{-0.10294324f, -0.10294324f}, f32x2{-2.3022082f, -2.3022082f});
  const f32x2 d = f32x2{__builtin_amdgcn_exp2f(y[0]), __builtin_amdgcn_exp2f(y[1])} + f32x2{1.0f, 1.0f};
  const f32x2 o = z * f32x2{__builtin_amdgcn_rcpf(d[0]), __builtin_amdgcn_rcpf(d[1])};
  return pack2(o[0], o[1]);
}

DI void tile_of_block(int bid, int& rb, int& cb) {
  const int xcd = bid & 7, idx = bid >> 3;
  rb = (xcd >> 1) * 8 + (idx & 7);
  cb = (xcd & 1) * 4 + (idx >> 3);
}

typedef unsigned long long u64;
typedef __attribute__((address_space(1))) u64 gu64;
#define RLX_AGENT __ATOMIC_RELAXED, __HIP_MEMORY_SCOPE_AGENT
DI constexpr int ring_slot(int st) {
  return 16384 * ((st % 9) == 0 ? 0 : (st % 9) == 1 ? 4 : (st % 9) == 2 ? 1 : (st % 9) == 3 ? 5 : (st % 9) == 4 ? 6 :
                  (st % 9) == 5 ? 2 : (st % 9) == 6 ? 7 : (st % 9) == 7 ? 3 : 8);
}
DI void wait_flags(const u64* f, int n, int l, u64 tag) {
  const gu64* fp = (const gu64*)f + (l < n ? l : 0) * 32;
  for (unsigned spins = 0; spins < (1u << 20); ++spins) {
    if (__all(__hip_atomic_load(fp, RLX_AGENT) == tag)) break;
    __builtin_amdgcn_s_sleep(4);
  }
}

__global__ __launch_bounds__(768, 3) void gmlp_fused(const u16* __restrict__ x, const u16* __restrict__ w_in,
                                                    const u16* __restrict__ w_sp, const u16* __restrict__ b_sp,
                                                    const u16* __restrict__ w_out, u16* __restrict__ out,
                                                    float* stats, u16* z2N, u16* hF, u64* sync) {
  __shared__ __attribute__((aligned(16))) char smem[XW_LDS + 4096 + 1024];
  const int bid = blockIdx.x;
  int rb, cb;
  tile_of_block(bid, rb, cb);
  const int tid = threadIdx.x, l = tid & 63, w = __builtin_amdgcn_readfirstlane(tid >> 6);
  const int t0 = rb * 64;
  const int koff = (2 * (rb & 7) + (cb & 1)) & 15;

  if (w >= 8) {
    const int lw = w - 8;
    const XwLoader ld(x, w_in, rb, cb * 112, lw, l);
    const rsrc_t rsA = make_rsrc(w_sp, S_ * S_ * 2);
    const rsrc_t rsO = make_rsrc(w_out, cb < 7 ? FH * D_ * 2 : 0);
    unsigned ap[4];
#pragma unroll
    for (int i = 0; i < 4; ++i) {
      const int row = 4 * (4 * i + lw) + (l >> 4);
      ap[i] = (unsigned)((t0 + row) * S_ + (((l & 15) ^ (row & 15)) * 8)) * 2u;
    }
    const unsigned oo = (unsigned)((8 * lw + (l >> 3)) * D_ + cb * 64 + (((l & 7) ^ (((l >> 4) & 3) << 1)) * 8)) * 2u;
#define LOAD_W(st)                                                                                \
  {                                                                                               \
    const int sk_ = ((st) + koff) & 15;                                                           \
    char* dst_ = smem + ring_slot(st) + lw * 1024;                                                \
    _Pragma("unroll") for (int i = 0; i < 4; ++i) blds16(rsA, dst_ + i * 4096, ap[i], 256 * sk_); \
  }
#define LOAD_O(c)        \
  {                                                                                                           \
    char* dst_ = smem + ring_slot((c) + 7) + lw * 1024;                                                       \
    _Pragma("unroll") for (int i = 0; i < 4; ++i) blds16(rsO, dst_ + i * 4096, oo, (128 * (c) + 32 * i) * D_ * 2); \
  }
    ld.stage_x(smem, 0); ld.stage_w(smem, 0, FH);
    ld.stage_x(smem, 1); ld.stage_w(smem, 1, FH);
    WAITV(11); BAR();
    ld.stage_x(smem, 2); ld.stage_w(smem, 2, FH);
    ld.stage_x(smem, 3); ld.stage_w(smem, 3, FH);
    {
      const int c = bid * 196 + min(lw * 64 + l, 195);
      blds16(ld.rw, smem + XW_LDS + 4096, (unsigned)((c / 112) * F_ + (c % 112) * 8) * 2u, 0);
    }
    WAITV(17); BAR();
    ld.stage_w(smem, 0, 0);
    WAITV(13); BAR();
    ld.stage_w(smem, 1, 0);
    WAITV(15); BAR();
    ld.stage_w(smem, 2, 0);
    WAITV(14); BAR();
    ld.stage_w(smem, 3, 0);
#pragma unroll
    for (int i = 0; i < 8; ++i) blds16(rsA, smem + XW_LDS + 4096, (unsigned)(lw * 64 + l) * 16u, bid * 32768 + i * 4096);
    WAITV(18); BAR();
    LOAD_W(0) LOAD_W(1)
    WAITV(19); BAR();
    LOAD_W(2) LOAD_W(3) LOAD_W(4)
    WAITV(28); BAR();
    LOAD_W(5) LOAD_W(6)
    BAR();
    LOAD_W(7) LOAD_W(8)
    WAITV(32); BAR();
    WAITV(28); BAR();
#pragma unroll
    for (int st = 1; st < 8; ++st) {
      LOAD_W(st + 8)
      WAITV(28); BAR();
    }
#pragma unroll
    for (int c = 0; c < 7; ++c) {
      LOAD_O(c)
      WAITV(28); BAR();
    }
    BAR();
    WAITV(0); BAR();
    return;
  }

  const int fg = w & 3, kg = w >> 2, g = l >> 4, c16 = l & 15;
  const bool two = fg < 3;
  STAMPR(0, 0); STAMP(0, 1);
  const u64 tag = __hip_atomic_load((const gu64*)sync, RLX_AGENT) + 1;
  u64* sflag = sync + 32;
  u64* zflag = sflag + 256 * 32;
  u64* hflag = zflag + 256 * 32;
  const rsrc_t rsS = make_rsrc(stats, S_ * 8 * 8);
  const rsrc_t rsN = make_rsrc(z2N, S_ * FH * 2);
  const rsrc_t rsN1 = make_rsrc(z2N, two ? S_ * FH * 2 : 0);
  f32x2* sst = (f32x2*)(smem + XW_LDS);

  u32x4 zp[2];
  {
    const XwAddr<true> ad(l, fg, kg);
    f32x4 acc[2][2];
#pragma unroll
    for (int rt = 0; rt < 2; ++rt)
#pragma unroll
      for (int jj = 0; jj < 2; ++jj) acc[rt][jj] = f32x4{0.f, 0.f, 0.f, 0.f};
    BAR(); STAMP(0, 2); xw_compute<true, 0, 4>(ad, smem, acc);
    BAR(); xw_compute<true, 4, 4>(ad, smem, acc);
    BAR(); xw_compute<true, 8, 4>(ad, smem, acc);
    BAR(); xw_compute<true, 12, 2>(ad, smem, acc);
#pragma unroll
    for (int jj = 0; jj < 2; ++jj)
      zp[jj] = u32x4{gelu_pair(acc[0][jj][0], acc[0][jj][1]), gelu_pair(acc[0][jj][2], acc[0][jj][3]),
                     gelu_pair(acc[1][jj][0], acc[1][jj][1]), gelu_pair(acc[1][jj][2], acc[1][jj][3])};
    if (!two) zp[1] = u32x4{0u, 0u, 0u, 0u};
    float v[16];
#pragma unroll
    for (int e = 0; e < 4; ++e) {
      const f32x2 a0 = {bf_lo(zp[0][e]), bf_hi(zp[0][e])}, a1 = {bf_lo(zp[1][e]), bf_hi(zp[1][e])};
      const f32x2 s1 = a0 + a1, s2 = __builtin_elementwise_fma(a0, a0, a1 * a1);
      v[4 * e] = s1[0]; v[4 * e + 1] = s2[0]; v[4 * e + 2] = s1[1]; v[4 * e + 3] = s2[1];
    }
#pragma unroll
    for (int i = 0; i < 8; ++i) { const bool b = c16 & 1; v[i] = (b ? v[i + 8] : v[i]) + dpp_mov<0xB1>(b ? v[i] : v[i + 8]); }
#pragma unroll
    for (int i = 0; i < 4; ++i) { const bool b = c16 & 2; v[i] = (b ? v[i + 4] : v[i]) + dpp_mov<0x4E>(b ? v[i] : v[i + 4]); }
#pragma unroll
    for (int i = 0; i < 2; ++i) { const bool b = c16 & 4; v[i] = (b ? v[i + 2] : v[i]) + dpp_mov<0x124>(b ? v[i] : v[i + 2]); }
    { const bool b = c16 & 8; v[0] = (b ? v[1] : v[0]) + dpp_mov<0x128>(b ? v[0] : v[1]); }
    const int idx = ((c16 & 1) << 3) | ((c16 & 2) << 1) | ((c16 & 4) >> 1) | (c16 >> 3);
    ((float*)(sst + fg * 64 + 32 * kg + 8 * g))[idx] = v[0];
  }
  STAMP(0, 3);

  u32x2 z1p[2][2];
  {
    const XwAddr<false> ad(l, fg, kg);
    f32x4 za[2][2];
#pragma unroll
    for (int i = 0; i < 2; ++i)
#pragma unroll
      for (int j = 0; j < 2; ++j) za[i][j] = f32x4{0.f, 0.f, 0.f, 0.f};
    BAR();
    if (w == 7) {
      const f32x2 t = sst[l] + sst[64 + l] + sst[128 + l] + sst[192 + l];
      const u32x2 sv = __builtin_bit_cast(u32x2, f32x2{t[0], fmaxf(t[1] - t[0] * t[0] * (1.0f / 112.0f), 0.f)});
      __builtin_amdgcn_raw_buffer_store_b64(sv, rsS, (unsigned)((t0 + l) * 8 + cb) * 8u, 0, 16);
    }
    xw_compute<false, 0, 4>(ad, smem, za);
    if (w == 7) {
      WAITV(0);
      if (l == 0) __hip_atomic_store((gu64*)sflag + (rb * 8 + cb) * 32, tag, RLX_AGENT);
    }
    BAR(); xw_compute<false, 4, 4>(ad, smem, za);
    if (w == 0) wait_flags(sflag + rb * 8 * 32, 8, l, tag);
    BAR();
    STAMP(0, 4);
    u32x4 sp[4];
#pragma unroll
    for (int i = 0; i < 4; ++i)
      sp[i] = __builtin_amdgcn_raw_buffer_load_b128(rsS, (unsigned)(t0 + 32 * kg + (l & 31)) * 64u + 16u * i, 0, 16);
    xw_compute<false, 8, 4>(ad, smem, za);
    f32x2* mrs = (f32x2*)(smem + XW_LDS + 2048 + w * 256);
    float sum = 0.f, m2 = 0.f;
#pragma unroll
    for (int i = 0; i < 4; ++i) {
      const f32x4 q = __builtin_bit_cast(f32x4, sp[i]);
      sum += q[0] + q[2]; m2 += q[1] + q[3];
    }
    const float mean = sum * (1.0f / 896.0f);
#pragma unroll
    for (int i = 0; i < 4; ++i) {
      const f32x4 q = __builtin_bit_cast(f32x4, sp[i]);
      const float d0 = q[0] * (1.0f / 112.0f) - mean, d1 = q[2] * (1.0f / 112.0f) - mean;
      m2 += 112.0f * (d0 * d0 + d1 * d1);
    }
    const float var = bfr(m2 * (1.0f / 896.0f));
    const float den = bfr(sqrtf(bfr(var + 1e-5f)));
    mrs[l & 31] = f32x2{bfr(mean), 1.0f / den};
    const unsigned zoff = (unsigned)(((cb * 7 + 2 * fg) * 64 + 2 * rb + kg) * 64 + l) * 16u;
#pragma unroll
    for (int jj = 0; jj < 2; ++jj) {
      u32x4 v;
#pragma unroll
      for (int e = 0; e < 4; ++e) {
        const f32x4 mr = *(const f32x4*)(mrs + 8 * g + 2 * e);
        v[e] = pack2(bfr(bf_lo(zp[jj][e]) - mr[0]) * mr[1], bfr(bf_hi(zp[jj][e]) - mr[2]) * mr[3]);
      }
      __builtin_amdgcn_raw_buffer_store_b128(v, jj ? rsN1 : rsN, zoff + 65536u * jj, 0, 16);
    }
    BAR();
    xw_compute<false, 12, 2>(ad, smem, za);
#pragma unroll
    for (int i = 0; i < 2; ++i)
#pragma unroll
      for (int j = 0; j < 2; ++j) {
        z1p[i][j] = u32x2{gelu_pair(za[i][j][0], za[i][j][1]), gelu_pair(za[i][j][2], za[i][j][3])};
        asm volatile("" : "+v"(z1p[i][j]));
      }
    WAITV(0);
    BAR();
    if (w == 0) {
      if (l == 0) __hip_atomic_store((gu64*)zflag + (cb * 32 + rb) * 32, tag, RLX_AGENT);
      wait_flags(zflag + cb * 32 * 32, 32, l, tag);
    }
  }
  STAMP(0, 5);

  const unsigned boff = (unsigned)(((cb * 7 + 2 * fg) * 64 + kg) * 64 + l) * 16u;
  u32x4 bst[4][4];
#define LOAD_B(slot, st)                                                                          \
  {                                                                                               \
    const int sk_ = ((st) + koff) & 15;                                                           \
    bst[slot][0] = __builtin_amdgcn_raw_buffer_load_b128(rsN, boff, 4096 * sk_, 16);              \
    bst[slot][1] = __builtin_amdgcn_raw_buffer_load_b128(rsN1, boff + 65536, 4096 * sk_, 16);     \
    bst[slot][2] = __builtin_amdgcn_raw_buffer_load_b128(rsN, boff + 2048, 4096 * sk_, 16);       \
    bst[slot][3] = __builtin_amdgcn_raw_buffer_load_b128(rsN1, boff + 65536 + 2048, 4096 * sk_, 16); \
  }
  int ard[4];
#pragma unroll
  for (int i = 0; i < 4; ++i) ard[i] = (16 * ((i + 2 * kg) & 3) + c16) * 256 + (((4 * kg + g) ^ c16) * 16);
  f32x4 acc[4][2];
#pragma unroll
  for (int i = 0; i < 4; ++i)
#pragma unroll
    for (int j = 0; j < 2; ++j) acc[i][j] = f32x4{0.f, 0.f, 0.f, 0.f};
  bf16x8 af[2][4];
#define READ_A(st, h)                                                                                    \
  {                                                                                                      \
    const char* ab = smem + ring_slot(st);                                                               \
    _Pragma("unroll") for (int i = 0; i < 4; ++i) af[h][i] = *(const bf16x8*)(ab + (ard[i] ^ (128 * (h)))); \
    __builtin_amdgcn_sched_barrier(0);                                                                   \
  }
#define MMA(u, h)                                                                                        \
  {                                                                                                      \
    _Pragma("unroll") for (int j = 0; j < 2; ++j) {                                                      \
      const bf16x8 zf = __builtin_bit_cast(bf16x8, bst[u][2 * (h) + j]);                                 \
      _Pragma("unroll") for (int i = 0; i < 4; ++i)                                                      \
        acc[i][j] = __builtin_amdgcn_mfma_f32_16x16x32_bf16(zf, af[h][i], acc[i][j], 0, 0, 0);           \
    }                                                                                                    \
    __builtin_amdgcn_sched_barrier(0);                                                                   \
  }
#define ITER(it)                                                \
  {                                                             \
    BAR();                                                      \
    READ_A(it, 0)                                               \
    MMA(((it) + 3) & 3, 1)                                      \
    READ_A(it, 1)                                               \
    if ((it) + 3 < 16) { LOAD_B(((it) + 3) & 3, (it) + 3) }     \
    MMA((it) & 3, 0)                                            \
  }
  BAR();
  STAMP(0, 6);
  LOAD_B(0, 0) LOAD_B(1, 1) LOAD_B(2, 2) LOAD_B(3, 3)
  READ_A(0, 0)
  READ_A(0, 1)
  MMA(0, 0)
  STAMP(0, 7);
  ITER(1) ITER(2) ITER(3) ITER(4) ITER(5) ITER(6) ITER(7) ITER(8)
  STAMP(0, 8);
  ITER(9) ITER(10) ITER(11) ITER(12) ITER(13)
  int lz = threadIdx.x & 63;
  asm volatile("" : "+v"(lz));
  float bias[2];
#pragma unroll
  for (int i = 0; i < 2; ++i) bias[i] = bf_lo(b_sp[(rb * 4 + 2 * kg + i) * 16 + (lz & 15)]);
  ITER(14) ITER(15)
  MMA(3, 1)
  STAMP(0, 9);
  BAR();
  char* redw = smem + ring_slot(14 + (w >> 2 ^ 1)) + (w & 3) * 4096 + lz * 16;
  const char* redr = smem + ring_slot(14 + (w >> 2)) + (w & 3) * 4096 + lz * 16;
#pragma unroll
  for (int i = 0; i < 2; ++i)
#pragma unroll
    for (int j = 0; j < 2; ++j) *(f32x4*)(redw + (2 * i + j) * 1024) = acc[2 + i][j];
  BAR();
  const rsrc_t rsH = make_rsrc(hF, S_ * FH * 2);
#pragma unroll
  for (int i = 0; i < 2; ++i) {
    u32x2 hv[2];
#pragma unroll
    for (int j = 0; j < 2; ++j) {
      const f32x4 a = acc[i][j] + *(const f32x4*)(redr + (2 * i + j) * 1024);
      const unsigned g01 = pack2(a[0], a[1]), g23 = pack2(a[2], a[3]);
      const unsigned s01 = pack2(bf_lo(g01) + bias[i], bf_hi(g01) + bias[i]);
      const unsigned s23 = pack2(bf_lo(g23) + bias[i], bf_hi(g23) + bias[i]);
      hv[j] = u32x2{pack2(bf_lo(z1p[i][j][0]) * bf_lo(s01), bf_hi(z1p[i][j][0]) * bf_hi(s01)),
                    pack2(bf_lo(z1p[i][j][1]) * bf_lo(s23), bf_hi(z1p[i][j][1]) * bf_hi(s23))};
    }
    const unsigned ho = (unsigned)(((rb * 4 + 2 * kg + i) * 28 + 7 * (cb >> 1)) * 64 + lz) * 16u;
    if (two) __builtin_amdgcn_raw_buffer_store_b128(u32x4{hv[0][0], hv[0][1], hv[1][0], hv[1][1]}, rsH, ho + (3 * (cb & 1) + fg) * 1024u, 0, 16);
    else     __builtin_amdgcn_raw_buffer_store_b64(hv[0], rsH, ho + 6 * 1024u + 8u * (cb & 1), 0, 16);
  }
  {
    const int tq = w & 3, gz = lz >> 4, cz = lz & 15;
    const int rrow = 4 * gz + (cz >> 2);
    int rb4[4];
#pragma unroll
    for (int j = 0; j < 4; ++j) rb4[j] = rrow * 128 + ((j ^ ((rrow >> 1) & 3)) * 32) + 8 * (lz & 3);
#define OUT_TILE(t) (ring_slot(((t) >> 3) + 7) + ((t) & 7) * 2048)
#define OUT_FRAG(dst, ks)                                                                                 \
  {                                                                                                       \
    constexpr int pc_ = (ks) / 7, e_ = (ks) % 7;                                                          \
    constexpr int tA_ = (e_ < 6) ? (14 * pc_ + 7 * (e_ / 3) + 2 * (e_ % 3)) : (14 * pc_ + 6);             \
    constexpr int tB_ = (e_ < 6) ? (tA_ + 1) : (14 * pc_ + 13);                                           \
    _Pragma("unroll") for (int j = 0; j < 4; ++j)                                                         \
      dst[j] = tr_frag(smem + rb4[j] + OUT_TILE(tA_), smem + rb4[j] + OUT_TILE(tB_));                     \
  }
    bf16x8 wpre[5][4];
    if (kg == 0) { OUT_FRAG(wpre[0], 0) OUT_FRAG(wpre[1], 2) OUT_FRAG(wpre[2], 4) OUT_FRAG(wpre[3], 6) OUT_FRAG(wpre[4], 8) }
    else         { OUT_FRAG(wpre[0], 1) OUT_FRAG(wpre[1], 3) OUT_FRAG(wpre[2], 5) OUT_FRAG(wpre[3], 7) OUT_FRAG(wpre[4], 9) }
    WAITV(0);
    BAR();
    STAMP(0, 10);
    if (w == 0) {
      if (lz == 0) __hip_atomic_store((gu64*)hflag + (rb * 8 + cb) * 32, tag, RLX_AGENT);
      if (cb < 7) wait_flags(hflag + rb * 8 * 32, 8, lz, tag);
    }
    if (cb == 7) return;
    BAR();
    STAMP(0, 11);
    const unsigned ho = (unsigned)(((rb * 4 + tq) * 28 + kg) * 64 + lz) * 16u;
    u32x4 hf[14];
#pragma unroll
    for (int i = 0; i < 14; ++i) hf[i] = __builtin_amdgcn_raw_buffer_load_b128(rsH, ho, 2048 * i, 16);
    f32x4 oc[4];
#pragma unroll
    for (int j = 0; j < 4; ++j) oc[j] = f32x4{0.f, 0.f, 0.f, 0.f};
#pragma unroll
    for (int i = 0; i < 5; ++i)
#pragma unroll
      for (int j = 0; j < 4; ++j)
        oc[j] = __builtin_amdgcn_mfma_f32_16x16x32_bf16(wpre[i][j], __builtin_bit_cast(bf16x8, hf[i]), oc[j], 0, 0, 0);
#define OUT_STEP(i, ks)                                                                                   \
  {                                                                                                       \
    bf16x8 wf[4];                                                                                         \
    OUT_FRAG(wf, ks)                                                                                      \
    _Pragma("unroll") for (int j = 0; j < 4; ++j)                                                         \
      oc[j] = __builtin_amdgcn_mfma_f32_16x16x32_bf16(wf[j], __builtin_bit_cast(bf16x8, hf[i]), oc[j], 0, 0, 0); \
  }
#define OUT_ALL(par)                                                                                       \
  OUT_STEP(5, 10 + par) OUT_STEP(6, 12 + par) OUT_STEP(7, 14 + par) OUT_STEP(8, 16 + par) OUT_STEP(9, 18 + par) \
  OUT_STEP(10, 20 + par) OUT_STEP(11, 22 + par) OUT_STEP(12, 24 + par) OUT_STEP(13, 26 + par)
    if (kg == 0) { OUT_ALL(0) } else { OUT_ALL(1) }
    char* red = smem + ring_slot(14);
    *(f32x4*)(red + (w ^ 4) * 2048 + lz * 16) = kg ? oc[0] : oc[2];
    *(f32x4*)(red + (w ^ 4) * 2048 + 1024 + lz * 16) = kg ? oc[1] : oc[3];
    BAR();
    const f32x4 a0 = (kg ? oc[2] : oc[0]) + *(const f32x4*)(red + w * 2048 + lz * 16);
    const f32x4 a1 = (kg ? oc[3] : oc[1]) + *(const f32x4*)(red + w * 2048 + 1024 + lz * 16);
    u16* op = out + (size_t)((rb * 4 + tq) * 16 + cz) * D_ + cb * 64 + 32 * kg + 4 * gz;
    *(u32x2*)op = u32x2{pack2(a0[0], a0[1]), pack2(a0[2], a0[3])};
    *(u32x2*)(op + 16) = u32x2{pack2(a1[0], a1[1]), pack2(a1[2], a1[3])};
  }
  if (bid == 0 && tid == 0) __hip_atomic_store((gu64*)sync, tag, RLX_AGENT);
  STAMP(0, 12); STAMPR(0, 15);
}

extern "C" void kernel_launch(void* const* d_in, const int* in_sizes, int n_in, void* d_out, int out_size,
                              void* d_ws, size_t ws_size, hipStream_t stream) {
  const u16* x = (const u16*)d_in[0];
  const u16* w_in = (const u16*)d_in[1];
  const u16* w_sp = (const u16*)d_in[2];
  const u16* b_sp = (const u16*)d_in[3];
  const u16* w_out = (const u16*)d_in[4];
  char* ws = (char*)d_ws;
  u16* z2N = (u16*)ws;
  u16* hF = (u16*)(ws + (size_t)4 * 1024 * 1024);
  float* stats = (float*)(ws + (size_t)8 * 1024 * 1024);
  u64* sync = (u64*)(ws + (size_t)9 * 1024 * 1024);
  gmlp_fused<<<256, 768, 0, stream>>>(x, w_in, w_sp, b_sp, w_out, (u16*)d_out, stats, z2N, hF, sync);
}
```

```cpp
#include <hip/hip_runtime.h>
#include <hip/hip_bf16.h>

typedef __bf16 bf16x8 __attribute__((ext_vector_type(8)));
typedef __bf16 bf16x4 __attribute__((ext_vector_type(4)));
typedef __bf16 bf16x2 __attribute__((ext_vector_type(2)));
typedef float f32x4 __attribute__((ext_vector_type(4)));
typedef float f32x2 __attribute__((ext_vector_type(2)));
typedef unsigned u32x4 __attribute__((ext_vector_type(4)));
typedef unsigned u32x2 __attribute__((ext_vector_type(2)));
typedef unsigned short u16;
#define LAS __attribute__((address_space(3)))
#define DI __device__ __forceinline__

#define STAMP(k, i)
#define STAMPR(k, i)
constexpr int S_ = 2048, D_ = 448, F_ = 1792, FH = 896;

DI float bf_lo(unsigned u) { return __builtin_bit_cast(float, u << 16); }
DI float bf_hi(unsigned u) { return __builtin_bit_cast(float, u & 0xffff0000u); }
DI unsigned pack2(float a, float b) {
  f32x2 f = {a, b};
  return __builtin_bit_cast(unsigned, __builtin_convertvector(f, bf16x2));
}
DI float bfr(float a) { return (float)(__bf16)a; }
DI float gelu_tanh(float z) {
  const float y = z * __builtin_fmaf(z * z, -0.10294324f, -2.3022082f);
  const float e = __builtin_amdgcn_exp2f(y);
  return z * __builtin_amdgcn_rcpf(1.0f + e);
}
DI bf16x8 tr_frag(const char* a0, const char* a1) {
  bf16x4 lo = __builtin_amdgcn_ds_read_tr16_b64_v4bf16((LAS bf16x4*)a0);
  bf16x4 hi = __builtin_amdgcn_ds_read_tr16_b64_v4bf16((LAS bf16x4*)a1);
  return __builtin_shufflevector(lo, hi, 0, 1, 2, 3, 4, 5, 6, 7);
}
template <int CTRL> DI float dpp_mov(float x) {
  return __builtin_bit_cast(float, __builtin_amdgcn_update_dpp(0, __builtin_bit_cast(int, x), CTRL, 0xf, 0xf, true));
}
DI float sum16(float x) {
  x += dpp_mov<0xB1>(x);
  x += dpp_mov<0x4E>(x);
  x += dpp_mov<0x141>(x);
  x += dpp_mov<0x140>(x);
  return x;
}

#define BAR()                                              \
  do {                                                     \
    asm volatile("s_waitcnt lgkmcnt(0)" ::: "memory");     \
    __builtin_amdgcn_s_barrier();                          \
    asm volatile("" ::: "memory");                         \
  } while (0)
#define WAITV(n) asm volatile("s_waitcnt vmcnt(" #n ")" ::: "memory")
typedef __amdgpu_buffer_rsrc_t rsrc_t;
DI rsrc_t make_rsrc(const void* p, unsigned bytes) { return __builtin_amdgcn_make_buffer_rsrc((void*)p, 0, bytes, 0x00020000); }
DI void blds16(rsrc_t rs, char* lds_wave_base, unsigned voff, unsigned soff) {
  __builtin_amdgcn_raw_ptr_buffer_load_lds(rs, (LAS void*)lds_wave_base, 16, voff, soff, 0, 0);
}

constexpr int XW_XB = 7 * 8192, XW_WB = 448 * 224, XW_LDS = XW_XB + XW_WB;

DI int x_swz(int row) { return ((row >> 1) & 1) | (((row >> 3) & 1) << 1) | ((((row >> 2) ^ (row >> 4)) & 1) << 2); }

struct XwLoader {
  rsrc_t rx, rw;
  unsigned xo[4], wo[7];
  int lw;
  DI XwLoader(const u16* x, const u16* w_in, int rb, int n0, int lw_, int l) : lw(lw_) {
    rx = make_rsrc(x, S_ * D_ * 2);
    rw = make_rsrc(w_in, D_ * F_ * 2);
#pragma unroll
    for (int i = 0; i < 4; ++i) {
      const int pc = 4 * i + lw, row = 8 * (pc & 7) + (l >> 3);
      xo[i] = (unsigned)((rb * 64 + row) * D_ + (pc >> 3) * 64 + (((l & 7) ^ x_swz(row)) * 8)) * 2u;
    }
#pragma unroll
    for (int i = 0; i < 7; ++i) {
      const int c = (4 * i + lw) * 64 + l, row = c / 14, cc = c - row * 14;
      wo[i] = (unsigned)(row * F_ + n0 + cc * 8) * 2u;
    }
  }
  DI void stage_x(char* smem, int s) const {
#pragma unroll
    for (int i = 0; i < (s < 3 ? 4 : 2); ++i) blds16(rx, smem + 2 * s * 8192 + (4 * i + lw) * 1024, xo[i], 256 * s);
  }
  DI void stage_w(char* smem, int s, int col0) const {
#pragma unroll
    for (int i = 0; i < (s < 3 ? 7 : 4); ++i)
      if (s < 3 || 4 * i + lw < 14) blds16(rw, smem + XW_XB + (28 * s + 4 * i + lw) * 1024, wo[i], 128 * F_ * 2 * s + col0 * 2);
  }
};
template <bool Z2> struct XwAddr {
  int xa[2], wa[2];
  DI XwAddr(int l, int fg, int mh) {
    const int g = l >> 4, c16 = l & 15, q = c16 >> 2, p = l & 3;
#pragma unroll
    for (int rt = 0; rt < 2; ++rt) {
      const int row = 32 * mh + (Z2 ? (8 * q + 4 * rt + p) : (16 * rt + c16));
      xa[rt] = row * 128 + (((g >> 1) ^ x_swz(row)) * 16) + (g & 1) * 8;
    }
#pragma unroll
    for (int jj = 0; jj < 2; ++jj) wa[jj] = XW_XB + (4 * g + q) * 224 + min(2 * fg + jj, 6) * 32 + 8 * p;
  }
};
template <int OFF> DI u32x2 lds_read64(unsigned addr) {
  u32x2 r;
  asm volatile("ds_read_b64 %0, %1 offset:%2" : "=v"(r) : "v"(addr), "i"(OFF) : "memory");
  return r;
}
template <bool Z2, int KS0, int NKS>
DI void xw_compute(const XwAddr<Z2>& ad, const char* smem, f32x4 (&acc)[2][2]) {
  u32x2 xl[NKS][2], xh[NKS][2];
  bf16x8 wf[NKS][2];
  const unsigned sb = (unsigned)(size_t)smem;
#pragma unroll
  for (int k = 0; k < NKS; ++k) {
    constexpr int dummy = 0; (void)dummy;
#pragma unroll
    for (int rt = 0; rt < 2; ++rt) {
      const unsigned o = sb + (ad.xa[rt] ^ (64 * ((KS0 + k) & 1)));
      if (k < 2) { xl[k][rt] = lds_read64<8192 * (KS0 >> 1)>(o); xh[k][rt] = lds_read64<8192 * (KS0 >> 1)>(o ^ 32); }
      else       { xl[k][rt] = lds_read64<8192 * (KS0 >> 1) + 8192>(o); xh[k][rt] = lds_read64<8192 * (KS0 >> 1) + 8192>(o ^ 32); }
    }
#pragma unroll
    for (int jj = 0; jj < 2; ++jj) {
      const char* a = smem + ad.wa[jj] + 32 * (KS0 + k) * 224;
      wf[k][jj] = tr_frag(a, a + 16 * 224);
    }
  }
  asm volatile("s_waitcnt lgkmcnt(0)" ::: "memory");
  __builtin_amdgcn_sched_barrier(0);
#pragma unroll
  for (int k = 0; k < NKS; ++k)
#pragma unroll
    for (int rt = 0; rt < 2; ++rt) {
      const bf16x8 xf = __builtin_bit_cast(bf16x8, u32x4{xl[k][rt][0], xl[k][rt][1], xh[k][rt][0], xh[k][rt][1]});
#pragma unroll
      for (int jj = 0; jj < 2; ++jj) {
        if (Z2) acc[rt][jj] = __builtin_amdgcn_mfma_f32_16x16x32_bf16(xf, wf[k][jj], acc[rt][jj], 0, 0, 0);
        else    acc[rt][jj] = __builtin_amdgcn_mfma_f32_16x16x32_bf16(wf[k][jj], xf, acc[rt][jj], 0, 0, 0);
      }
    }
}
DI unsigned gelu_pair(float a, float b) {
  const unsigned u = pack2(a, b);
  const f32x2 z = {bf_lo(u), bf_hi(u)};
  const f32x2 y = z * __builtin_elementwise_fma(z * z, f32x2{-0.10294324f, -0.10294324f}, f32x2{-2.3022082f, -2.3022082f});
  const f32x2 d = f32x2{__builtin_amdgcn_exp2f(y[0]), __builtin_amdgcn_exp2f(y[1])} + f32x2{1.0f, 1.0f};
  const f32x2 o = z * f32x2{__builtin_amdgcn_rcpf(d[0]), __builtin_amdgcn_rcpf(d[1])};
  return pack2(o[0], o[1]);
}

DI void tile_of_block(int bid, int& rb, int& cb) {
  const int xcd = bid & 7, idx = bid >> 3;
  rb = (xcd >> 1) * 8 + (idx & 7);
  cb = (xcd & 1) * 4 + (idx >> 3);
}

typedef unsigned long long u64;
typedef __attribute__((address_space(1))) u64 gu64;
#define RLX_AGENT __ATOMIC_RELAXED, __HIP_MEMORY_SCOPE_AGENT
DI constexpr int ring_slot(int st) {
  return 16384 * ((st % 9) == 0 ? 0 : (st % 9) == 1 ? 4 : (st % 9) == 2 ? 1 : (st % 9) == 3 ? 5 : (st % 9) == 4 ? 6 :
                  (st % 9) == 5 ? 2 : (st % 9) == 6 ? 7 : (st % 9) == 7 ? 3 : 8);
}
DI void wait_flags(const u64* f, int n, int l, u64 tag) {
  const gu64* fp = (const gu64*)f + (l < n ? l : 0) * 32;
  for (unsigned spins = 0; spins < (1u << 20); ++spins) {
    if (__all(__hip_atomic_load(fp, RLX_AGENT) == tag)) break;
    __builtin_amdgcn_s_sleep(4);
  }
}

__global__ __launch_bounds__(768, 3) void gmlp_fused(const u16* __restrict__ x, const u16* __restrict__ w_in,
                                                    const u16* __restrict__ w_sp, const u16* __restrict__ b_sp,
                                                    const u16* __restrict__ w_out, u16* __restrict__ out,
                                                    float* stats, u16* z2N, u16* hF, u64* sync) {
  __shared__ __attribute__((aligned(16))) char smem[XW_LDS + 4096 + 1024];
  const int bid = blockIdx.x;
  int rb, cb;
  tile_of_block(bid, rb, cb);
  const int tid = threadIdx.x, l = tid & 63, w = __builtin_amdgcn_readfirstlane(tid >> 6);
  const int t0 = rb * 64;
  const int koff = 0;

  if (w >= 8) {
    const int lw = w - 8;
    const XwLoader ld(x, w_in, rb, cb * 112, lw, l);
    const rsrc_t rsA = make_rsrc(w_sp, S_ * S_ * 2);
    const rsrc_t rsO = make_rsrc(w_out, cb < 7 ? FH * D_ * 2 : 0);
    unsigned ap[4];
#pragma unroll
    for (int i = 0; i < 4; ++i) {
      const int row = 4 * (4 * i + lw) + (l >> 4);
      ap[i] = (unsigned)((t0 + row) * S_ + (((l & 15) ^ (row & 15)) * 8)) * 2u;
    }
    const unsigned oo = (unsigned)((8 * lw + (l >> 3)) * D_ + cb * 64 + (((l & 7) ^ (((l >> 4) & 3) << 1)) * 8)) * 2u;
#define LOAD_W(st)                                                                                \
  {                                                                                               \
    const int sk_ = ((st) + koff) & 15;                                                           \
    char* dst_ = smem + ring_slot(st) + lw * 1024;                                                \
    _Pragma("unroll") for (int i = 0; i < 4; ++i) blds16(rsA, dst_ + i * 4096, ap[i], 256 * sk_); \
  }
#define LOAD_O(c)        \
  {                                                                                                           \
    char* dst_ = smem + ring_slot((c) + 7) + lw * 1024;                                                       \
    _Pragma("unroll") for (int i = 0; i < 4; ++i) blds16(rsO, dst_ + i * 4096, oo, (128 * (c) + 32 * i) * D_ * 2); \
  }
    ld.stage_x(smem, 0); ld.stage_w(smem, 0, FH);
    ld.stage_x(smem, 1); ld.stage_w(smem, 1, FH);
    WAITV(11); BAR();
    ld.stage_x(smem, 2); ld.stage_w(smem, 2, FH);
    ld.stage_x(smem, 3); ld.stage_w(smem, 3, FH);
    {
      const int c = bid * 196 + min(lw * 64 + l, 195);
      blds16(ld.rw, smem + XW_LDS + 4096, (unsigned)((c / 112) * F_ + (c % 112) * 8) * 2u, 0);
    }
    WAITV(17); BAR();
    ld.stage_w(smem, 0, 0);
    WAITV(13); BAR();
    ld.stage_w(smem, 1, 0);
    WAITV(15); BAR();
    ld.stage_w(smem, 2, 0);
    WAITV(14); BAR();
    ld.stage_w(smem, 3, 0);
#pragma unroll
    for (int i = 0; i < 8; ++i) blds16(rsA, smem + XW_LDS + 4096, (unsigned)(lw * 64 + l) * 16u, bid * 32768 + i * 4096);
    WAITV(18); BAR();
    LOAD_W(0) LOAD_W(1)
    WAITV(19); BAR();
    LOAD_W(2) LOAD_W(3) LOAD_W(4)
    WAITV(28); BAR();
    LOAD_W(5) LOAD_W(6)
    BAR();
    LOAD_W(7) LOAD_W(8)
    WAITV(32); BAR();
    WAITV(28); BAR();
    LOAD_W(9) WAITV(28); BAR();
    WAITV(24); BAR();
    LOAD_W(10) WAITV(24); BAR();
    WAITV(20); BAR();
    LOAD_W(11) WAITV(20); BAR();
    WAITV(16); BAR();
    LOAD_W(12) WAITV(16); BAR();
    WAITV(12); BAR();
    LOAD_W(13) WAITV(12); BAR();
    LOAD_W(14) WAITV(12); BAR();
    LOAD_W(15) WAITV(12); BAR();
    LOAD_O(0) WAITV(12); BAR();
    LOAD_O(1) WAITV(12); BAR();
    LOAD_O(2) WAITV(12); BAR();
    LOAD_O(3) BAR();
    LOAD_O(4) BAR();
    LOAD_O(5) LOAD_O(6)
    WAITV(0); BAR();
    return;
  }

  const int fg = w & 3, kg = w >> 2, g = l >> 4, c16 = l & 15;
  const bool two = fg < 3;
  STAMPR(0, 0); STAMP(0, 1);
  const u64 tag = __hip_atomic_load((const gu64*)sync, RLX_AGENT) + 1;
  u64* sflag = sync + 32;
  u64* zflag = sflag + 256 * 32;
  u64* hflag = zflag + 256 * 32;
  const rsrc_t rsS = make_rsrc(stats, S_ * 8 * 8);
  const rsrc_t rsN = make_rsrc(z2N, S_ * FH * 2);
  const rsrc_t rsN1 = make_rsrc(z2N, two ? S_ * FH * 2 : 0);
  f32x2* sst = (f32x2*)(smem + XW_LDS);

  u32x4 zp[2];
  {
    const XwAddr<true> ad(l, fg, kg);
    f32x4 acc[2][2];
#pragma unroll
    for (int rt = 0; rt < 2; ++rt)
#pragma unroll
      for (int jj = 0; jj < 2; ++jj) acc[rt][jj] = f32x4{0.f, 0.f, 0.f, 0.f};
    BAR(); STAMP(0, 2); xw_compute<true, 0, 4>(ad, smem, acc);
    BAR(); xw_compute<true, 4, 4>(ad, smem, acc);
    BAR(); xw_compute<true, 8, 4>(ad, smem, acc);
    BAR(); xw_compute<true, 12, 2>(ad, smem, acc);
#pragma unroll
    for (int jj = 0; jj < 2; ++jj)
      zp[jj] = u32x4{gelu_pair(acc[0][jj][0], acc[0][jj][1]), gelu_pair(acc[0][jj][2], acc[0][jj][3]),
                     gelu_pair(acc[1][jj][0], acc[1][jj][1]), gelu_pair(acc[1][jj][2], acc[1][jj][3])};
    if (!two) zp[1] = u32x4{0u, 0u, 0u, 0u};
    float v[16];
#pragma unroll
    for (int e = 0; e < 4; ++e) {
      const f32x2 a0 = {bf_lo(zp[0][e]), bf_hi(zp[0][e])}, a1 = {bf_lo(zp[1][e]), bf_hi(zp[1][e])};
      const f32x2 s1 = a0 + a1, s2 = __builtin_elementwise_fma(a0, a0, a1 * a1);
      v[4 * e] = s1[0]; v[4 * e + 1] = s2[0]; v[4 * e + 2] = s1[1]; v[4 * e + 3] = s2[1];
    }
#pragma unroll
    for (int i = 0; i < 8; ++i) { const bool b = c16 & 1; v[i] = (b ? v[i + 8] : v[i]) + dpp_mov<0xB1>(b ? v[i] : v[i + 8]); }
#pragma unroll
    for (int i = 0; i < 4; ++i) { const bool b = c16 & 2; v[i] = (b ? v[i + 4] : v[i]) + dpp_mov<0x4E>(b ? v[i] : v[i + 4]); }
#pragma unroll
    for (int i = 0; i < 2; ++i) { const bool b = c16 & 4; v[i] = (b ? v[i + 2] : v[i]) + dpp_mov<0x124>(b ? v[i] : v[i + 2]); }
    { const bool b = c16 & 8; v[0] = (b ? v[1] : v[0]) + dpp_mov<0x128>(b ? v[0] : v[1]); }
    const int idx = ((c16 & 1) << 3) | ((c16 & 2) << 1) | ((c16 & 4) >> 1) | (c16 >> 3);
    ((float*)(sst + fg * 64 + 32 * kg + 8 * g))[idx] = v[0];
  }
  STAMP(0, 3);

  u32x2 z1p[2][2];
  {
    const XwAddr<false> ad(l, fg, kg);
    f32x4 za[2][2];
#pragma unroll
    for (int i = 0; i < 2; ++i)
#pragma unroll
      for (int j = 0; j < 2; ++j) za[i][j] = f32x4{0.f, 0.f, 0.f, 0.f};
    BAR();
    if (w == 7) {
      const f32x2 t = sst[l] + sst[64 + l] + sst[128 + l] + sst[192 + l];
      const u32x2 sv = __builtin_bit_cast(u32x2, f32x2{t[0], fmaxf(t[1] - t[0] * t[0] * (1.0f / 112.0f), 0.f)});
      __builtin_amdgcn_raw_buffer_store_b64(sv, rsS, (unsigned)((t0 + l) * 8 + cb) * 8u, 0, 16);
    }
    xw_compute<false, 0, 4>(ad, smem, za);
    if (w == 7) {
      WAITV(0);
      if (l == 0) __hip_atomic_store((gu64*)sflag + (rb * 8 + cb) * 32, tag, RLX_AGENT);
    }
    BAR(); xw_compute<false, 4, 4>(ad, smem, za);
    if (w == 0) wait_flags(sflag + rb * 8 * 32, 8, l, tag);
    BAR();
    STAMP(0, 4);
    u32x4 sp[4];
#pragma unroll
    for (int i = 0; i < 4; ++i)
      sp[i] = __builtin_amdgcn_raw_buffer_load_b128(rsS, (unsigned)(t0 + 32 * kg + (l & 31)) * 64u + 16u * i, 0, 16);
    xw_compute<false, 8, 4>(ad, smem, za);
    f32x2* mrs = (f32x2*)(smem + XW_LDS + 2048 + w * 256);
    float sum = 0.f, m2 = 0.f;
#pragma unroll
    for (int i = 0; i < 4; ++i) {
      const f32x4 q = __builtin_bit_cast(f32x4, sp[i]);
      sum += q[0] + q[2]; m2 += q[1] + q[3];
    }
    const float mean = sum * (1.0f / 896.0f);
#pragma unroll
    for (int i = 0; i < 4; ++i) {
      const f32x4 q = __builtin_bit_cast(f32x4, sp[i]);
      const float d0 = q[0] * (1.0f / 112.0f) - mean, d1 = q[2] * (1.0f / 112.0f) - mean;
      m2 += 112.0f * (d0 * d0 + d1 * d1);
    }
    const float var = bfr(m2 * (1.0f / 896.0f));
    const float den = bfr(sqrtf(bfr(var + 1e-5f)));
    mrs[l & 31] = f32x2{bfr(mean), 1.0f / den};
    const unsigned zoff = (unsigned)(((cb * 7 + 2 * fg) * 64 + 2 * rb + kg) * 64 + l) * 16u;
#pragma unroll
    for (int jj = 0; jj < 2; ++jj) {
      u32x4 v;
#pragma unroll
      for (int e = 0; e < 4; ++e) {
        const f32x4 mr = *(const f32x4*)(mrs + 8 * g + 2 * e);
        v[e] = pack2(bfr(bf_lo(zp[jj][e]) - mr[0]) * mr[1], bfr(bf_hi(zp[jj][e]) - mr[2]) * mr[3]);
      }
      __builtin_amdgcn_raw_buffer_store_b128(v, jj ? rsN1 : rsN, zoff + 65536u * jj, 0, 16);
    }
    BAR();
    xw_compute<false, 12, 2>(ad, smem, za);
#pragma unroll
    for (int i = 0; i < 2; ++i)
#pragma unroll
      for (int j = 0; j < 2; ++j) {
        z1p[i][j] = u32x2{gelu_pair(za[i][j][0], za[i][j][1]), gelu_pair(za[i][j][2], za[i][j][3])};
        asm volatile("" : "+v"(z1p[i][j]));
      }
    WAITV(0);
    BAR();
    if (w == 0) {
      if (l == 0) __hip_atomic_store((gu64*)zflag + (cb * 32 + rb) * 32, tag, RLX_AGENT);
      wait_flags(zflag + cb * 32 * 32, 32, l, tag);
    }
  }
  STAMP(0, 5);

  const unsigned boff = (unsigned)(((cb * 7 + 2 * fg) * 64 + kg) * 64 + l) * 16u;
  u32x4 bst[4][4];
#define LOAD_B(slot, st)                                                                          \
  {                                                                                               \
    const int sk_ = ((st) + koff) & 15;                                                           \
    bst[slot][0] = __builtin_amdgcn_raw_buffer_load_b128(rsN, boff, 4096 * sk_, 16);              \
    bst[slot][1] = __builtin_amdgcn_raw_buffer_load_b128(rsN1, boff + 65536, 4096 * sk_, 16);     \
    bst[slot][2] = __builtin_amdgcn_raw_buffer_load_b128(rsN, boff + 2048, 4096 * sk_, 16);       \
    bst[slot][3] = __builtin_amdgcn_raw_buffer_load_b128(rsN1, boff + 65536 + 2048, 4096 * sk_, 16); \
  }
  int ard[4];
#pragma unroll
  for (int i = 0; i < 4; ++i) ard[i] = (16 * ((i + 2 * kg) & 3) + c16) * 256 + (((4 * kg + g) ^ c16) * 16);
  f32x4 acc[4][2];
#pragma unroll
  for (int i = 0; i < 4; ++i)
#pragma unroll
    for (int j = 0; j < 2; ++j) acc[i][j] = f32x4{0.f, 0.f, 0.f, 0.f};
  bf16x8 af[2][4];
#define READ_A(st, h)                                                                                    \
  {                                                                                                      \
    const char* ab = smem + ring_slot(st);                                                               \
    _Pragma("unroll") for (int i = 0; i < 4; ++i) af[h][i] = *(const bf16x8*)(ab + (ard[i] ^ (128 * (h)))); \
    __builtin_amdgcn_sched_barrier(0);                                                                   \
  }
#define MMA(u, h)                                                                                        \
  {                                                                                                      \
    _Pragma("unroll") for (int j = 0; j < 2; ++j) {                                                      \
      const bf16x8 zf = __builtin_bit_cast(bf16x8, bst[u][2 * (h) + j]);                                 \
      _Pragma("unroll") for (int i = 0; i < 4; ++i)                                                      \
        acc[i][j] = __builtin_amdgcn_mfma_f32_16x16x32_bf16(zf, af[h][i], acc[i][j], 0, 0, 0);           \
    }                                                                                                    \
    __builtin_amdgcn_sched_barrier(0);                                                                   \
  }
#define ITER(it)                                                \
  {                                                             \
    BAR();                                                      \
    READ_A(it, 0)                                               \
    MMA(((it) + 3) & 3, 1)                                      \
    READ_A(it, 1)                                               \
    if ((it) + 3 < 16) { LOAD_B(((it) + 3) & 3, (it) + 3) }     \
    MMA((it) & 3, 0)                                            \
  }
  BAR();
  STAMP(0, 6);
  LOAD_B(0, 0) LOAD_B(1, 1) LOAD_B(2, 2) LOAD_B(3, 3)
  READ_A(0, 0)
  READ_A(0, 1)
  MMA(0, 0)
  STAMP(0, 7);
  ITER(1) ITER(2) ITER(3) ITER(4) ITER(5) ITER(6) ITER(7) ITER(8)
  STAMP(0, 8);
  ITER(9) ITER(10) ITER(11) ITER(12) ITER(13)
  int lz = threadIdx.x & 63;
  asm volatile("" : "+v"(lz));
  float bias[2];
#pragma unroll
  for (int i = 0; i < 2; ++i) bias[i] = bf_lo(b_sp[(rb * 4 + 2 * kg + i) * 16 + (lz & 15)]);
  ITER(14) ITER(15)
  MMA(3, 1)
  STAMP(0, 9);
  BAR();
  char* redw = smem + ring_slot(14 + (w >> 2 ^ 1)) + (w & 3) * 4096 + lz * 16;
  const char* redr = smem + ring_slot(14 + (w >> 2)) + (w & 3) * 4096 + lz * 16;
#pragma unroll
  for (int i = 0; i < 2; ++i)
#pragma unroll
    for (int j = 0; j < 2; ++j) *(f32x4*)(redw + (2 * i + j) * 1024) = acc[2 + i][j];
  BAR();
  const rsrc_t rsH = make_rsrc(hF, S_ * FH * 2);
#pragma unroll
  for (int i = 0; i < 2; ++i) {
    u32x2 hv[2];
#pragma unroll
    for (int j = 0; j < 2; ++j) {
      const f32x4 a = acc[i][j] + *(const f32x4*)(redr + (2 * i + j) * 1024);
      const unsigned g01 = pack2(a[0], a[1]), g23 = pack2(a[2], a[3]);
      const unsigned s01 = pack2(bf_lo(g01) + bias[i], bf_hi(g01) + bias[i]);
      const unsigned s23 = pack2(bf_lo(g23) + bias[i], bf_hi(g23) + bias[i]);
      hv[j] = u32x2{pack2(bf_lo(z1p[i][j][0]) * bf_lo(s01), bf_hi(z1p[i][j][0]) * bf_hi(s01)),
                    pack2(bf_lo(z1p[i][j][1]) * bf_lo(s23), bf_hi(z1p[i][j][1]) * bf_hi(s23))};
    }
    const unsigned ho = (unsigned)(((rb * 4 + 2 * kg + i) * 28 + 7 * (cb >> 1)) * 64 + lz) * 16u;
    if (two) __builtin_amdgcn_raw_buffer_store_b128(u32x4{hv[0][0], hv[0][1], hv[1][0], hv[1][1]}, rsH, ho + (3 * (cb & 1) + fg) * 1024u, 0, 16);
    else     __builtin_amdgcn_raw_buffer_store_b64(hv[0], rsH, ho + 6 * 1024u + 8u * (cb & 1), 0, 16);
  }
  {
    const int tq = w & 3, gz = lz >> 4, cz = lz & 15;
    const int rrow = 4 * gz + (cz >> 2);
    int rb4[4];
#pragma unroll
    for (int j = 0; j < 4; ++j) rb4[j] = rrow * 128 + ((j ^ ((rrow >> 1) & 3)) * 32) + 8 * (lz & 3);
#define OUT_TILE(t) (ring_slot(((t) >> 3) + 7) + ((t) & 7) * 2048)
#define OUT_FRAG(dst, ks)                                                                                 \
  {                                                                                                       \
    constexpr int pc_ = (ks) / 7, e_ = (ks) % 7;                                                          \
    constexpr int tA_ = (e_ < 6) ? (14 * pc_ + 7 * (e_ / 3) + 2 * (e_ % 3)) : (14 * pc_ + 6);             \
    constexpr int tB_ = (e_ < 6) ? (tA_ + 1) : (14 * pc_ + 13);                                           \
    _Pragma("unroll") for (int j = 0; j < 4; ++j)                                                         \
      dst[j] = tr_frag(smem + rb4[j] + OUT_TILE(tA_), smem + rb4[j] + OUT_TILE(tB_));                     \
  }
    BAR();
    bf16x8 wpre[5][4];
    if (kg == 0) { OUT_FRAG(wpre[0], 0) OUT_FRAG(wpre[1], 2) OUT_FRAG(wpre[2], 4) OUT_FRAG(wpre[3], 6) OUT_FRAG(wpre[4], 8) }
    else         { OUT_FRAG(wpre[0], 1) OUT_FRAG(wpre[1], 3) OUT_FRAG(wpre[2], 5) OUT_FRAG(wpre[3], 7) OUT_FRAG(wpre[4], 9) }
    WAITV(0);
    BAR();
    STAMP(0, 10);
    if (w == 0) {
      if (lz == 0) __hip_atomic_store((gu64*)hflag + (rb * 8 + cb) * 32, tag, RLX_AGENT);
      if (cb < 7) wait_flags(hflag + rb * 8 * 32, 8, lz, tag);
    }
    if (cb == 7) return;
    BAR();
    STAMP(0, 11);
    const unsigned ho = (unsigned)(((rb * 4 + tq) * 28 + kg) * 64 + lz) * 16u;
    u32x4 hf[14];
#pragma unroll
    for (int i = 0; i < 14; ++i) hf[i] = __builtin_amdgcn_raw_buffer_load_b128(rsH, ho, 2048 * i, 16);
    f32x4 oc[4];
#pragma unroll
    for (int j = 0; j < 4; ++j) oc[j] = f32x4{0.f, 0.f, 0.f, 0.f};
#pragma unroll
    for (int i = 0; i < 5; ++i)
#pragma unroll
      for (int j = 0; j < 4; ++j)
        oc[j] = __builtin_amdgcn_mfma_f32_16x16x32_bf16(wpre[i][j], __builtin_bit_cast(bf16x8, hf[i]), oc[j], 0, 0, 0);
#define OUT_STEP(i, ks)                                                                                   \
  {                                                                                                       \
    bf16x8 wf[4];                                                                                         \
    OUT_FRAG(wf, ks)                                                                                      \
    _Pragma("unroll") for (int j = 0; j < 4; ++j)                                                         \
      oc[j] = __builtin_amdgcn_mfma_f32_16x16x32_bf16(wf[j], __builtin_bit_cast(bf16x8, hf[i]), oc[j], 0, 0, 0); \
  }
#define OUT_ALL(par)                                                                                       \
  OUT_STEP(5, 10 + par) OUT_STEP(6, 12 + par) OUT_STEP(7, 14 + par) OUT_STEP(8, 16 + par) OUT_STEP(9, 18 + par) \
  OUT_STEP(10, 20 + par) OUT_STEP(11, 22 + par) OUT_STEP(12, 24 + par) OUT_STEP(13, 26 + par)
    if (kg == 0) { OUT_ALL(0) } else { OUT_ALL(1) }
    char* red = smem + ring_slot(14);
    *(f32x4*)(red + (w ^ 4) * 2048 + lz * 16) = kg ? oc[0] : oc[2];
    *(f32x4*)(red + (w ^ 4) * 2048 + 1024 + lz * 16) = kg ? oc[1] : oc[3];
    BAR();
    const f32x4 a0 = (kg ? oc[2] : oc[0]) + *(const f32x4*)(red + w * 2048 + lz * 16);
    const f32x4 a1 = (kg ? oc[3] : oc[1]) + *(const f32x4*)(red + w * 2048 + 1024 + lz * 16);
    u16* op = out + (size_t)((rb * 4 + tq) * 16 + cz) * D_ + cb * 64 + 32 * kg + 4 * gz;
    *(u32x2*)op = u32x2{pack2(a0[0], a0[1]), pack2(a0[2], a0[3])};
    *(u32x2*)(op + 16) = u32x2{pack2(a1[0], a1[1]), pack2(a1[2], a1[3])};
  }
  if (bid == 0 && tid == 0) __hip_atomic_store((gu64*)sync, tag, RLX_AGENT);
  STAMP(0, 12); STAMPR(0, 15);
}

extern "C" void kernel_launch(void* const* d_in, const int* in_sizes, int n_in, void* d_out, int out_size,
                              void* d_ws, size_t ws_size, hipStream_t stream) {
  const u16* x = (const u16*)d_in[0];
  const u16* w_in = (const u16*)d_in[1];
  const u16* w_sp = (const u16*)d_in[2];
  const u16* b_sp = (const u16*)d_in[3];
  const u16* w_out = (const u16*)d_in[4];
  char* ws = (char*)d_ws;
  u16* z2N = (u16*)ws;
  u16* hF = (u16*)(ws + (size_t)4 * 1024 * 1024);
  float* stats = (float*)(ws + (size_t)8 * 1024 * 1024);
  u64* sync = (u64*)(ws + (size_t)9 * 1024 * 1024);
  gmlp_fused<<<256, 768, 0, stream>>>(x, w_in, w_sp, b_sp, w_out, (u16*)d_out, stats, z2N, hF, sync);
}
```

```cpp
#include <hip/hip_runtime.h>
#include <hip/hip_bf16.h>

typedef __bf16 bf16x8 __attribute__((ext_vector_type(8)));
typedef __bf16 bf16x4 __attribute__((ext_vector_type(4)));
typedef __bf16 bf16x2 __attribute__((ext_vector_type(2)));
typedef float f32x4 __attribute__((ext_vector_type(4)));
typedef float f32x2 __attribute__((ext_vector_type(2)));
typedef unsigned u32x4 __attribute__((ext_vector_type(4)));
typedef unsigned u32x2 __attribute__((ext_vector_type(2)));
typedef unsigned short u16;
#define LAS __attribute__((address_space(3)))
#define DI __device__ __forceinline__

#define STAMP(k, i)
#define STAMPR(k, i)
constexpr int S_ = 2048, D_ = 448, F_ = 1792, FH = 896;

DI float bf_lo(unsigned u) { return __builtin_bit_cast(float, u << 16); }
DI float bf_hi(unsigned u) { return __builtin_bit_cast(float, u & 0xffff0000u); }
DI unsigned pack2(float a, float b) {
  f32x2 f = {a, b};
  return __builtin_bit_cast(unsigned, __builtin_convertvector(f, bf16x2));
}
DI float bfr(float a) { return (float)(__bf16)a; }
DI float gelu_tanh(float z) {
  const float y = z * __builtin_fmaf(z * z, -0.10294324f, -2.3022082f);
  const float e = __builtin_amdgcn_exp2f(y);
  return z * __builtin_amdgcn_rcpf(1.0f + e);
}
DI bf16x8 tr_frag(const char* a0, const char* a1) {
  bf16x4 lo = __builtin_amdgcn_ds_read_tr16_b64_v4bf16((LAS bf16x4*)a0);
  bf16x4 hi = __builtin_amdgcn_ds_read_tr16_b64_v4bf16((LAS bf16x4*)a1);
  return __builtin_shufflevector(lo, hi, 0, 1, 2, 3, 4, 5, 6, 7);
}
template <int CTRL> DI float dpp_mov(float x) {
  return __builtin_bit_cast(float, __builtin_amdgcn_update_dpp(0, __builtin_bit_cast(int, x), CTRL, 0xf, 0xf, true));
}
DI float sum16(float x) {
  x += dpp_mov<0xB1>(x);
  x += dpp_mov<0x4E>(x);
  x += dpp_mov<0x141>(x);
  x += dpp_mov<0x140>(x);
  return x;
}

#define BAR()                                              \
  do {                                                     \
    asm volatile("s_waitcnt lgkmcnt(0)" ::: "memory");     \
    __builtin_amdgcn_s_barrier();                          \
    asm volatile("" ::: "memory");                         \
  } while (0)
#define WAITV(n) asm volatile("s_waitcnt vmcnt(" #n ")" ::: "memory")
typedef __amdgpu_buffer_rsrc_t rsrc_t;
DI rsrc_t make_rsrc(const void* p, unsigned bytes) { return __builtin_amdgcn_make_buffer_rsrc((void*)p, 0, bytes, 0x00020000); }
DI void blds16(rsrc_t rs, char* lds_wave_base, unsigned voff, unsigned soff) {
  __builtin_amdgcn_raw_ptr_buffer_load_lds(rs, (LAS void*)lds_wave_base, 16, voff, soff, 0, 0);
}

constexpr int XW_XB = 7 * 8192, XW_WB = 448 * 224, XW_LDS = XW_XB + XW_WB;

DI int x_swz(int row) { return ((row >> 1) & 1) | (((row >> 3) & 1) << 1) | ((((row >> 2) ^ (row >> 4)) & 1) << 2); }

struct XwLoader {
  rsrc_t rx, rw;
  unsigned xo[4], wo[7];
  int lw;
  DI XwLoader(const u16* x, const u16* w_in, int rb, int n0, int lw_, int l) : lw(lw_) {
    rx = make_rsrc(x, S_ * D_ * 2);
    rw = make_rsrc(w_in, D_ * F_ * 2);
#pragma unroll
    for (int i = 0; i < 4; ++i) {
      const int pc = 4 * i + lw, row = 8 * (pc & 7) + (l >> 3);
      xo[i] = (unsigned)((rb * 64 + row) * D_ + (pc >> 3) * 64 + (((l & 7) ^ x_swz(row)) * 8)) * 2u;
    }
#pragma unroll
    for (int i = 0; i < 7; ++i) {
      const int c = (4 * i + lw) * 64 + l, row = c / 14, cc = c - row * 14;
      wo[i] = (unsigned)(row * F_ + n0 + cc * 8) * 2u;
    }
  }
  DI void stage_x(char* smem, int s) const {
#pragma unroll
    for (int i = 0; i < (s < 3 ? 4 : 2); ++i) blds16(rx, smem + 2 * s * 8192 + (4 * i + lw) * 1024, xo[i], 256 * s);
  }
  DI void stage_w(char* smem, int s, int col0) const {
#pragma unroll
    for (int i = 0; i < (s < 3 ? 7 : 4); ++i)
      if (s < 3 || 4 * i + lw < 14) blds16(rw, smem + XW_XB + (28 * s + 4 * i + lw) * 1024, wo[i], 128 * F_ * 2 * s + col0 * 2);
  }
};
template <bool Z2> struct XwAddr {
  int xa[2], wa[2];
  DI XwAddr(int l, int fg, int mh) {
    const int g = l >> 4, c16 = l & 15, q = c16 >> 2, p = l & 3;
#pragma unroll
    for (int rt = 0; rt < 2; ++rt) {
      const int row = 32 * mh + (Z2 ? (8 * q + 4 * rt + p) : (16 * rt + c16));
      xa[rt] = row * 128 + (((g >> 1) ^ x_swz(row)) * 16) + (g & 1) * 8;
    }
#pragma unroll
    for (int jj = 0; jj < 2; ++jj) wa[jj] = XW_XB + (4 * g + q) * 224 + min(2 * fg + jj, 6) * 32 + 8 * p;
  }
};
template <int OFF> DI u32x2 lds_read64(unsigned addr) {
  u32x2 r;
  asm volatile("ds_read_b64 %0, %1 offset:%2" : "=v"(r) : "v"(addr), "i"(OFF) : "memory");
  return r;
}
template <bool Z2, int KS0, int NKS>
DI void xw_compute(const XwAddr<Z2>& ad, const char* smem, f32x4 (&acc)[2][2]) {
  u32x2 xl[NKS][2], xh[NKS][2];
  bf16x8 wf[NKS][2];
  const unsigned sb = (unsigned)(size_t)smem;
#pragma unroll
  for (int k = 0; k < NKS; ++k) {
    constexpr int dummy = 0; (void)dummy;
#pragma unroll
    for (int rt = 0; rt < 2; ++rt) {
      const unsigned o = sb + (ad.xa[rt] ^ (64 * ((KS0 + k) & 1)));
      if (k < 2) { xl[k][rt] = lds_read64<8192 * (KS0 >> 1)>(o); xh[k][rt] = lds_read64<8192 * (KS0 >> 1)>(o ^ 32); }
      else       { xl[k][rt] = lds_read64<8192 * (KS0 >> 1) + 8192>(o); xh[k][rt] = lds_read64<8192 * (KS0 >> 1) + 8192>(o ^ 32); }
    }
#pragma unroll
    for (int jj = 0; jj < 2; ++jj) {
      const char* a = smem + ad.wa[jj] + 32 * (KS0 + k) * 224;
      wf[k][jj] = tr_frag(a, a + 16 * 224);
    }
  }
  asm volatile("s_waitcnt lgkmcnt(0)" ::: "memory");
  __builtin_amdgcn_sched_barrier(0);
#pragma unroll
  for (int k = 0; k < NKS; ++k)
#pragma unroll
    for (int rt = 0; rt < 2; ++rt) {
      const bf16x8 xf = __builtin_bit_cast(bf16x8, u32x4{xl[k][rt][0], xl[k][rt][1], xh[k][rt][0], xh[k][rt][1]});
#pragma unroll
      for (int jj = 0; jj < 2; ++jj) {
        if (Z2) acc[rt][jj] = __builtin_amdgcn_mfma_f32_16x16x32_bf16(xf, wf[k][jj], acc[rt][jj], 0, 0, 0);
        else    acc[rt][jj] = __builtin_amdgcn_mfma_f32_16x16x32_bf16(wf[k][jj], xf, acc[rt][jj], 0, 0, 0);
      }
    }
}
DI unsigned gelu_pair(float a, float b) {
  const unsigned u = pack2(a, b);
  const f32x2 z = {bf_lo(u), bf_hi(u)};
  const f32x2 y = z * __builtin_elementwise_fma(z * z, f32x2{-0.10294324f, -0.10294324f}, f32x2{-2.3022082f, -2.3022082f});
  const f32x2 d = f32x2{__builtin_amdgcn_exp2f(y[0]), __builtin_amdgcn_exp2f(y[1])} + f32x2{1.0f, 1.0f};
  const f32x2 o = z * f32x2{__builtin_amdgcn_rcpf(d[0]), __builtin_amdgcn_rcpf(d[1])};
  return pack2(o[0], o[1]);
}

DI void tile_of_block(int bid, int& rb, int& cb) {
  const int xcd = bid & 7, idx = bid >> 3;
  rb = (xcd >> 1) * 8 + (idx & 7);
  cb = (xcd & 1) * 4 + (idx >> 3);
}

typedef unsigned long long u64;
typedef __attribute__((address_space(1))) u64 gu64;
#define RLX_AGENT __ATOMIC_RELAXED, __HIP_MEMORY_SCOPE_AGENT
DI constexpr int ring_slot(int st) {
  return 16384 * ((st % 9) == 0 ? 0 : (st % 9) == 1 ? 4 : (st % 9) == 2 ? 1 : (st % 9) == 3 ? 5 : (st % 9) == 4 ? 6 :
                  (st % 9) == 5 ? 2 : (st % 9) == 6 ? 7 : (st % 9) == 7 ? 3 : 8);
}
DI void wait_flags(const u64* f, int n, int l, u64 tag) {
  const gu64* fp = (const gu64*)f + (l < n ? l : 0) * 32;
  for (unsigned spins = 0; spins < (1u << 20); ++spins) {
    if (__all(__hip_atomic_load(fp, RLX_AGENT) == tag)) break;
    __builtin_amdgcn_s_sleep(4);
  }
}

__global__ __launch_bounds__(768, 3) void gmlp_fused(const u16* __restrict__ x, const u16* __restrict__ w_in,
                                                    const u16* __restrict__ w_sp, const u16* __restrict__ b_sp,
                                                    const u16* __restrict__ w_out, u16* __restrict__ out,
                                                    float* stats, u16* z2N, u16* hF, u64* sync) {
  __shared__ __attribute__((aligned(16))) char smem[XW_LDS + 4096 + 1024];
  const int bid = blockIdx.x;
  int rb, cb;
  tile_of_block(bid, rb, cb);
  const int tid = threadIdx.x, l = tid & 63, w = __builtin_amdgcn_readfirstlane(tid >> 6);
  const int t0 = rb * 64;
  const int koff = (4 * (rb & 3)) & 15;

  if (w >= 8) {
    const int lw = w - 8;
    const XwLoader ld(x, w_in, rb, cb * 112, lw, l);
    const rsrc_t rsA = make_rsrc(w_sp, S_ * S_ * 2);
    const rsrc_t rsO = make_rsrc(w_out, cb < 7 ? FH * D_ * 2 : 0);
    unsigned ap[4];
#pragma unroll
    for (int i = 0; i < 4; ++i) {
      const int row = 4 * (4 * i + lw) + (l >> 4);
      ap[i] = (unsigned)((t0 + row) * S_ + (((l & 15) ^ (row & 15)) * 8)) * 2u;
    }
    const unsigned oo = (unsigned)((8 * lw + (l >> 3)) * D_ + cb * 64 + (((l & 7) ^ (((l >> 4) & 3) << 1)) * 8)) * 2u;
#define LOAD_W(st)                                                                                \
  {                                                                                               \
    const int sk_ = ((st) + koff) & 15;                                                           \
    char* dst_ = smem + ring_slot(st) + lw * 1024;                                                \
    _Pragma("unroll") for (int i = 0; i < 4; ++i) blds16(rsA, dst_ + i * 4096, ap[i], 256 * sk_); \
  }
#define LOAD_O(c)        \
  {                                                                                                           \
    char* dst_ = smem + ring_slot((c) + 7) + lw * 1024;                                                       \
    _Pragma("unroll") for (int i = 0; i < 4; ++i) blds16(rsO, dst_ + i * 4096, oo, (128 * (c) + 32 * i) * D_ * 2); \
  }
    ld.stage_x(smem, 0); ld.stage_w(smem, 0, FH);
    ld.stage_x(smem, 1); ld.stage_w(smem, 1, FH);
    WAITV(11); BAR();
    ld.stage_x(smem, 2); ld.stage_w(smem, 2, FH);
    ld.stage_x(smem, 3); ld.stage_w(smem, 3, FH);
    {
      const int c = bid * 196 + min(lw * 64 + l, 195);
      blds16(ld.rw, smem + XW_LDS + 4096, (unsigned)((c / 112) * F_ + (c % 112) * 8) * 2u, 0);
    }
    WAITV(17); BAR();
    ld.stage_w(smem, 0, 0);
    WAITV(13); BAR();
    ld.stage_w(smem, 1, 0);
    WAITV(15); BAR();
    ld.stage_w(smem, 2, 0);
    WAITV(14); BAR();
    ld.stage_w(smem, 3, 0);
    WAITV(10); BAR();
    LOAD_W(0) LOAD_W(1)
#pragma unroll
    for (int i = 0; i < 8; ++i) blds16(rsA, smem + XW_LDS + 4096, (unsigned)(lw * 64 + l) * 16u, bid * 32768 + i * 4096);
    WAITV(19); BAR();
    LOAD_W(2) LOAD_W(3) LOAD_W(4)
    WAITV(28); BAR();
    LOAD_W(5) LOAD_W(6)
    BAR();
    LOAD_W(7) LOAD_W(8)
    WAITV(32); BAR();
    WAITV(28); BAR();
    LOAD_W(9) WAITV(28); BAR();
    WAITV(24); BAR();
    LOAD_W(10) WAITV(24); BAR();
    WAITV(20); BAR();
    LOAD_W(11) WAITV(20); BAR();
    WAITV(16); BAR();
    LOAD_W(12) WAITV(16); BAR();
    WAITV(12); BAR();
    LOAD_W(13) WAITV(12); BAR();
    LOAD_W(14) WAITV(12); BAR();
    LOAD_W(15) WAITV(12); BAR();
    LOAD_O(0) WAITV(12); BAR();
    LOAD_O(1) WAITV(12); BAR();
    LOAD_O(2) WAITV(12); BAR();
    LOAD_O(3) BAR();
    LOAD_O(4) BAR();
    LOAD_O(5) LOAD_O(6)
    WAITV(0); BAR();
    return;
  }

  const int fg = w & 3, kg = w >> 2, g = l >> 4, c16 = l & 15;
  const bool two = fg < 3;
  STAMPR(0, 0); STAMP(0, 1);
  const u64 nonce = __hip_atomic_load((const gu64*)sync, RLX_AGENT);
  const u64 tag = (((u64)(unsigned)__builtin_amdgcn_readfirstlane((unsigned)(nonce >> 32)) << 32) |
                   (unsigned)__builtin_amdgcn_readfirstlane((unsigned)nonce)) + 1;
  u64* sflag = sync + 32;
  u64* zflag = sflag + 256 * 32;
  u64* hflag = zflag + 256 * 32;
  const rsrc_t rsN = make_rsrc(z2N, S_ * FH * 2);
  const rsrc_t rsN1 = make_rsrc(z2N, two ? S_ * FH * 2 : 0);
  f32x2* sst = (f32x2*)(smem + XW_LDS);

  u32x4 zp[2];
  {
    const XwAddr<true> ad(l, fg, kg);
    f32x4 acc[2][2];
#pragma unroll
    for (int rt = 0; rt < 2; ++rt)
#pragma unroll
      for (int jj = 0; jj < 2; ++jj) acc[rt][jj] = f32x4{0.f, 0.f, 0.f, 0.f};
    BAR(); STAMP(0, 2); xw_compute<true, 0, 4>(ad, smem, acc);
    BAR(); xw_compute<true, 4, 4>(ad, smem, acc);
    BAR(); xw_compute<true, 8, 4>(ad, smem, acc);
    BAR(); xw_compute<true, 12, 2>(ad, smem, acc);
#pragma unroll
    for (int jj = 0; jj < 2; ++jj)
      zp[jj] = u32x4{gelu_pair(acc[0][jj][0], acc[0][jj][1]), gelu_pair(acc[0][jj][2], acc[0][jj][3]),
                     gelu_pair(acc[1][jj][0], acc[1][jj][1]), gelu_pair(acc[1][jj][2], acc[1][jj][3])};
    if (!two) zp[1] = u32x4{0u, 0u, 0u, 0u};
    float v[16];
#pragma unroll
    for (int e = 0; e < 4; ++e) {
      const f32x2 a0 = {bf_lo(zp[0][e]), bf_hi(zp[0][e])}, a1 = {bf_lo(zp[1][e]), bf_hi(zp[1][e])};
      const f32x2 s1 = a0 + a1, s2 = __builtin_elementwise_fma(a0, a0, a1 * a1);
      v[4 * e] = s1[0]; v[4 * e + 1] = s2[0]; v[4 * e + 2] = s1[1]; v[4 * e + 3] = s2[1];
    }
#pragma unroll
    for (int i = 0; i < 8; ++i) { const bool b = c16 & 1; v[i] = (b ? v[i + 8] : v[i]) + dpp_mov<0xB1>(b ? v[i] : v[i + 8]); }
#pragma unroll
    for (int i = 0; i < 4; ++i) { const bool b = c16 & 2; v[i] = (b ? v[i + 4] : v[i]) + dpp_mov<0x4E>(b ? v[i] : v[i + 4]); }
#pragma unroll
    for (int i = 0; i < 2; ++i) { const bool b = c16 & 4; v[i] = (b ? v[i + 2] : v[i]) + dpp_mov<0x124>(b ? v[i] : v[i + 2]); }
    { const bool b = c16 & 8; v[0] = (b ? v[1] : v[0]) + dpp_mov<0x128>(b ? v[0] : v[1]); }
    const int idx = ((c16 & 1) << 3) | ((c16 & 2) << 1) | ((c16 & 4) >> 1) | (c16 >> 3);
    ((float*)(sst + fg * 64 + 32 * kg + 8 * g))[idx] = v[0];
  }
  STAMP(0, 3);

  u32x2 z1p[2][2];
  {
    const XwAddr<false> ad(l, fg, kg);
    f32x4 za[2][2];
#pragma unroll
    for (int i = 0; i < 2; ++i)
#pragma unroll
      for (int j = 0; j < 2; ++j) za[i][j] = f32x4{0.f, 0.f, 0.f, 0.f};
    BAR();
    STAMP(0, 13);
    const unsigned tagA = (unsigned)tag, tagB = (unsigned)(tag >> 32) ^ (tagA * 0x9E3779B1u);
    if (w == 7) {
      const f32x2 t = sst[l] + sst[64 + l] + sst[128 + l] + sst[192 + l];
      const float m2 = fmaxf(t[1] - t[0] * t[0] * (1.0f / 112.0f), 0.f);
      gu64* gp = (gu64*)stats + (size_t)(rb * 8 + cb) * 128 + l;
      __hip_atomic_store(gp, ((u64)tagA << 32) | __builtin_bit_cast(unsigned, t[0]), RLX_AGENT);
      __hip_atomic_store(gp + 64, ((u64)tagB << 32) | __builtin_bit_cast(unsigned, m2), RLX_AGENT);
    }
    xw_compute<false, 0, 4>(ad, smem, za);
    BAR(); STAMP(0, 14);
    f32x2* mrs = (f32x2*)(smem + XW_LDS + 2048);
    const gu64* gq = (const gu64*)stats + (size_t)rb * 1024 + l;
    u64 gv[16];
    if (w == 0) {
#pragma unroll
      for (int i = 0; i < 16; ++i) gv[i] = __hip_atomic_load(gq + 64 * i, RLX_AGENT);
    }
    xw_compute<false, 4, 4>(ad, smem, za);
    if (w == 0) {
      for (unsigned spins = 0; spins < (1u << 19); ++spins) {
        bool ok = true;
#pragma unroll
        for (int i = 0; i < 16; ++i) ok &= (unsigned)(gv[i] >> 32) == ((i & 1) ? tagB : tagA);
        if (__all(ok)) break;
#pragma unroll
        for (int i = 0; i < 16; ++i) gv[i] = __hip_atomic_load(gq + 64 * i, RLX_AGENT);
      }
      float sum = 0.f, m2 = 0.f;
#pragma unroll
      for (int i = 0; i < 8; ++i) {
        sum += __builtin_bit_cast(float, (unsigned)gv[2 * i]);
        m2 += __builtin_bit_cast(float, (unsigned)gv[2 * i + 1]);
      }
      const float mean = sum * (1.0f / 896.0f);
#pragma unroll
      for (int i = 0; i < 8; ++i) {
        const float d = __builtin_bit_cast(float, (unsigned)gv[2 * i]) * (1.0f / 112.0f) - mean;
        m2 += 112.0f * d * d;
      }
      const float var = bfr(m2 * (1.0f / 896.0f));
      const float den = bfr(sqrtf(bfr(var + 1e-5f)));
      mrs[l] = f32x2{bfr(mean), 1.0f / den};
    }
    BAR();
    STAMP(0, 4);
    const unsigned zoff = (unsigned)(((cb * 7 + 2 * fg) * 64 + 2 * rb + kg) * 64 + l) * 16u;
#pragma unroll
    for (int jj = 0; jj < 2; ++jj) {
      u32x4 v;
#pragma unroll
      for (int e = 0; e < 4; ++e) {
        const f32x4 mr = *(const f32x4*)(mrs + 32 * kg + 8 * g + 2 * e);
        v[e] = pack2(bfr(bf_lo(zp[jj][e]) - mr[0]) * mr[1], bfr(bf_hi(zp[jj][e]) - mr[2]) * mr[3]);
      }
      __builtin_amdgcn_raw_buffer_store_b128(v, jj ? rsN1 : rsN, zoff + 65536u * jj, 0, 16);
    }
    xw_compute<false, 8, 4>(ad, smem, za);
    BAR();
    xw_compute<false, 12, 2>(ad, smem, za);
#pragma unroll
    for (int i = 0; i < 2; ++i)
#pragma unroll
      for (int j = 0; j < 2; ++j) {
        z1p[i][j] = u32x2{gelu_pair(za[i][j][0], za[i][j][1]), gelu_pair(za[i][j][2], za[i][j][3])};
        asm volatile("" : "+v"(z1p[i][j]));
      }
    WAITV(0);
    BAR();
    if (w == 0) {
      if (l == 0) __hip_atomic_store((gu64*)zflag + (cb * 32 + rb) * 32, tag, RLX_AGENT);
      wait_flags(zflag + cb * 32 * 32, 32, l, tag);
    }
  }
  STAMP(0, 5);

  const unsigned boff = (unsigned)(((cb * 7 + 2 * fg) * 64 + kg) * 64 + l) * 16u;
  u32x4 bst[4][4];
#define LOAD_B(slot, st)                                                                          \
  {                                                                                               \
    const int sk_ = ((st) + koff) & 15;                                                           \
    bst[slot][0] = __builtin_amdgcn_raw_buffer_load_b128(rsN, boff, 4096 * sk_, 16);              \
    bst[slot][1] = __builtin_amdgcn_raw_buffer_load_b128(rsN1, boff + 65536, 4096 * sk_, 16);     \
    bst[slot][2] = __builtin_amdgcn_raw_buffer_load_b128(rsN, boff + 2048, 4096 * sk_, 16);       \
    bst[slot][3] = __builtin_amdgcn_raw_buffer_load_b128(rsN1, boff + 65536 + 2048, 4096 * sk_, 16); \
  }
  int ard[4];
#pragma unroll
  for (int i = 0; i < 4; ++i) ard[i] = (16 * ((i + 2 * kg) & 3) + c16) * 256 + (((4 * kg + g) ^ c16) * 16);
  f32x4 acc[4][2];
#pragma unroll
  for (int i = 0; i < 4; ++i)
#pragma unroll
    for (int j = 0; j < 2; ++j) acc[i][j] = f32x4{0.f, 0.f, 0.f, 0.f};
  bf16x8 af[2][4];
#define READ_A(st, h)                                                                                    \
  {                                                                                                      \
    const char* ab = smem + ring_slot(st);                                                               \
    _Pragma("unroll") for (int i = 0; i < 4; ++i) {                                                      \
      int a_ = ard[i];                                                                                   \
      asm volatile("" : "+v"(a_));   \
      af[h][i] = *(const bf16x8*)(ab + (a_ ^ (128 * (h))));                                              \
    }                                                                                                    \
    __builtin_amdgcn_sched_barrier(0);                                                                   \
  }
#define MMA(u, h)                                                                                        \
  {                                                                                                      \
    _Pragma("unroll") for (int j = 0; j < 2; ++j) {                                                      \
      const bf16x8 zf = __builtin_bit_cast(bf16x8, bst[u][2 * (h) + j]);                                 \
      _Pragma("unroll") for (int i = 0; i < 4; ++i)                                                      \
        acc[i][j] = __builtin_amdgcn_mfma_f32_16x16x32_bf16(zf, af[h][i], acc[i][j], 0, 0, 0);           \
    }                                                                                                    \
    __builtin_amdgcn_sched_barrier(0);                                                                   \
  }
#define ITER(it)                                                \
  {                                                             \
    BAR();                                                      \
    READ_A(it, 0)                                               \
    MMA(((it) + 3) & 3, 1)                                      \
    READ_A(it, 1)                                               \
    if ((it) + 3 < 16) { LOAD_B(((it) + 3) & 3, (it) + 3) }     \
    MMA((it) & 3, 0)                                            \
  }
  BAR();
  STAMP(0, 6);
  LOAD_B(0, 0) LOAD_B(1, 1) LOAD_B(2, 2) LOAD_B(3, 3)
  READ_A(0, 0)
  READ_A(0, 1)
  MMA(0, 0)
  STAMP(0, 7);
  ITER(1) ITER(2) ITER(3) ITER(4) ITER(5) ITER(6) ITER(7) ITER(8)
  STAMP(0, 8);
  ITER(9) ITER(10) ITER(11) ITER(12) ITER(13)
  int lz = __builtin_amdgcn_mbcnt_hi(~0u, __builtin_amdgcn_mbcnt_lo(~0u, 0u));
  asm volatile("" : "+v"(lz));
  float bias[2];
#pragma unroll
  for (int i = 0; i < 2; ++i) bias[i] = bf_lo(b_sp[(rb * 4 + 2 * kg + i) * 16 + (lz & 15)]);
  ITER(14) ITER(15)
  MMA(3, 1)
  STAMP(0, 9);
  BAR();
  char* redw = smem + (kg ? ring_slot(14) : ring_slot(15)) + fg * 4096 + lz * 16;
  const char* redr = smem + (kg ? ring_slot(15) : ring_slot(14)) + fg * 4096 + lz * 16;
#pragma unroll
  for (int i = 0; i < 2; ++i)
#pragma unroll
    for (int j = 0; j < 2; ++j) *(f32x4*)(redw + (2 * i + j) * 1024) = acc[2 + i][j];
  BAR();
  const rsrc_t rsH = make_rsrc(hF, S_ * FH * 2);
#pragma unroll
  for (int i = 0; i < 2; ++i) {
    u32x2 hv[2];
#pragma unroll
    for (int j = 0; j < 2; ++j) {
      const f32x4 a = acc[i][j] + *(const f32x4*)(redr + (2 * i + j) * 1024);
      const unsigned g01 = pack2(a[0], a[1]), g23 = pack2(a[2], a[3]);
      const unsigned s01 = pack2(bf_lo(g01) + bias[i], bf_hi(g01) + bias[i]);
      const unsigned s23 = pack2(bf_lo(g23) + bias[i], bf_hi(g23) + bias[i]);
      hv[j] = u32x2{pack2(bf_lo(z1p[i][j][0]) * bf_lo(s01), bf_hi(z1p[i][j][0]) * bf_hi(s01)),
                    pack2(bf_lo(z1p[i][j][1]) * bf_lo(s23), bf_hi(z1p[i][j][1]) * bf_hi(s23))};
    }
    const unsigned ho = (unsigned)(((rb * 4 + 2 * kg + i) * 28 + 7 * (cb >> 1)) * 64 + lz) * 16u;
    if (two) __builtin_amdgcn_raw_buffer_store_b128(u32x4{hv[0][0], hv[0][1], hv[1][0], hv[1][1]}, rsH, ho + (3 * (cb & 1) + fg) * 1024u, 0, 16);
    else     __builtin_amdgcn_raw_buffer_store_b64(hv[0], rsH, ho + 6 * 1024u + 8u * (cb & 1), 0, 16);
  }
  {
    const int tq = w & 3, gz = lz >> 4, cz = lz & 15;
    const int rrow = 4 * gz + (cz >> 2);
    int rb4[4];
#pragma unroll
    for (int j = 0; j < 4; ++j) rb4[j] = rrow * 128 + ((j ^ ((rrow >> 1) & 3)) * 32) + 8 * (lz & 3);
#define OUT_TILE(t) (ring_slot(((t) >> 3) + 7) + ((t) & 7) * 2048)
#define OUT_FRAG(dst, ks)                                                                                 \
  {                                                                                                       \
    constexpr int pc_ = (ks) / 7, e_ = (ks) % 7;                                                          \
    constexpr int tA_ = (e_ < 6) ? (14 * pc_ + 7 * (e_ / 3) + 2 * (e_ % 3)) : (14 * pc_ + 6);             \
    constexpr int tB_ = (e_ < 6) ? (tA_ + 1) : (14 * pc_ + 13);                                           \
    _Pragma("unroll") for (int j = 0; j < 4; ++j)                                                         \
      dst[j] = tr_frag(smem + rb4[j] + OUT_TILE(tA_), smem + rb4[j] + OUT_TILE(tB_));                     \
  }
    BAR();
    bf16x8 wpre[5][4];
    if (kg == 0) { OUT_FRAG(wpre[0], 0) OUT_FRAG(wpre[1], 2) OUT_FRAG(wpre[2], 4) OUT_FRAG(wpre[3], 6) OUT_FRAG(wpre[4], 8) }
    else         { OUT_FRAG(wpre[0], 1) OUT_FRAG(wpre[1], 3) OUT_FRAG(wpre[2], 5) OUT_FRAG(wpre[3], 7) OUT_FRAG(wpre[4], 9) }
    WAITV(0);
    BAR();
    STAMP(0, 10);
    if (w == 0) {
      if (lz == 0) __hip_atomic_store((gu64*)hflag + (rb * 8 + cb) * 32, tag, RLX_AGENT);
      if (cb < 7) wait_flags(hflag + rb * 8 * 32, 8, lz, tag);
    }
    if (cb == 7) return;
    BAR();
    STAMP(0, 11);
    const unsigned ho = (unsigned)(((rb * 4 + tq) * 28 + kg) * 64 + lz) * 16u;
    u32x4 hf[14];
#pragma unroll
    for (int i = 0; i < 14; ++i) hf[i] = __builtin_amdgcn_raw_buffer_load_b128(rsH, ho, 2048 * i, 16);
    f32x4 oc[4];
#pragma unroll
    for (int j = 0; j < 4; ++j) oc[j] = f32x4{0.f, 0.f, 0.f, 0.f};
#pragma unroll
    for (int i = 0; i < 5; ++i)
#pragma unroll
      for (int j = 0; j < 4; ++j)
        oc[j] = __builtin_amdgcn_mfma_f32_16x16x32_bf16(wpre[i][j], __builtin_bit_cast(bf16x8, hf[i]), oc[j], 0, 0, 0);
#define OUT_STEP(i, ks)                                                                                   \
  {                                                                                                       \
    bf16x8 wf[4];                                                                                         \
    OUT_FRAG(wf, ks)                                                                                      \
    _Pragma("unroll") for (int j = 0; j < 4; ++j)                                                         \
      oc[j] = __builtin_amdgcn_mfma_f32_16x16x32_bf16(wf[j], __builtin_bit_cast(bf16x8, hf[i]), oc[j], 0, 0, 0); \
  }
#define OUT_ALL(par)                                                                                       \
  OUT_STEP(5, 10 + par) OUT_STEP(6, 12 + par) OUT_STEP(7, 14 + par) OUT_STEP(8, 16 + par) OUT_STEP(9, 18 + par) \
  OUT_STEP(10, 20 + par) OUT_STEP(11, 22 + par) OUT_STEP(12, 24 + par) OUT_STEP(13, 26 + par)
    if (kg == 0) { OUT_ALL(0) } else { OUT_ALL(1) }
    char* red = smem + ring_slot(14);
    *(f32x4*)(red + (w ^ 4) * 2048 + lz * 16) = kg ? oc[0] : oc[2];
    *(f32x4*)(red + (w ^ 4) * 2048 + 1024 + lz * 16) = kg ? oc[1] : oc[3];
    BAR();
    const f32x4 a0 = (kg ? oc[2] : oc[0]) + *(const f32x4*)(red + w * 2048 + lz * 16);
    const f32x4 a1 = (kg ? oc[3] : oc[1]) + *(const f32x4*)(red + w * 2048 + 1024 + lz * 16);
    u16* op = out + (size_t)((rb * 4 + tq) * 16 + cz) * D_ + cb * 64 + 32 * kg + 4 * gz;
    *(u32x2*)op = u32x2{pack2(a0[0], a0[1]), pack2(a0[2], a0[3])};
    *(u32x2*)(op + 16) = u32x2{pack2(a1[0], a1[1]), pack2(a1[2], a1[3])};
  }
  if (bid == 0 && w == 0 && lz == 0) __hip_atomic_store((gu64*)sync, tag, RLX_AGENT);
  STAMP(0, 12); STAMPR(0, 15);
}

extern "C" void kernel_launch(void* const* d_in, const int* in_sizes, int n_in, void* d_out, int out_size,
                              void* d_ws, size_t ws_size, hipStream_t stream) {
  const u16* x = (const u16*)d_in[0];
  const u16* w_in = (const u16*)d_in[1];
  const u16* w_sp = (const u16*)d_in[2];
  const u16* b_sp = (const u16*)d_in[3];
  const u16* w_out = (const u16*)d_in[4];
  char* ws = (char*)d_ws;
  u16* z2N = (u16*)ws;
  u16* hF = (u16*)(ws + (size_t)4 * 1024 * 1024);
  float* stats = (float*)(ws + (size_t)8 * 1024 * 1024);
  u64* sync = (u64*)(ws + (size_t)9 * 1024 * 1024);
  gmlp_fused<<<256, 768, 0, stream>>>(x, w_in, w_sp, b_sp, w_out, (u16*)d_out, stats, z2N, hF, sync);
}
```

```cpp
#include <hip/hip_runtime.h>
#include <hip/hip_bf16.h>

typedef __bf16 bf16x8 __attribute__((ext_vector_type(8)));
typedef __bf16 bf16x4 __attribute__((ext_vector_type(4)));
typedef __bf16 bf16x2 __attribute__((ext_vector_type(2)));
typedef float f32x4 __attribute__((ext_vector_type(4)));
typedef float f32x2 __attribute__((ext_vector_type(2)));
typedef unsigned u32x4 __attribute__((ext_vector_type(4)));
typedef unsigned u32x2 __attribute__((ext_vector_type(2)));
typedef unsigned short u16;
#define LAS __attribute__((address_space(3)))
#define DI __device__ __forceinline__

#define STAMP(k, i)
#define STAMPR(k, i)
constexpr int S_ = 2048, D_ = 448, F_ = 1792, FH = 896;

DI float bf_lo(unsigned u) { return __builtin_bit_cast(float, u << 16); }
DI float bf_hi(unsigned u) { return __builtin_bit_cast(float, u & 0xffff0000u); }
DI unsigned pack2(float a, float b) {
  f32x2 f = {a, b};
  return __builtin_bit_cast(unsigned, __builtin_convertvector(f, bf16x2));
}
DI float bfr(float a) { return (float)(__bf16)a; }
DI float gelu_tanh(float z) {
  const float y = z * __builtin_fmaf(z * z, -0.10294324f, -2.3022082f);
  const float e = __builtin_amdgcn_exp2f(y);
  return z * __builtin_amdgcn_rcpf(1.0f + e);
}
DI bf16x8 tr_frag(const char* a0, const char* a1) {
  bf16x4 lo = __builtin_amdgcn_ds_read_tr16_b64_v4bf16((LAS bf16x4*)a0);
  bf16x4 hi = __builtin_amdgcn_ds_read_tr16_b64_v4bf16((LAS bf16x4*)a1);
  return __builtin_shufflevector(lo, hi, 0, 1, 2, 3, 4, 5, 6, 7);
}
template <int CTRL> DI float dpp_mov(float x) {
  return __builtin_bit_cast(float, __builtin_amdgcn_update_dpp(0, __builtin_bit_cast(int, x), CTRL, 0xf, 0xf, true));
}
DI float sum16(float x) {
  x += dpp_mov<0xB1>(x);
  x += dpp_mov<0x4E>(x);
  x += dpp_mov<0x141>(x);
  x += dpp_mov<0x140>(x);
  return x;
}

#define BAR()                                              \
  do {                                                     \
    asm volatile("s_waitcnt lgkmcnt(0)" ::: "memory");     \
    __builtin_amdgcn_s_barrier();                          \
    asm volatile("" ::: "memory");                         \
  } while (0)
#define WAITV(n) asm volatile("s_waitcnt vmcnt(" #n ")" ::: "memory")
typedef __amdgpu_buffer_rsrc_t rsrc_t;
DI rsrc_t make_rsrc(const void* p, unsigned bytes) { return __builtin_amdgcn_make_buffer_rsrc((void*)p, 0, bytes, 0x00020000); }
DI void blds16(rsrc_t rs, char* lds_wave_base, unsigned voff, unsigned soff) {
  __builtin_amdgcn_raw_ptr_buffer_load_lds(rs, (LAS void*)lds_wave_base, 16, voff, soff, 0, 0);
}

constexpr int XW_XB = 7 * 8192, XW_WB = 448 * 224, XW_LDS = XW_XB + XW_WB;

DI int x_swz(int row) { return ((row >> 1) & 1) | (((row >> 3) & 1) << 1) | ((((row >> 2) ^ (row >> 4)) & 1) << 2); }

struct XwLoader {
  rsrc_t rx, rw;
  unsigned xo[4], wo[7];
  int lw;
  DI XwLoader(const u16* x, const u16* w_in, int rb, int n0, int lw_, int l) : lw(lw_) {
    rx = make_rsrc(x, S_ * D_ * 2);
    rw = make_rsrc(w_in, D_ * F_ * 2);
#pragma unroll
    for (int i = 0; i < 4; ++i) {
      const int pc = 4 * i + lw, row = 8 * (pc & 7) + (l >> 3);
      xo[i] = (unsigned)((rb * 64 + row) * D_ + (pc >> 3) * 64 + (((l & 7) ^ x_swz(row)) * 8)) * 2u;
    }
#pragma unroll
    for (int i = 0; i < 7; ++i) {
      const int c = (4 * i + lw) * 64 + l, row = c / 14, cc = c - row * 14;
      wo[i] = (unsigned)(row * F_ + n0 + cc * 8) * 2u;
    }
  }
  DI void stage_x(char* smem, int s) const {
#pragma unroll
    for (int i = 0; i < (s < 3 ? 4 : 2); ++i) blds16(rx, smem + 2 * s * 8192 + (4 * i + lw) * 1024, xo[i], 256 * s);
  }
  DI void stage_w(char* smem, int s, int col0) const {
#pragma unroll
    for (int i = 0; i < (s < 3 ? 7 : 4); ++i)
      if (s < 3 || 4 * i + lw < 14) blds16(rw, smem + XW_XB + (28 * s + 4 * i + lw) * 1024, wo[i], 128 * F_ * 2 * s + col0 * 2);
  }
};
template <bool Z2> struct XwAddr {
  int xa[2], wa[2];
  DI XwAddr(int l, int fg, int mh) {
    const int g = l >> 4, c16 = l & 15, q = c16 >> 2, p = l & 3;
#pragma unroll
    for (int rt = 0; rt < 2; ++rt) {
      const int row = 32 * mh + (Z2 ? (8 * q + 4 * rt + p) : (16 * rt + c16));
      xa[rt] = row * 128 + (((g >> 1) ^ x_swz(row)) * 16) + (g & 1) * 8;
    }
#pragma unroll
    for (int jj = 0; jj < 2; ++jj) wa[jj] = XW_XB + (4 * g + q) * 224 + min(2 * fg + jj, 6) * 32 + 8 * p;
  }
};
template <int OFF> DI u32x2 lds_read64(unsigned addr) {
  u32x2 r;
  asm volatile("ds_read_b64 %0, %1 offset:%2" : "=v"(r) : "v"(addr), "i"(OFF) : "memory");
  return r;
}
template <bool Z2, int KS0, int NKS>
DI void xw_compute(const XwAddr<Z2>& ad, const char* smem, f32x4 (&acc)[2][2]) {
  u32x2 xl[NKS][2], xh[NKS][2];
  bf16x8 wf[NKS][2];
  const unsigned sb = (unsigned)(size_t)smem;
#pragma unroll
  for (int k = 0; k < NKS; ++k) {
    constexpr int dummy = 0; (void)dummy;
#pragma unroll
    for (int rt = 0; rt < 2; ++rt) {
      const unsigned o = sb + (ad.xa[rt] ^ (64 * ((KS0 + k) & 1)));
      if (k < 2) { xl[k][rt] = lds_read64<8192 * (KS0 >> 1)>(o); xh[k][rt] = lds_read64<8192 * (KS0 >> 1)>(o ^ 32); }
      else       { xl[k][rt] = lds_read64<8192 * (KS0 >> 1) + 8192>(o); xh[k][rt] = lds_read64<8192 * (KS0 >> 1) + 8192>(o ^ 32); }
    }
#pragma unroll
    for (int jj = 0; jj < 2; ++jj) {
      const char* a = smem + ad.wa[jj] + 32 * (KS0 + k) * 224;
      wf[k][jj] = tr_frag(a, a + 16 * 224);
    }
  }
  asm volatile("s_waitcnt lgkmcnt(0)" ::: "memory");
  __builtin_amdgcn_sched_barrier(0);
#pragma unroll
  for (int k = 0; k < NKS; ++k)
#pragma unroll
    for (int rt = 0; rt < 2; ++rt) {
      const bf16x8 xf = __builtin_bit_cast(bf16x8, u32x4{xl[k][rt][0], xl[k][rt][1], xh[k][rt][0], xh[k][rt][1]});
#pragma unroll
      for (int jj = 0; jj < 2; ++jj) {
        if (Z2) acc[rt][jj] = __builtin_amdgcn_mfma_f32_16x16x32_bf16(xf, wf[k][jj], acc[rt][jj], 0, 0, 0);
        else    acc[rt][jj] = __builtin_amdgcn_mfma_f32_16x16x32_bf16(wf[k][jj], xf, acc[rt][jj], 0, 0, 0);
      }
    }
}
DI unsigned gelu_pair(float a, float b) {
  const unsigned u = pack2(a, b);
  const f32x2 z = {bf_lo(u), bf_hi(u)};
  const f32x2 y = z * __builtin_elementwise_fma(z * z, f32x2{-0.10294324f, -0.10294324f}, f32x2{-2.3022082f, -2.3022082f});
  const f32x2 d = f32x2{__builtin_amdgcn_exp2f(y[0]), __builtin_amdgcn_exp2f(y[1])} + f32x2{1.0f, 1.0f};
  const f32x2 o = z * f32x2{__builtin_amdgcn_rcpf(d[0]), __builtin_amdgcn_rcpf(d[1])};
  return pack2(o[0], o[1]);
}

DI void tile_of_block(int bid, int& rb, int& cb) {
  const int xcd = bid & 7, idx = bid >> 3;
  rb = (xcd >> 1) * 8 + (idx & 7);
  cb = (xcd & 1) * 4 + (idx >> 3);
}

typedef unsigned long long u64;
typedef __attribute__((address_space(1))) u64 gu64;
#define RLX_AGENT __ATOMIC_RELAXED, __HIP_MEMORY_SCOPE_AGENT
DI constexpr int ring_slot(int st) {
  return 16384 * ((st % 9) == 0 ? 0 : (st % 9) == 1 ? 4 : (st % 9) == 2 ? 1 : (st % 9) == 3 ? 5 : (st % 9) == 4 ? 6 :
                  (st % 9) == 5 ? 2 : (st % 9) == 6 ? 7 : (st % 9) == 7 ? 3 : 8);
}
DI int ring_slot_rt(int st) {
  const int m = st >= 9 ? st - 9 : st;
  return (int)((0x837265140ull >> (4 * m)) & 15) * 16384;
}
DI void wait_flags(const u64* f, int n, int l, u64 tag) {
  const gu64* fp = (const gu64*)f + (l < n ? l : 0) * 32;
  for (unsigned spins = 0; spins < (1u << 20); ++spins) {
    if (__all(__hip_atomic_load(fp, RLX_AGENT) == tag)) break;
    __builtin_amdgcn_s_sleep(4);
  }
}

__global__ __launch_bounds__(768, 3) void gmlp_fused(const u16* __restrict__ x, const u16* __restrict__ w_in,
                                                    const u16* __restrict__ w_sp, const u16* __restrict__ b_sp,
                                                    const u16* __restrict__ w_out, u16* __restrict__ out,
                                                    float* stats, u16* z2N, u16* hF, u64* sync) {
  __shared__ __attribute__((aligned(16))) char smem[XW_LDS + 4096 + 1024];
  const int bid = blockIdx.x;
  int rb, cb;
  tile_of_block(bid, rb, cb);
  const int tid = threadIdx.x, l = tid & 63, w = __builtin_amdgcn_readfirstlane(tid >> 6);
  const int t0 = rb * 64;
  const int koff = (4 * (rb & 3)) & 15;

  if (w >= 8) {
    const int lw = w - 8;
    const XwLoader ld(x, w_in, rb, cb * 112, lw, l);
    const rsrc_t rsA = make_rsrc(w_sp, S_ * S_ * 2);
    const rsrc_t rsO = make_rsrc(w_out, cb < 7 ? FH * D_ * 2 : 0);
    unsigned ap[4];
#pragma unroll
    for (int i = 0; i < 4; ++i) {
      const int row = 4 * (4 * i + lw) + (l >> 4);
      ap[i] = (unsigned)((t0 + row) * S_ + (((l & 15) ^ (row & 15)) * 8)) * 2u;
    }
    const unsigned oo = (unsigned)((8 * lw + (l >> 3)) * D_ + cb * 64 + (((l & 7) ^ (((l >> 4) & 3) << 1)) * 8)) * 2u;
#define LOAD_W(st)                                                                                \
  {                                                                                               \
    const int sk_ = ((st) + koff) & 15;                                                           \
    char* dst_ = smem + ring_slot(st) + lw * 1024;                                                \
    _Pragma("unroll") for (int i = 0; i < 4; ++i) blds16(rsA, dst_ + i * 4096, ap[i], 256 * sk_); \
  }
#define LOAD_O(c)        \
  {                                                                                                           \
    char* dst_ = smem + ring_slot((c) + 7) + lw * 1024;                                                       \
    _Pragma("unroll") for (int i = 0; i < 4; ++i) blds16(rsO, dst_ + i * 4096, oo, (128 * (c) + 32 * i) * D_ * 2); \
  }
    ld.stage_x(smem, 0); ld.stage_w(smem, 0, FH);
    ld.stage_x(smem, 1); ld.stage_w(smem, 1, FH);
    WAITV(11); BAR();
    ld.stage_x(smem, 2); ld.stage_w(smem, 2, FH);
    ld.stage_x(smem, 3); ld.stage_w(smem, 3, FH);
    {
      const int c = bid * 196 + min(lw * 64 + l, 195);
      blds16(ld.rw, smem + XW_LDS + 4096, (unsigned)((c / 112) * F_ + (c % 112) * 8) * 2u, 0);
      blds16(make_rsrc(w_out, FH * D_ * 2), smem + XW_LDS + 4096, (unsigned)c * 16u, 0);
    }
    WAITV(18); BAR();
    ld.stage_w(smem, 0, 0);
    WAITV(14); BAR();
    ld.stage_w(smem, 1, 0);
    WAITV(16); BAR();
    ld.stage_w(smem, 2, 0);
    WAITV(14); BAR();
    ld.stage_w(smem, 3, 0);
    WAITV(10); BAR();
    LOAD_W(0) LOAD_W(1)
#pragma unroll
    for (int i = 0; i < 8; ++i) blds16(rsA, smem + XW_LDS + 4096, (unsigned)(lw * 64 + l) * 16u, bid * 32768 + i * 4096);
    WAITV(19); BAR();
    LOAD_W(2) LOAD_W(3) LOAD_W(4)
    WAITV(28); BAR();
    LOAD_W(5) LOAD_W(6)
    BAR();
    LOAD_W(7) LOAD_W(8)
    WAITV(32); BAR();
    WAITV(28); BAR();
    LOAD_W(9) WAITV(28); BAR();
    WAITV(24); BAR();
    LOAD_W(10) WAITV(24); BAR();
    WAITV(20); BAR();
    LOAD_W(11) WAITV(20); BAR();
    WAITV(16); BAR();
    LOAD_W(12) WAITV(16); BAR();
    WAITV(12); BAR();
    LOAD_W(13) WAITV(12); BAR();
    LOAD_W(14) WAITV(12); BAR();
    LOAD_W(15) WAITV(12); BAR();
    LOAD_O(0) WAITV(12); BAR();
    LOAD_O(1) WAITV(12); BAR();
    LOAD_O(2) WAITV(12); BAR();
    LOAD_O(3) BAR();
    LOAD_O(4) BAR();
    LOAD_O(5) LOAD_O(6)
    WAITV(0); BAR();
    return;
  }

  const int fg = w & 3, kg = w >> 2, g = l >> 4, c16 = l & 15;
  const bool two = fg < 3;
  STAMPR(0, 0); STAMP(0, 1);
  const u64 nonce = __hip_atomic_load((const gu64*)sync, RLX_AGENT);
  const u64 tag = (((u64)(unsigned)__builtin_amdgcn_readfirstlane((unsigned)(nonce >> 32)) << 32) |
                   (unsigned)__builtin_amdgcn_readfirstlane((unsigned)nonce)) + 1;
  u64* sflag = sync + 32;
  u64* zflag = sflag + 256 * 32;
  u64* hflag = zflag + 256 * 32;
  const rsrc_t rsN = make_rsrc(z2N, S_ * FH * 2);
  const rsrc_t rsN1 = make_rsrc(z2N, two ? S_ * FH * 2 : 0);
  f32x2* sst = (f32x2*)(smem + XW_LDS);

  u32x4 zp[2];
  {
    const XwAddr<true> ad(l, fg, kg);
    f32x4 acc[2][2];
#pragma unroll
    for (int rt = 0; rt < 2; ++rt)
#pragma unroll
      for (int jj = 0; jj < 2; ++jj) acc[rt][jj] = f32x4{0.f, 0.f, 0.f, 0.f};
    BAR(); STAMP(0, 2); xw_compute<true, 0, 4>(ad, smem, acc);
    BAR(); xw_compute<true, 4, 4>(ad, smem, acc);
    BAR(); xw_compute<true, 8, 4>(ad, smem, acc);
    BAR(); xw_compute<true, 12, 2>(ad, smem, acc);
#pragma unroll
    for (int jj = 0; jj < 2; ++jj)
      zp[jj] = u32x4{gelu_pair(acc[0][jj][0], acc[0][jj][1]), gelu_pair(acc[0][jj][2], acc[0][jj][3]),
                     gelu_pair(acc[1][jj][0], acc[1][jj][1]), gelu_pair(acc[1][jj][2], acc[1][jj][3])};
    if (!two) zp[1] = u32x4{0u, 0u, 0u, 0u};
    float v[16];
#pragma unroll
    for (int e = 0; e < 4; ++e) {
      const f32x2 a0 = {bf_lo(zp[0][e]), bf_hi(zp[0][e])}, a1 = {bf_lo(zp[1][e]), bf_hi(zp[1][e])};
      const f32x2 s1 = a0 + a1, s2 = __builtin_elementwise_fma(a0, a0, a1 * a1);
      v[4 * e] = s1[0]; v[4 * e + 1] = s2[0]; v[4 * e + 2] = s1[1]; v[4 * e + 3] = s2[1];
    }
#pragma unroll
    for (int i = 0; i < 8; ++i) { const bool b = c16 & 1; v[i] = (b ? v[i + 8] : v[i]) + dpp_mov<0xB1>(b ? v[i] : v[i + 8]); }
#pragma unroll
    for (int i = 0; i < 4; ++i) { const bool b = c16 & 2; v[i] = (b ? v[i + 4] : v[i]) + dpp_mov<0x4E>(b ? v[i] : v[i + 4]); }
#pragma unroll
    for (int i = 0; i < 2; ++i) { const bool b = c16 & 4; v[i] = (b ? v[i + 2] : v[i]) + dpp_mov<0x124>(b ? v[i] : v[i + 2]); }
    { const bool b = c16 & 8; v[0] = (b ? v[1] : v[0]) + dpp_mov<0x128>(b ? v[0] : v[1]); }
    const int idx = ((c16 & 1) << 3) | ((c16 & 2) << 1) | ((c16 & 4) >> 1) | (c16 >> 3);
    ((float*)(sst + fg * 64 + 32 * kg + 8 * g))[idx] = v[0];
  }
  STAMP(0, 3);

  u32x2 z1p[2][2];
  {
    const XwAddr<false> ad(l, fg, kg);
    f32x4 za[2][2];
#pragma unroll
    for (int i = 0; i < 2; ++i)
#pragma unroll
      for (int j = 0; j < 2; ++j) za[i][j] = f32x4{0.f, 0.f, 0.f, 0.f};
    BAR();
    STAMP(0, 13);
    const unsigned tagA = (unsigned)tag, tagB = (unsigned)(tag >> 32) ^ (tagA * 0x9E3779B1u);
    if (w == 7) {
      const f32x2 t = sst[l] + sst[64 + l] + sst[128 + l] + sst[192 + l];
      const float m2 = fmaxf(t[1] - t[0] * t[0] * (1.0f / 112.0f), 0.f);
      gu64* gp = (gu64*)stats + (size_t)(rb * 8 + cb) * 128 + l;
      __hip_atomic_store(gp, ((u64)tagA << 32) | __builtin_bit_cast(unsigned, t[0]), RLX_AGENT);
      __hip_atomic_store(gp + 64, ((u64)tagB << 32) | __builtin_bit_cast(unsigned, m2), RLX_AGENT);
    }
    xw_compute<false, 0, 4>(ad, smem, za);
    BAR(); STAMP(0, 14);
    f32x2* mrs = (f32x2*)(smem + XW_LDS + 2048);
    const gu64* gq = (const gu64*)stats + (size_t)rb * 1024 + l;
    u64 gv[16];
    if (w == 0) {
#pragma unroll
      for (int i = 0; i < 16; ++i) gv[i] = __hip_atomic_load(gq + 64 * i, RLX_AGENT);
    }
    xw_compute<false, 4, 4>(ad, smem, za);
    if (w == 0) {
      for (unsigned spins = 0; spins < (1u << 19); ++spins) {
        bool ok = true;
#pragma unroll
        for (int i = 0; i < 16; ++i) ok &= (unsigned)(gv[i] >> 32) == ((i & 1) ? tagB : tagA);
        if (__all(ok)) break;
#pragma unroll
        for (int i = 0; i < 16; ++i) gv[i] = __hip_atomic_load(gq + 64 * i, RLX_AGENT);
      }
      float sum = 0.f, m2 = 0.f;
#pragma unroll
      for (int i = 0; i < 8; ++i) {
        sum += __builtin_bit_cast(float, (unsigned)gv[2 * i]);
        m2 += __builtin_bit_cast(float, (unsigned)gv[2 * i + 1]);
      }
      const float mean = sum * (1.0f / 896.0f);
#pragma unroll
      for (int i = 0; i < 8; ++i) {
        const float d = __builtin_bit_cast(float, (unsigned)gv[2 * i]) * (1.0f / 112.0f) - mean;
        m2 += 112.0f * d * d;
      }
      const float var = bfr(m2 * (1.0f / 896.0f));
      const float den = bfr(sqrtf(bfr(var + 1e-5f)));
      mrs[l] = f32x2{bfr(mean), 1.0f / den};
    }
    BAR();
    STAMP(0, 4);
    const unsigned zoff = (unsigned)(((cb * 7 + 2 * fg) * 64 + 2 * rb + kg) * 64 + l) * 16u;
#pragma unroll
    for (int jj = 0; jj < 2; ++jj) {
      u32x4 v;
#pragma unroll
      for (int e = 0; e < 4; ++e) {
        const f32x4 mr = *(const f32x4*)(mrs + 32 * kg + 8 * g + 2 * e);
        v[e] = pack2(bfr(bf_lo(zp[jj][e]) - mr[0]) * mr[1], bfr(bf_hi(zp[jj][e]) - mr[2]) * mr[3]);
      }
      __builtin_amdgcn_raw_buffer_store_b128(v, jj ? rsN1 : rsN, zoff + 65536u * jj, 0, 16);
    }
    xw_compute<false, 8, 4>(ad, smem, za);
    BAR();
    xw_compute<false, 12, 2>(ad, smem, za);
#pragma unroll
    for (int i = 0; i < 2; ++i)
#pragma unroll
      for (int j = 0; j < 2; ++j) {
        z1p[i][j] = u32x2{gelu_pair(za[i][j][0], za[i][j][1]), gelu_pair(za[i][j][2], za[i][j][3])};
        asm volatile("" : "+v"(z1p[i][j]));
      }
    WAITV(0);
    BAR();
    if (w == 0) {
      if (l == 0) __hip_atomic_store((gu64*)zflag + (cb * 32 + rb) * 32, tag, RLX_AGENT);
      wait_flags(zflag + cb * 32 * 32, 32, l, tag);
    }
  }
  STAMP(0, 5);

  const unsigned boff = (unsigned)(((cb * 7 + 2 * fg) * 64 + kg) * 64 + l) * 16u;
  u32x4 bst[4][4];
#define LOAD_B(slot, st)                                                                          \
  {                                                                                               \
    const int sk_ = ((st) + koff) & 15;                                                           \
    bst[slot][0] = __builtin_amdgcn_raw_buffer_load_b128(rsN, boff, 4096 * sk_, 16);              \
    bst[slot][1] = __builtin_amdgcn_raw_buffer_load_b128(rsN1, boff + 65536, 4096 * sk_, 16);     \
    bst[slot][2] = __builtin_amdgcn_raw_buffer_load_b128(rsN, boff + 2048, 4096 * sk_, 16);       \
    bst[slot][3] = __builtin_amdgcn_raw_buffer_load_b128(rsN1, boff + 65536 + 2048, 4096 * sk_, 16); \
  }
  int ard[4];
#pragma unroll
  for (int i = 0; i < 4; ++i) ard[i] = (16 * ((i + 2 * kg) & 3) + c16) * 256 + (((4 * kg + g) ^ c16) * 16);
  f32x4 acc[4][2];
#pragma unroll
  for (int i = 0; i < 4; ++i)
#pragma unroll
    for (int j = 0; j < 2; ++j) acc[i][j] = f32x4{0.f, 0.f, 0.f, 0.f};
  bf16x8 af[2][4];
#define READ_A(so, h)                                                                                    \
  {                                                                                                      \
    _Pragma("unroll") for (int i = 0; i < 4; ++i) {                                                      \
      int a_ = ard[i];                                                                                   \
      asm volatile("" : "+v"(a_));   \
      af[h][i] = *(const bf16x8*)(smem + (so) + (a_ ^ (128 * (h))));                                     \
    }                                                                                                    \
    __builtin_amdgcn_sched_barrier(0);                                                                   \
  }
#define MMA(u, h)                                                                                        \
  {                                                                                                      \
    _Pragma("unroll") for (int j = 0; j < 2; ++j) {                                                      \
      const bf16x8 zf = __builtin_bit_cast(bf16x8, bst[u][2 * (h) + j]);                                 \
      _Pragma("unroll") for (int i = 0; i < 4; ++i)                                                      \
        acc[i][j] = __builtin_amdgcn_mfma_f32_16x16x32_bf16(zf, af[h][i], acc[i][j], 0, 0, 0);           \
    }                                                                                                    \
    __builtin_amdgcn_sched_barrier(0);                                                                   \
  }
#define ITER(u, it, PF)                                         \
  {                                                             \
    const int so_ = ring_slot_rt(it);                           \
    BAR();                                                      \
    READ_A(so_, 0)                                              \
    MMA(((u) + 3) & 3, 1)                                       \
    READ_A(so_, 1)                                              \
    if (PF) { LOAD_B(((u) + 3) & 3, (it) + 3) }                 \
    MMA(u, 0)                                                   \
  }
  BAR();
  STAMP(0, 6);
  LOAD_B(0, 0) LOAD_B(1, 1) LOAD_B(2, 2) LOAD_B(3, 3)
  READ_A(ring_slot(0), 0)
  READ_A(ring_slot(0), 1)
  MMA(0, 0)
  STAMP(0, 7);
#pragma unroll 1
  for (int it = 1; it < 13; it += 4) { ITER(1, it, true) ITER(2, it + 1, true) ITER(3, it + 2, true) ITER(0, it + 3, true) }
  STAMP(0, 8);
  ITER(1, 13, false)
  int lz = __builtin_amdgcn_mbcnt_hi(~0u, __builtin_amdgcn_mbcnt_lo(~0u, 0u));
  asm volatile("" : "+v"(lz));
  float bias[2];
#pragma unroll
  for (int i = 0; i < 2; ++i) bias[i] = bf_lo(b_sp[(rb * 4 + 2 * kg + i) * 16 + (lz & 15)]);
  ITER(2, 14, false) ITER(3, 15, false)
  MMA(3, 1)
  STAMP(0, 9);
  BAR();
  char* redw = smem + (kg ? ring_slot(14) : ring_slot(15)) + fg * 4096 + lz * 16;
  const char* redr = smem + (kg ? ring_slot(15) : ring_slot(14)) + fg * 4096 + lz * 16;
#pragma unroll
  for (int i = 0; i < 2; ++i)
#pragma unroll
    for (int j = 0; j < 2; ++j) *(f32x4*)(redw + (2 * i + j) * 1024) = acc[2 + i][j];
  BAR();
  const rsrc_t rsH = make_rsrc(hF, S_ * FH * 2);
#pragma unroll
  for (int i = 0; i < 2; ++i) {
    u32x2 hv[2];
#pragma unroll
    for (int j = 0; j < 2; ++j) {
      const f32x4 a = acc[i][j] + *(const f32x4*)(redr + (2 * i + j) * 1024);
      const unsigned g01 = pack2(a[0], a[1]), g23 = pack2(a[2], a[3]);
      const unsigned s01 = pack2(bf_lo(g01) + bias[i], bf_hi(g01) + bias[i]);
      const unsigned s23 = pack2(bf_lo(g23) + bias[i], bf_hi(g23) + bias[i]);
      hv[j] = u32x2{pack2(bf_lo(z1p[i][j][0]) * bf_lo(s01), bf_hi(z1p[i][j][0]) * bf_hi(s01)),
                    pack2(bf_lo(z1p[i][j][1]) * bf_lo(s23), bf_hi(z1p[i][j][1]) * bf_hi(s23))};
    }
    const unsigned ho = (unsigned)(((rb * 4 + 2 * kg + i) * 28 + 7 * (cb >> 1)) * 64 + lz) * 16u;
    if (two) __builtin_amdgcn_raw_buffer_store_b128(u32x4{hv[0][0], hv[0][1], hv[1][0], hv[1][1]}, rsH, ho + (3 * (cb & 1) + fg) * 1024u, 0, 16);
    else     __builtin_amdgcn_raw_buffer_store_b64(hv[0], rsH, ho + 6 * 1024u + 8u * (cb & 1), 0, 16);
  }
  {
    const int tq = w & 3, gz = lz >> 4, cz = lz & 15;
    const int rrow = 4 * gz + (cz >> 2);
    int rb4[4];
#pragma unroll
    for (int j = 0; j < 4; ++j) rb4[j] = rrow * 128 + ((j ^ ((rrow >> 1) & 3)) * 32) + 8 * (lz & 3);
#define OUT_TILE(t) (ring_slot(((t) >> 3) + 7) + ((t) & 7) * 2048)
#define OUT_FRAG(dst, ks)                                                                                 \
  {                                                                                                       \
    constexpr int pc_ = (ks) / 7, e_ = (ks) % 7;                                                          \
    constexpr int tA_ = (e_ < 6) ? (14 * pc_ + 7 * (e_ / 3) + 2 * (e_ % 3)) : (14 * pc_ + 6);             \
    constexpr int tB_ = (e_ < 6) ? (tA_ + 1) : (14 * pc_ + 13);                                           \
    _Pragma("unroll") for (int j = 0; j < 4; ++j)                                                         \
      dst[j] = tr_frag(smem + rb4[j] + OUT_TILE(tA_), smem + rb4[j] + OUT_TILE(tB_));                     \
  }
    BAR();
    bf16x8 wpre[5][4];
    if (kg == 0) { OUT_FRAG(wpre[0], 0) OUT_FRAG(wpre[1], 2) OUT_FRAG(wpre[2], 4) OUT_FRAG(wpre[3], 6) OUT_FRAG(wpre[4], 8) }
    else         { OUT_FRAG(wpre[0], 1) OUT_FRAG(wpre[1], 3) OUT_FRAG(wpre[2], 5) OUT_FRAG(wpre[3], 7) OUT_FRAG(wpre[4], 9) }
    WAITV(0);
    BAR();
    STAMP(0, 10);
    if (w == 0) {
      if (lz == 0) __hip_atomic_store((gu64*)hflag + (rb * 8 + cb) * 32, tag, RLX_AGENT);
      if (cb < 7) wait_flags(hflag + rb * 8 * 32, 8, lz, tag);
    }
    if (cb == 7) return;
    BAR();
    STAMP(0, 11);
    const unsigned ho = (unsigned)(((rb * 4 + tq) * 28 + kg) * 64 + lz) * 16u;
    u32x4 hf[14];
#pragma unroll
    for (int i = 0; i < 14; ++i) hf[i] = __builtin_amdgcn_raw_buffer_load_b128(rsH, ho, 2048 * i, 16);
    f32x4 oc[4];
#pragma unroll
    for (int j = 0; j < 4; ++j) oc[j] = f32x4{0.f, 0.f, 0.f, 0.f};
#pragma unroll
    for (int i = 0; i < 5; ++i)
#pragma unroll
      for (int j = 0; j < 4; ++j)
        oc[j] = __builtin_amdgcn_mfma_f32_16x16x32_bf16(wpre[i][j], __builtin_bit_cast(bf16x8, hf[i]), oc[j], 0, 0, 0);
#define OUT_STEP(i, ks)                                                                                   \
  {                                                                                                       \
    bf16x8 wf[4];                                                                                         \
    OUT_FRAG(wf, ks)                                                                                      \
    _Pragma("unroll") for (int j = 0; j < 4; ++j)                                                         \
      oc[j] = __builtin_amdgcn_mfma_f32_16x16x32_bf16(wf[j], __builtin_bit_cast(bf16x8, hf[i]), oc[j], 0, 0, 0); \
  }
#define OUT_ALL(par)                                                                                       \
  OUT_STEP(5, 10 + par) OUT_STEP(6, 12 + par) OUT_STEP(7, 14 + par) OUT_STEP(8, 16 + par) OUT_STEP(9, 18 + par) \
  OUT_STEP(10, 20 + par) OUT_STEP(11, 22 + par) OUT_STEP(12, 24 + par) OUT_STEP(13, 26 + par)
    if (kg == 0) { OUT_ALL(0) } else { OUT_ALL(1) }
    char* red = smem + ring_slot(14);
    *(f32x4*)(red + (w ^ 4) * 2048 + lz * 16) = kg ? oc[0] : oc[2];
    *(f32x4*)(red + (w ^ 4) * 2048 + 1024 + lz * 16) = kg ? oc[1] : oc[3];
    BAR();
    const f32x4 a0 = (kg ? oc[2] : oc[0]) + *(const f32x4*)(red + w * 2048 + lz * 16);
    const f32x4 a1 = (kg ? oc[3] : oc[1]) + *(const f32x4*)(red + w * 2048 + 1024 + lz * 16);
    u16* op = out + (size_t)((rb * 4 + tq) * 16 + cz) * D_ + cb * 64 + 32 * kg + 4 * gz;
    *(u32x2*)op = u32x2{pack2(a0[0], a0[1]), pack2(a0[2], a0[3])};
    *(u32x2*)(op + 16) = u32x2{pack2(a1[0], a1[1]), pack2(a1[2], a1[3])};
  }
  if (bid == 0 && w == 0 && lz == 0) __hip_atomic_store((gu64*)sync, tag, RLX_AGENT);
  STAMP(0, 12); STAMPR(0, 15);
}

extern "C" void kernel_launch(void* const* d_in, const int* in_sizes, int n_in, void* d_out, int out_size,
                              void* d_ws, size_t ws_size, hipStream_t stream) {
  const u16* x = (const u16*)d_in[0];
  const u16* w_in = (const u16*)d_in[1];
  const u16* w_sp = (const u16*)d_in[2];
  const u16* b_sp = (const u16*)d_in[3];
  const u16* w_out = (const u16*)d_in[4];
  char* ws = (char*)d_ws;
  u16* z2N = (u16*)ws;
  u16* hF = (u16*)(ws + (size_t)4 * 1024 * 1024);
  float* stats = (float*)(ws + (size_t)8 * 1024 * 1024);
  u64* sync = (u64*)(ws + (size_t)9 * 1024 * 1024);
  gmlp_fused<<<256, 768, 0, stream>>>(x, w_in, w_sp, b_sp, w_out, (u16*)d_out, stats, z2N, hF, sync);
}
```

```cpp
#include <hip/hip_runtime.h>
#include <hip/hip_bf16.h>

typedef __bf16 bf16x8 __attribute__((ext_vector_type(8)));
typedef __bf16 bf16x4 __attribute__((ext_vector_type(4)));
typedef __bf16 bf16x2 __attribute__((ext_vector_type(2)));
typedef float f32x4 __attribute__((ext_vector_type(4)));
typedef float f32x2 __attribute__((ext_vector_type(2)));
typedef unsigned u32x4 __attribute__((ext_vector_type(4)));
typedef unsigned u32x2 __attribute__((ext_vector_type(2)));
typedef unsigned short u16;
#define LAS __attribute__((address_space(3)))
#define DI __device__ __forceinline__

constexpr int S_ = 2048, D_ = 448, F_ = 1792, FH = 896;

DI float bf_lo(unsigned u) { return __builtin_bit_cast(float, u << 16); }
DI float bf_hi(unsigned u) { return __builtin_bit_cast(float, u & 0xffff0000u); }
DI unsigned pack2(float a, float b) {
  f32x2 f = {a, b};
  return __builtin_bit_cast(unsigned, __builtin_convertvector(f, bf16x2));
}
DI float bfr(float a) { return (float)(__bf16)a; }
DI bf16x8 tr_frag(const char* a0, const char* a1) {
  bf16x4 lo = __builtin_amdgcn_ds_read_tr16_b64_v4bf16((LAS bf16x4*)a0);
  bf16x4 hi = __builtin_amdgcn_ds_read_tr16_b64_v4bf16((LAS bf16x4*)a1);
  return __builtin_shufflevector(lo, hi, 0, 1, 2, 3, 4, 5, 6, 7);
}
template <int CTRL> DI float dpp_mov(float x) {
  return __builtin_bit_cast(float, __builtin_amdgcn_update_dpp(0, __builtin_bit_cast(int, x), CTRL, 0xf, 0xf, true));
}

#define BAR()                                              \
  do {                                                     \
    asm volatile("s_waitcnt lgkmcnt(0)" ::: "memory");     \
    __builtin_amdgcn_s_barrier();                          \
    asm volatile("" ::: "memory");                         \
  } while (0)
#define WAITV(n) asm volatile("s_waitcnt vmcnt(" #n ")" ::: "memory")
typedef __amdgpu_buffer_rsrc_t rsrc_t;
DI rsrc_t make_rsrc(const void* p, unsigned bytes) { return __builtin_amdgcn_make_buffer_rsrc((void*)p, 0, bytes, 0x00020000); }
DI void blds16(rsrc_t rs, char* lds_wave_base, unsigned voff, unsigned soff) {
  __builtin_amdgcn_raw_ptr_buffer_load_lds(rs, (LAS void*)lds_wave_base, 16, voff, soff, 0, 0);
}

constexpr int XW_XB = 7 * 8192, XW_WB = 448 * 224, XW_LDS = XW_XB + XW_WB;

DI int x_swz(int row) { return ((row >> 1) & 1) | (((row >> 3) & 1) << 1) | ((((row >> 2) ^ (row >> 4)) & 1) << 2); }

struct XwLoader {
  rsrc_t rx, rw;
  unsigned xo[4], wo[7];
  int lw;
  DI XwLoader(const u16* x, const u16* w_in, int rb, int n0, int lw_, int l) : lw(lw_) {
    rx = make_rsrc(x, S_ * D_ * 2);
    rw = make_rsrc(w_in, D_ * F_ * 2);
#pragma unroll
    for (int i = 0; i < 4; ++i) {
      const int pc = 4 * i + lw, row = 8 * (pc & 7) + (l >> 3);
      xo[i] = (unsigned)((rb * 64 + row) * D_ + (pc >> 3) * 64 + (((l & 7) ^ x_swz(row)) * 8)) * 2u;
    }
#pragma unroll
    for (int i = 0; i < 7; ++i) {
      const int c = (4 * i + lw) * 64 + l, row = c / 14, cc = c - row * 14;
      wo[i] = (unsigned)(row * F_ + n0 + cc * 8) * 2u;
    }
  }
  DI void stage_x(char* smem, int s) const {
#pragma unroll
    for (int i = 0; i < (s < 3 ? 4 : 2); ++i) blds16(rx, smem + 2 * s * 8192 + (4 * i + lw) * 1024, xo[i], 256 * s);
  }
  DI void stage_w(char* smem, int s, int col0) const {
#pragma unroll
    for (int i = 0; i < (s < 3 ? 7 : 4); ++i)
      if (s < 3 || 4 * i + lw < 14) blds16(rw, smem + XW_XB + (28 * s + 4 * i + lw) * 1024, wo[i], 128 * F_ * 2 * s + col0 * 2);
  }
};
template <bool Z2> struct XwAddr {
  int xa[2], wa[2];
  DI XwAddr(int l, int fg, int mh) {
    const int g = l >> 4, c16 = l & 15, q = c16 >> 2, p = l & 3;
#pragma unroll
    for (int rt = 0; rt < 2; ++rt) {
      const int row = 32 * mh + (Z2 ? (8 * q + 4 * rt + p) : (16 * rt + c16));
      xa[rt] = row * 128 + (((g >> 1) ^ x_swz(row)) * 16) + (g & 1) * 8;
    }
#pragma unroll
    for (int jj = 0; jj < 2; ++jj) wa[jj] = XW_XB + (4 * g + q) * 224 + min(2 * fg + jj, 6) * 32 + 8 * p;
  }
};
template <int OFF> DI u32x2 lds_read64(unsigned addr) {
  u32x2 r;
  asm volatile("ds_read_b64 %0, %1 offset:%2" : "=v"(r) : "v"(addr), "i"(OFF) : "memory");
  return r;
}
template <bool Z2, int KS0, int NKS>
DI void xw_compute(const XwAddr<Z2>& ad, const char* smem, f32x4 (&acc)[2][2]) {
  u32x2 xl[NKS][2], xh[NKS][2];
  bf16x8 wf[NKS][2];
  const unsigned sb = (unsigned)(size_t)smem;
#pragma unroll
  for (int k = 0; k < NKS; ++k) {
#pragma unroll
    for (int rt = 0; rt < 2; ++rt) {
      const unsigned o = sb + (ad.xa[rt] ^ (64 * ((KS0 + k) & 1)));
      if (k < 2) { xl[k][rt] = lds_read64<8192 * (KS0 >> 1)>(o); xh[k][rt] = lds_read64<8192 * (KS0 >> 1)>(o ^ 32); }
      else       { xl[k][rt] = lds_read64<8192 * (KS0 >> 1) + 8192>(o); xh[k][rt] = lds_read64<8192 * (KS0 >> 1) + 8192>(o ^ 32); }
    }
#pragma unroll
    for (int jj = 0; jj < 2; ++jj) {
      const char* a = smem + ad.wa[jj] + 32 * (KS0 + k) * 224;
      wf[k][jj] = tr_frag(a, a + 16 * 224);
    }
  }
  asm volatile("s_waitcnt lgkmcnt(0)" ::: "memory");
  __builtin_amdgcn_sched_barrier(0);
#pragma unroll
  for (int k = 0; k < NKS; ++k)
#pragma unroll
    for (int rt = 0; rt < 2; ++rt) {
      const bf16x8 xf = __builtin_bit_cast(bf16x8, u32x4{xl[k][rt][0], xl[k][rt][1], xh[k][rt][0], xh[k][rt][1]});
#pragma unroll
      for (int jj = 0; jj < 2; ++jj) {
        if (Z2) acc[rt][jj] = __builtin_amdgcn_mfma_f32_16x16x32_bf16(xf, wf[k][jj], acc[rt][jj], 0, 0, 0);
        else    acc[rt][jj] = __builtin_amdgcn_mfma_f32_16x16x32_bf16(wf[k][jj], xf, acc[rt][jj], 0, 0, 0);
      }
    }
}
DI unsigned gelu_pair(float a, float b) {
  const unsigned u = pack2(a, b);
  const f32x2 z = {bf_lo(u), bf_hi(u)};
  const f32x2 y = z * __builtin_elementwise_fma(z * z, f32x2{-0.10294324f, -0.10294324f}, f32x2{-2.3022082f, -2.3022082f});
  const f32x2 d = f32x2{__builtin_amdgcn_exp2f(y[0]), __builtin_amdgcn_exp2f(y[1])} + f32x2{1.0f, 1.0f};
  const f32x2 o = z * f32x2{__builtin_amdgcn_rcpf(d[0]), __builtin_amdgcn_rcpf(d[1])};
  return pack2(o[0], o[1]);
}

DI void tile_of_block(int bid, int& rb, int& cb) {
  const int xcd = bid & 7, idx = bid >> 3;
  rb = (xcd >> 1) * 8 + (idx & 7);
  cb = (xcd & 1) * 4 + (idx >> 3);
}

typedef unsigned long long u64;
typedef __attribute__((address_space(1))) u64 gu64;
#define RLX_AGENT __ATOMIC_RELAXED, __HIP_MEMORY_SCOPE_AGENT
DI constexpr int ring_slot(int st) {
  return 16384 * ((st % 9) == 0 ? 0 : (st % 9) == 1 ? 4 : (st % 9) == 2 ? 1 : (st % 9) == 3 ? 5 : (st % 9) == 4 ? 6 :
                  (st % 9) == 5 ? 2 : (st % 9) == 6 ? 7 : (st % 9) == 7 ? 3 : 8);
}
DI void wait_flags(const u64* f, int n, int l, u64 tag) {
  const gu64* fp = (const gu64*)f + (l < n ? l : 0) * 32;
  for (unsigned spins = 0; spins < (1u << 20); ++spins) {
    if (__all(__hip_atomic_load(fp, RLX_AGENT) == tag)) break;
    __builtin_amdgcn_s_sleep(4);
  }
}

__global__ __launch_bounds__(768, 3) void gmlp_fused(const u16* __restrict__ x, const u16* __restrict__ w_in,
                                                    const u16* __restrict__ w_sp, const u16* __restrict__ b_sp,
                                                    const u16* __restrict__ w_out, u16* __restrict__ out,
                                                    float* stats, u16* z2N, u16* hF, u64* sync) {
  __shared__ __attribute__((aligned(16))) char smem[XW_LDS + 4096 + 1024];
  const int bid = blockIdx.x;
  int rb, cb;
  tile_of_block(bid, rb, cb);
  const int tid = threadIdx.x, l = tid & 63, w = __builtin_amdgcn_readfirstlane(tid >> 6);
  const int t0 = rb * 64;
  const int koff = (4 * (rb & 3)) & 15;

  if (w >= 8) {
    const int lw = w - 8;
    const XwLoader ld(x, w_in, rb, cb * 112, lw, l);
    const rsrc_t rsA = make_rsrc(w_sp, S_ * S_ * 2);
    const rsrc_t rsO = make_rsrc(w_out, cb < 7 ? FH * D_ * 2 : 0);
    unsigned ap[4];
#pragma unroll
    for (int i = 0; i < 4; ++i) {
      const int row = 4 * (4 * i + lw) + (l >> 4);
      ap[i] = (unsigned)((t0 + row) * S_ + (((l & 15) ^ (row & 15)) * 8)) * 2u;
    }
    const unsigned oo = (unsigned)((8 * lw + (l >> 3)) * D_ + cb * 64 + (((l & 7) ^ (((l >> 4) & 3) << 1)) * 8)) * 2u;
#define LOAD_W(st)                                                                                \
  {                                                                                               \
    const int sk_ = ((st) + koff) & 15;                                                           \
    char* dst_ = smem + ring_slot(st) + lw * 1024;                                                \
    _Pragma("unroll") for (int i = 0; i < 4; ++i) blds16(rsA, dst_ + i * 4096, ap[i], 256 * sk_); \
  }
#define LOAD_O(c)        \
  {                                                                                                           \
    char* dst_ = smem + ring_slot((c) + 7) + lw * 1024;                                                       \
    _Pragma("unroll") for (int i = 0; i < 4; ++i) blds16(rsO, dst_ + i * 4096, oo, (128 * (c) + 32 * i) * D_ * 2); \
  }
    ld.stage_x(smem, 0); ld.stage_w(smem, 0, FH);
    ld.stage_x(smem, 1); ld.stage_w(smem, 1, FH);
    WAITV(11); BAR();
    ld.stage_x(smem, 2); ld.stage_w(smem, 2, FH);
    ld.stage_x(smem, 3); ld.stage_w(smem, 3, FH);
    {
      const int c = bid * 196 + min(lw * 64 + l, 195);
      blds16(ld.rw, smem + XW_LDS + 4096, (unsigned)((c / 112) * F_ + (c % 112) * 8) * 2u, 0);
      blds16(make_rsrc(w_out, FH * D_ * 2), smem + XW_LDS + 4096, (unsigned)c * 16u, 0);
    }
    WAITV(18); BAR();
    ld.stage_w(smem, 0, 0);
    WAITV(14); BAR();
    ld.stage_w(smem, 1, 0);
    WAITV(16); BAR();
    ld.stage_w(smem, 2, 0);
    WAITV(14); BAR();
    ld.stage_w(smem, 3, 0);
    WAITV(10); BAR();
    LOAD_W(0) LOAD_W(1)
#pragma unroll
    for (int i = 0; i < 8; ++i) blds16(rsA, smem + XW_LDS + 4096, (unsigned)(lw * 64 + l) * 16u, bid * 32768 + i * 4096);
    WAITV(19); BAR();
    LOAD_W(2) LOAD_W(3) LOAD_W(4)
    WAITV(28); BAR();
    LOAD_W(5) LOAD_W(6)
    BAR();
    LOAD_W(7) LOAD_W(8)
    WAITV(32); BAR();
    WAITV(28); BAR();
    LOAD_W(9) WAITV(28); BAR();
    WAITV(24); BAR();
    LOAD_W(10) WAITV(24); BAR();
    WAITV(20); BAR();
    LOAD_W(11) WAITV(20); BAR();
    WAITV(16); BAR();
    LOAD_W(12) WAITV(16); BAR();
    WAITV(12); BAR();
    LOAD_W(13) WAITV(12); BAR();
    LOAD_W(14) WAITV(12); BAR();
    LOAD_W(15) WAITV(12); BAR();
    LOAD_O(0) WAITV(12); BAR();
    LOAD_O(1) WAITV(12); BAR();
    LOAD_O(2) WAITV(12); BAR();
    LOAD_O(3) BAR();
    LOAD_O(4) BAR();
    LOAD_O(5) LOAD_O(6)
    WAITV(0); BAR();
    return;
  }

  const int fg = w & 3, kg = w >> 2, g = l >> 4, c16 = l & 15;
  const bool two = fg < 3;

  const u64 nonce = __hip_atomic_load((const gu64*)sync, RLX_AGENT);
  const u64 tag = (((u64)(unsigned)__builtin_amdgcn_readfirstlane((unsigned)(nonce >> 32)) << 32) |
                   (unsigned)__builtin_amdgcn_readfirstlane((unsigned)nonce)) + 1;
  u64* zflag = sync + 32;
  u64* hflag = zflag + 256 * 32;
  const rsrc_t rsN = make_rsrc(z2N, S_ * FH * 2);
  const rsrc_t rsN1 = make_rsrc(z2N, two ? S_ * FH * 2 : 0);
  f32x2* sst = (f32x2*)(smem + XW_LDS);

  u32x4 zp[2];
  {
    const XwAddr<true> ad(l, fg, kg);
    f32x4 acc[2][2];
#pragma unroll
    for (int rt = 0; rt < 2; ++rt)
#pragma unroll
      for (int jj = 0; jj < 2; ++jj) acc[rt][jj] = f32x4{0.f, 0.f, 0.f, 0.f};
    BAR(); xw_compute<true, 0, 4>(ad, smem, acc);
    BAR(); xw_compute<true, 4, 4>(ad, smem, acc);
    BAR(); xw_compute<true, 8, 4>(ad, smem, acc);
    BAR(); xw_compute<true, 12, 2>(ad, smem, acc);
#pragma unroll
    for (int jj = 0; jj < 2; ++jj)
      zp[jj] = u32x4{gelu_pair(acc[0][jj][0], acc[0][jj][1]), gelu_pair(acc[0][jj][2], acc[0][jj][3]),
                     gelu_pair(acc[1][jj][0], acc[1][jj][1]), gelu_pair(acc[1][jj][2], acc[1][jj][3])};
    if (!two) zp[1] = u32x4{0u, 0u, 0u, 0u};
    float v[16];
#pragma unroll
    for (int e = 0; e < 4; ++e) {
      const f32x2 a0 = {bf_lo(zp[0][e]), bf_hi(zp[0][e])}, a1 = {bf_lo(zp[1][e]), bf_hi(zp[1][e])};
      const f32x2 s1 = a0 + a1, s2 = __builtin_elementwise_fma(a0, a0, a1 * a1);
      v[4 * e] = s1[0]; v[4 * e + 1] = s2[0]; v[4 * e + 2] = s1[1]; v[4 * e + 3] = s2[1];
    }
#pragma unroll
    for (int i = 0; i < 8; ++i) { const bool b = c16 & 1; v[i] = (b ? v[i + 8] : v[i]) + dpp_mov<0xB1>(b ? v[i] : v[i + 8]); }
#pragma unroll
    for (int i = 0; i < 4; ++i) { const bool b = c16 & 2; v[i] = (b ? v[i + 4] : v[i]) + dpp_mov<0x4E>(b ? v[i] : v[i + 4]); }
#pragma unroll
    for (int i = 0; i < 2; ++i) { const bool b = c16 & 4; v[i] = (b ? v[i + 2] : v[i]) + dpp_mov<0x124>(b ? v[i] : v[i + 2]); }
    { const bool b = c16 & 8; v[0] = (b ? v[1] : v[0]) + dpp_mov<0x128>(b ? v[0] : v[1]); }
    const int idx = ((c16 & 1) << 3) | ((c16 & 2) << 1) | ((c16 & 4) >> 1) | (c16 >> 3);
    ((float*)(sst + fg * 64 + 32 * kg + 8 * g))[idx] = v[0];
  }

  u32x2 z1p[2][2];
  {
    const XwAddr<false> ad(l, fg, kg);
    f32x4 za[2][2];
#pragma unroll
    for (int i = 0; i < 2; ++i)
#pragma unroll
      for (int j = 0; j < 2; ++j) za[i][j] = f32x4{0.f, 0.f, 0.f, 0.f};
    BAR();

    const unsigned tagA = (unsigned)tag, tagB = (unsigned)(tag >> 32) ^ (tagA * 0x9E3779B1u);
    if (w == 7) {
      const f32x2 t = sst[l] + sst[64 + l] + sst[128 + l] + sst[192 + l];
      const float m2 = fmaxf(t[1] - t[0] * t[0] * (1.0f / 112.0f), 0.f);
      gu64* gp = (gu64*)stats + (size_t)(rb * 8 + cb) * 128 + l;
      __hip_atomic_store(gp, ((u64)tagA << 32) | __builtin_bit_cast(unsigned, t[0]), RLX_AGENT);
      __hip_atomic_store(gp + 64, ((u64)tagB << 32) | __builtin_bit_cast(unsigned, m2), RLX_AGENT);
    }
    xw_compute<false, 0, 4>(ad, smem, za);
    BAR();
    f32x2* mrs = (f32x2*)(smem + XW_LDS + 2048);
    const gu64* gq = (const gu64*)stats + (size_t)rb * 1024 + l;
    u64 gv[16];
    if (w == 0) {
#pragma unroll
      for (int i = 0; i < 16; ++i) gv[i] = __hip_atomic_load(gq + 64 * i, RLX_AGENT);
    }
    xw_compute<false, 4, 4>(ad, smem, za);
    if (w == 0) {
      for (unsigned spins = 0; spins < (1u << 19); ++spins) {
        bool ok = true;
#pragma unroll
        for (int i = 0; i < 16; ++i) ok &= (unsigned)(gv[i] >> 32) == ((i & 1) ? tagB : tagA);
        if (__all(ok)) break;
#pragma unroll
        for (int i = 0; i < 16; ++i) gv[i] = __hip_atomic_load(gq + 64 * i, RLX_AGENT);
      }
      float sum = 0.f, m2 = 0.f;
#pragma unroll
      for (int i = 0; i < 8; ++i) {
        sum += __builtin_bit_cast(float, (unsigned)gv[2 * i]);
        m2 += __builtin_bit_cast(float, (unsigned)gv[2 * i + 1]);
      }
      const float mean = sum * (1.0f / 896.0f);
#pragma unroll
      for (int i = 0; i < 8; ++i) {
        const float d = __builtin_bit_cast(float, (unsigned)gv[2 * i]) * (1.0f / 112.0f) - mean;
        m2 += 112.0f * d * d;
      }
      const float var = bfr(m2 * (1.0f / 896.0f));
      const float den = bfr(sqrtf(bfr(var + 1e-5f)));
      mrs[l] = f32x2{bfr(mean), 1.0f / den};
    }
    BAR();

    const unsigned zoff = (unsigned)(((cb * 7 + 2 * fg) * 64 + 2 * rb + kg) * 64 + l) * 16u;
#pragma unroll
    for (int jj = 0; jj < 2; ++jj) {
      u32x4 v;
#pragma unroll
      for (int e = 0; e < 4; ++e) {
        const f32x4 mr = *(const f32x4*)(mrs + 32 * kg + 8 * g + 2 * e);
        v[e] = pack2(bfr(bf_lo(zp[jj][e]) - mr[0]) * mr[1], bfr(bf_hi(zp[jj][e]) - mr[2]) * mr[3]);
      }
      __builtin_amdgcn_raw_buffer_store_b128(v, jj ? rsN1 : rsN, zoff + 65536u * jj, 0, 16);
    }
    xw_compute<false, 8, 4>(ad, smem, za);
    BAR();
    xw_compute<false, 12, 2>(ad, smem, za);
#pragma unroll
    for (int i = 0; i < 2; ++i)
#pragma unroll
      for (int j = 0; j < 2; ++j) {
        z1p[i][j] = u32x2{gelu_pair(za[i][j][0], za[i][j][1]), gelu_pair(za[i][j][2], za[i][j][3])};
        asm volatile("" : "+v"(z1p[i][j]));
      }
    WAITV(0);
    BAR();
    if (w == 0) {
      if (l == 0) __hip_atomic_store((gu64*)zflag + (cb * 32 + rb) * 32, tag, RLX_AGENT);
      wait_flags(zflag + cb * 32 * 32, 32, l, tag);
    }
  }

  const unsigned boff = (unsigned)(((cb * 7 + 2 * fg) * 64 + kg) * 64 + l) * 16u;
  u32x4 bst[4][4];
#define LOAD_B(slot, st)                                                                          \
  {                                                                                               \
    const int sk_ = ((st) + koff) & 15;                                                           \
    bst[slot][0] = __builtin_amdgcn_raw_buffer_load_b128(rsN, boff, 4096 * sk_, 16);              \
    bst[slot][1] = __builtin_amdgcn_raw_buffer_load_b128(rsN1, boff + 65536, 4096 * sk_, 16);     \
    bst[slot][2] = __builtin_amdgcn_raw_buffer_load_b128(rsN, boff + 2048, 4096 * sk_, 16);       \
    bst[slot][3] = __builtin_amdgcn_raw_buffer_load_b128(rsN1, boff + 65536 + 2048, 4096 * sk_, 16); \
  }
  unsigned ard[2][2][4];
#pragma unroll
  for (int h = 0; h < 2; ++h)
#pragma unroll
    for (int i = 0; i < 4; ++i) {
      ard[0][h][i] = (unsigned)(size_t)smem + (((16 * ((i + 2 * kg) & 3) + c16) * 256 + (((4 * kg + g) ^ c16) * 16)) ^ (128 * h));
      ard[1][h][i] = ard[0][h][i] + 65536u;
      asm volatile("" : "+v"(ard[1][h][i]));
    }
  f32x4 acc[4][2];
#pragma unroll
  for (int i = 0; i < 4; ++i)
#pragma unroll
    for (int j = 0; j < 2; ++j) acc[i][j] = f32x4{0.f, 0.f, 0.f, 0.f};
  bf16x8 af[2][4];
#define READ_A(st, h)                                                                                    \
  {                                                                                                      \
    constexpr int so_ = ring_slot(st);                                                                   \
    _Pragma("unroll") for (int i = 0; i < 4; ++i) {                                                      \
      unsigned a_ = ard[so_ >= 65536][h][i];                                                             \
      if (so_ >= 131072) asm volatile("v_add_u32 %0, 0x10000, %0" : "+v"(a_));                           \
      af[h][i] = *(const LAS bf16x8*)(size_t)(a_ + (so_ & 65535));                                       \
    }                                                                                                    \
    __builtin_amdgcn_sched_barrier(0);                                                                   \
  }
#define MMA(u, h)                                                                                        \
  {                                                                                                      \
    _Pragma("unroll") for (int j = 0; j < 2; ++j) {                                                      \
      const bf16x8 zf = __builtin_bit_cast(bf16x8, bst[u][2 * (h) + j]);                                 \
      _Pragma("unroll") for (int i = 0; i < 4; ++i)                                                      \
        acc[i][j] = __builtin_amdgcn_mfma_f32_16x16x32_bf16(zf, af[h][i], acc[i][j], 0, 0, 0);           \
    }                                                                                                    \
    __builtin_amdgcn_sched_barrier(0);                                                                   \
  }
#define ITER(it)                                                \
  {                                                             \
    BAR();                                                      \
    READ_A(it, 0)                                               \
    MMA(((it) + 3) & 3, 1)                                      \
    READ_A(it, 1)                                               \
    if ((it) + 3 < 16) { LOAD_B(((it) + 3) & 3, (it) + 3) }     \
    MMA((it) & 3, 0)                                            \
  }
  BAR();

  LOAD_B(0, 0) LOAD_B(1, 1) LOAD_B(2, 2) LOAD_B(3, 3)
  READ_A(0, 0)
  READ_A(0, 1)
  MMA(0, 0)

  ITER(1) ITER(2) ITER(3) ITER(4) ITER(5) ITER(6) ITER(7) ITER(8)

  ITER(9) ITER(10) ITER(11) ITER(12) ITER(13)
  int lz = __builtin_amdgcn_mbcnt_hi(~0u, __builtin_amdgcn_mbcnt_lo(~0u, 0u));
  asm volatile("" : "+v"(lz));
  float bias[2];
#pragma unroll
  for (int i = 0; i < 2; ++i) bias[i] = bf_lo(b_sp[(rb * 4 + 2 * kg + i) * 16 + (lz & 15)]);
  ITER(14) ITER(15)
  MMA(3, 1)

  BAR();
  char* redw = smem + (kg ? ring_slot(14) : ring_slot(15)) + fg * 4096 + lz * 16;
  const char* redr = smem + (kg ? ring_slot(15) : ring_slot(14)) + fg * 4096 + lz * 16;
#pragma unroll
  for (int i = 0; i < 2; ++i)
#pragma unroll
    for (int j = 0; j < 2; ++j) *(f32x4*)(redw + (2 * i + j) * 1024) = acc[2 + i][j];
  BAR();
  const rsrc_t rsH = make_rsrc(hF, S_ * FH * 2);
#pragma unroll
  for (int i = 0; i < 2; ++i) {
    u32x2 hv[2];
#pragma unroll
    for (int j = 0; j < 2; ++j) {
      const f32x4 a = acc[i][j] + *(const f32x4*)(redr + (2 * i + j) * 1024);
      const unsigned g01 = pack2(a[0], a[1]), g23 = pack2(a[2], a[3]);
      const unsigned s01 = pack2(bf_lo(g01) + bias[i], bf_hi(g01) + bias[i]);
      const unsigned s23 = pack2(bf_lo(g23) + bias[i], bf_hi(g23) + bias[i]);
      hv[j] = u32x2{pack2(bf_lo(z1p[i][j][0]) * bf_lo(s01), bf_hi(z1p[i][j][0]) * bf_hi(s01)),
                    pack2(bf_lo(z1p[i][j][1]) * bf_lo(s23), bf_hi(z1p[i][j][1]) * bf_hi(s23))};
    }
    const unsigned ho = (unsigned)(((rb * 4 + 2 * kg + i) * 28 + 7 * (cb >> 1)) * 64 + lz) * 16u;
    if (two) __builtin_amdgcn_raw_buffer_store_b128(u32x4{hv[0][0], hv[0][1], hv[1][0], hv[1][1]}, rsH, ho + (3 * (cb & 1) + fg) * 1024u, 0, 16);
    else     __builtin_amdgcn_raw_buffer_store_b64(hv[0], rsH, ho + 6 * 1024u + 8u * (cb & 1), 0, 16);
  }
  {
    const int tq = w & 3, gz = lz >> 4, cz = lz & 15;
    const int rrow = 4 * gz + (cz >> 2);
    int rb4[4];
#pragma unroll
    for (int j = 0; j < 4; ++j) rb4[j] = rrow * 128 + ((j ^ ((rrow >> 1) & 3)) * 32) + 8 * (lz & 3);
#define OUT_TILE(t) (ring_slot(((t) >> 3) + 7) + ((t) & 7) * 2048)
#define OUT_FRAG(dst, ks)                                                                                 \
  {                                                                                                       \
    constexpr int pc_ = (ks) / 7, e_ = (ks) % 7;                                                          \
    constexpr int tA_ = (e_ < 6) ? (14 * pc_ + 7 * (e_ / 3) + 2 * (e_ % 3)) : (14 * pc_ + 6);             \
    constexpr int tB_ = (e_ < 6) ? (tA_ + 1) : (14 * pc_ + 13);                                           \
    _Pragma("unroll") for (int j = 0; j < 4; ++j)                                                         \
      dst[j] = tr_frag(smem + rb4[j] + OUT_TILE(tA_), smem + rb4[j] + OUT_TILE(tB_));                     \
  }
    BAR();
    bf16x8 wpre[5][4];
    if (kg == 0) { OUT_FRAG(wpre[0], 0) OUT_FRAG(wpre[1], 2) OUT_FRAG(wpre[2], 4) OUT_FRAG(wpre[3], 6) OUT_FRAG(wpre[4], 8) }
    else         { OUT_FRAG(wpre[0], 1) OUT_FRAG(wpre[1], 3) OUT_FRAG(wpre[2], 5) OUT_FRAG(wpre[3], 7) OUT_FRAG(wpre[4], 9) }
    WAITV(0);
    BAR();

    if (w == 0) {
      if (lz == 0) __hip_atomic_store((gu64*)hflag + (rb * 8 + cb) * 32, tag, RLX_AGENT);
      if (cb < 7) wait_flags(hflag + rb * 8 * 32, 8, lz, tag);
    }
    if (cb == 7) return;
    BAR();

    const unsigned ho = (unsigned)(((rb * 4 + tq) * 28 + kg) * 64 + lz) * 16u;
    u32x4 hf[14];
#pragma unroll
    for (int i = 0; i < 14; ++i) hf[i] = __builtin_amdgcn_raw_buffer_load_b128(rsH, ho, 2048 * i, 16);
    f32x4 oc[4];
#pragma unroll
    for (int j = 0; j < 4; ++j) oc[j] = f32x4{0.f, 0.f, 0.f, 0.f};
#pragma unroll
    for (int i = 0; i < 5; ++i)
#pragma unroll
      for (int j = 0; j < 4; ++j)
        oc[j] = __builtin_amdgcn_mfma_f32_16x16x32_bf16(wpre[i][j], __builtin_bit_cast(bf16x8, hf[i]), oc[j], 0, 0, 0);
#define OUT_STEP(i, ks)                                                                                   \
  {                                                                                                       \
    bf16x8 wf[4];                                                                                         \
    OUT_FRAG(wf, ks)                                                                                      \
    _Pragma("unroll") for (int j = 0; j < 4; ++j)                                                         \
      oc[j] = __builtin_amdgcn_mfma_f32_16x16x32_bf16(wf[j], __builtin_bit_cast(bf16x8, hf[i]), oc[j], 0, 0, 0); \
  }
#define OUT_ALL(par)                                                                                       \
  OUT_STEP(5, 10 + par) OUT_STEP(6, 12 + par) OUT_STEP(7, 14 + par) OUT_STEP(8, 16 + par) OUT_STEP(9, 18 + par) \
  OUT_STEP(10, 20 + par) OUT_STEP(11, 22 + par) OUT_STEP(12, 24 + par) OUT_STEP(13, 26 + par)
    if (kg == 0) { OUT_ALL(0) } else { OUT_ALL(1) }
    char* red = smem + ring_slot(14);
    *(f32x4*)(red + (w ^ 4) * 2048 + lz * 16) = kg ? oc[0] : oc[2];
    *(f32x4*)(red + (w ^ 4) * 2048 + 1024 + lz * 16) = kg ? oc[1] : oc[3];
    BAR();
    const f32x4 a0 = (kg ? oc[2] : oc[0]) + *(const f32x4*)(red + w * 2048 + lz * 16);
    const f32x4 a1 = (kg ? oc[3] : oc[1]) + *(const f32x4*)(red + w * 2048 + 1024 + lz * 16);
    u16* op = out + (size_t)((rb * 4 + tq) * 16 + cz) * D_ + cb * 64 + 32 * kg + 4 * gz;
    *(u32x2*)op = u32x2{pack2(a0[0], a0[1]), pack2(a0[2], a0[3])};
    *(u32x2*)(op + 16) = u32x2{pack2(a1[0], a1[1]), pack2(a1[2], a1[3])};
  }
  if (bid == 0 && w == 0 && lz == 0) __hip_atomic_store((gu64*)sync, tag, RLX_AGENT);

}

extern "C" void kernel_launch(void* const* d_in, const int* in_sizes, int n_in, void* d_out, int out_size,
                              void* d_ws, size_t ws_size, hipStream_t stream) {
  const u16* x = (const u16*)d_in[0];
  const u16* w_in = (const u16*)d_in[1];
  const u16* w_sp = (const u16*)d_in[2];
  const u16* b_sp = (const u16*)d_in[3];
  const u16* w_out = (const u16*)d_in[4];
  char* ws = (char*)d_ws;
  u16* z2N = (u16*)ws;
  u16* hF = (u16*)(ws + (size_t)4 * 1024 * 1024);
  float* stats = (float*)(ws + (size_t)8 * 1024 * 1024);
  u64* sync = (u64*)(ws + (size_t)9 * 1024 * 1024);
  gmlp_fused<<<256, 768, 0, stream>>>(x, w_in, w_sp, b_sp, w_out, (u16*)d_out, stats, z2N, hF, sync);
}
```

```cpp
#include <hip/hip_runtime.h>
#include <hip/hip_bf16.h>

typedef __bf16 bf16x8 __attribute__((ext_vector_type(8)));
typedef __bf16 bf16x4 __attribute__((ext_vector_type(4)));
typedef __bf16 bf16x2 __attribute__((ext_vector_type(2)));
typedef float f32x4 __attribute__((ext_vector_type(4)));
typedef float f32x2 __attribute__((ext_vector_type(2)));
typedef unsigned u32x4 __attribute__((ext_vector_type(4)));
typedef unsigned u32x2 __attribute__((ext_vector_type(2)));
typedef unsigned short u16;
#define LAS __attribute__((address_space(3)))
#define DI __device__ __forceinline__

#define STAMP(k, i)
#define STAMPR(k, i)
constexpr int S_ = 2048, D_ = 448, F_ = 1792, FH = 896;

DI float bf_lo(unsigned u) { return __builtin_bit_cast(float, u << 16); }
DI float bf_hi(unsigned u) { return __builtin_bit_cast(float, u & 0xffff0000u); }
DI unsigned pack2(float a, float b) {
  f32x2 f = {a, b};
  return __builtin_bit_cast(unsigned, __builtin_convertvector(f, bf16x2));
}
DI float bfr(float a) { return (float)(__bf16)a; }
DI float gelu_tanh(float z) {
  const float y = z * __builtin_fmaf(z * z, -0.10294324f, -2.3022082f);
  const float e = __builtin_amdgcn_exp2f(y);
  return z * __builtin_amdgcn_rcpf(1.0f + e);
}
DI bf16x8 tr_frag(const char* a0, const char* a1) {
  bf16x4 lo = __builtin_amdgcn_ds_read_tr16_b64_v4bf16((LAS bf16x4*)a0);
  bf16x4 hi = __builtin_amdgcn_ds_read_tr16_b64_v4bf16((LAS bf16x4*)a1);
  return __builtin_shufflevector(lo, hi, 0, 1, 2, 3, 4, 5, 6, 7);
}
template <int CTRL> DI float dpp_mov(float x) {
  return __builtin_bit_cast(float, __builtin_amdgcn_update_dpp(0, __builtin_bit_cast(int, x), CTRL, 0xf, 0xf, true));
}
DI float sum16(float x) {
  x += dpp_mov<0xB1>(x);
  x += dpp_mov<0x4E>(x);
  x += dpp_mov<0x141>(x);
  x += dpp_mov<0x140>(x);
  return x;
}

#define BAR()                                              \
  do {                                                     \
    asm volatile("s_waitcnt lgkmcnt(0)" ::: "memory");     \
    __builtin_amdgcn_s_barrier();                          \
    asm volatile("" ::: "memory");                         \
  } while (0)
#define WAITV(n) asm volatile("s_waitcnt vmcnt(" #n ")" ::: "memory")
typedef __amdgpu_buffer_rsrc_t rsrc_t;
DI rsrc_t make_rsrc(const void* p, unsigned bytes) { return __builtin_amdgcn_make_buffer_rsrc((void*)p, 0, bytes, 0x00020000); }
DI void blds16(rsrc_t rs, char* lds_wave_base, unsigned voff, unsigned soff) {
  __builtin_amdgcn_raw_ptr_buffer_load_lds(rs, (LAS void*)lds_wave_base, 16, voff, soff, 0, 0);
}

constexpr int XW_XB = 7 * 8192, XW_WB = 448 * 224, XW_LDS = XW_XB + XW_WB;

DI int x_swz(int row) { return ((row >> 1) & 1) | (((row >> 3) & 1) << 1) | ((((row >> 2) ^ (row >> 4)) & 1) << 2); }

struct XwLoader {
  rsrc_t rx, rw;
  unsigned xo[4], wo[7];
  int lw;
  DI XwLoader(const u16* x, const u16* w_in, int rb, int n0, int lw_, int l) : lw(lw_) {
    rx = make_rsrc(x, S_ * D_ * 2);
    rw = make_rsrc(w_in, D_ * F_ * 2);
#pragma unroll
    for (int i = 0; i < 4; ++i) {
      const int pc = 4 * i + lw, row = 8 * (pc & 7) + (l >> 3);
      xo[i] = (unsigned)((rb * 64 + row) * D_ + (pc >> 3) * 64 + (((l & 7) ^ x_swz(row)) * 8)) * 2u;
    }
#pragma unroll
    for (int i = 0; i < 7; ++i) {
      const int c = (4 * i + lw) * 64 + l, row = c / 14, cc = c - row * 14;
      wo[i] = (unsigned)(row * F_ + n0 + cc * 8) * 2u;
    }
  }
  DI void stage_x(char* smem, int s) const {
#pragma unroll
    for (int i = 0; i < (s < 3 ? 4 : 2); ++i) blds16(rx, smem + 2 * s * 8192 + (4 * i + lw) * 1024, xo[i], 256 * s);
  }
  DI void stage_w(char* smem, int s, int col0) const {
#pragma unroll
    for (int i = 0; i < (s < 3 ? 7 : 4); ++i)
      if (s < 3 || 4 * i + lw < 14) blds16(rw, smem + XW_XB + (28 * s + 4 * i + lw) * 1024, wo[i], 128 * F_ * 2 * s + col0 * 2);
  }
};
template <bool Z2> struct XwAddr {
  int xa[2], wa[2];
  DI XwAddr(int l, int fg, int mh) {
    const int g = l >> 4, c16 = l & 15, q = c16 >> 2, p = l & 3;
#pragma unroll
    for (int rt = 0; rt < 2; ++rt) {
      const int row = 32 * mh + (Z2 ? (8 * q + 4 * rt + p) : (16 * rt + c16));
      xa[rt] = row * 128 + (((g >> 1) ^ x_swz(row)) * 16) + (g & 1) * 8;
    }
#pragma unroll
    for (int jj = 0; jj < 2; ++jj) wa[jj] = XW_XB + (4 * g + q) * 224 + min(2 * fg + jj, 6) * 32 + 8 * p;
  }
};
template <int OFF> DI u32x2 lds_read64(unsigned addr) {
  u32x2 r;
  asm volatile("ds_read_b64 %0, %1 offset:%2" : "=v"(r) : "v"(addr), "i"(OFF) : "memory");
  return r;
}
template <bool Z2, int KS0, int NKS>
DI void xw_compute(const XwAddr<Z2>& ad, const char* smem, f32x4 (&acc)[2][2]) {
  u32x2 xl[NKS][2], xh[NKS][2];
  bf16x8 wf[NKS][2];
  const unsigned sb = (unsigned)(size_t)smem;
#pragma unroll
  for (int k = 0; k < NKS; ++k) {
    constexpr int dummy = 0; (void)dummy;
#pragma unroll
    for (int rt = 0; rt < 2; ++rt) {
      const unsigned o = sb + (ad.xa[rt] ^ (64 * ((KS0 + k) & 1)));
      if (k < 2) { xl[k][rt] = lds_read64<8192 * (KS0 >> 1)>(o); xh[k][rt] = lds_read64<8192 * (KS0 >> 1)>(o ^ 32); }
      else       { xl[k][rt] = lds_read64<8192 * (KS0 >> 1) + 8192>(o); xh[k][rt] = lds_read64<8192 * (KS0 >> 1) + 8192>(o ^ 32); }
    }
#pragma unroll
    for (int jj = 0; jj < 2; ++jj) {
      const char* a = smem + ad.wa[jj] + 32 * (KS0 + k) * 224;
      wf[k][jj] = tr_frag(a, a + 16 * 224);
    }
  }
  asm volatile("s_waitcnt lgkmcnt(0)" ::: "memory");
  __builtin_amdgcn_sched_barrier(0);
#pragma unroll
  for (int k = 0; k < NKS; ++k)
#pragma unroll
    for (int rt = 0; rt < 2; ++rt) {
      const bf16x8 xf = __builtin_bit_cast(bf16x8, u32x4{xl[k][rt][0], xl[k][rt][1], xh[k][rt][0], xh[k][rt][1]});
#pragma unroll
      for (int jj = 0; jj < 2; ++jj) {
        if (Z2) acc[rt][jj] = __builtin_amdgcn_mfma_f32_16x16x32_bf16(xf, wf[k][jj], acc[rt][jj], 0, 0, 0);
        else    acc[rt][jj] = __builtin_amdgcn_mfma_f32_16x16x32_bf16(wf[k][jj], xf, acc[rt][jj], 0, 0, 0);
      }
    }
}
DI unsigned gelu_pair(float a, float b) {
  const unsigned u = pack2(a, b);
  const f32x2 z = {bf_lo(u), bf_hi(u)};
  const f32x2 y = z * __builtin_elementwise_fma(z * z, f32x2{-0.10294324f, -0.10294324f}, f32x2{-2.3022082f, -2.3022082f});
  const f32x2 d = f32x2{__builtin_amdgcn_exp2f(y[0]), __builtin_amdgcn_exp2f(y[1])} + f32x2{1.0f, 1.0f};
  const f32x2 o = z * f32x2{__builtin_amdgcn_rcpf(d[0]), __builtin_amdgcn_rcpf(d[1])};
  return pack2(o[0], o[1]);
}

DI void tile_of_block(int bid, int& rb, int& cb) {
  const int xcd = bid & 7, idx = bid >> 3;
  rb = (xcd >> 1) * 8 + (idx & 7);
  cb = (xcd & 1) * 4 + (idx >> 3);
}

typedef unsigned long long u64;
typedef __attribute__((address_space(1))) u64 gu64;
#define RLX_AGENT __ATOMIC_RELAXED, __HIP_MEMORY_SCOPE_AGENT
DI constexpr int ring_slot(int st) {
  return 16384 * ((st % 9) == 0 ? 0 : (st % 9) == 1 ? 4 : (st % 9) == 2 ? 1 : (st % 9) == 3 ? 5 : (st % 9) == 4 ? 6 :
                  (st % 9) == 5 ? 2 : (st % 9) == 6 ? 7 : (st % 9) == 7 ? 3 : 8);
}
DI void wait_flags(const u64* f, int n, int l, u64 tag) {
  const gu64* fp = (const gu64*)f + (l < n ? l : 0) * 32;
  for (unsigned spins = 0; spins < (1u << 20); ++spins) {
    if (__all(__hip_atomic_load(fp, RLX_AGENT) == tag)) break;
    __builtin_amdgcn_s_sleep(4);
  }
}

__global__ __launch_bounds__(768, 3) void gmlp_fused(const u16* __restrict__ x, const u16* __restrict__ w_in,
                                                    const u16* __restrict__ w_sp, const u16* __restrict__ b_sp,
                                                    const u16* __restrict__ w_out, u16* __restrict__ out,
                                                    float* stats, u16* z2N, u16* hF, u64* sync) {
  __shared__ __attribute__((aligned(16))) char smem[XW_LDS + 4096 + 1024];
  const int bid = blockIdx.x;
  int rb, cb;
  tile_of_block(bid, rb, cb);
  const int tid = threadIdx.x, l = tid & 63, w = __builtin_amdgcn_readfirstlane(tid >> 6);
  const int t0 = rb * 64;
  const int koff = (4 * (rb & 3)) & 15;

  if (w >= 8) {
    const int lw = w - 8;
    const XwLoader ld(x, w_in, rb, cb * 112, lw, l);
    const rsrc_t rsA = make_rsrc(w_sp, S_ * S_ * 2);
    const rsrc_t rsO = make_rsrc(w_out, cb < 7 ? FH * D_ * 2 : 0);
    unsigned ap[4];
#pragma unroll
    for (int i = 0; i < 4; ++i) {
      const int row = 4 * (4 * i + lw) + (l >> 4);
      ap[i] = (unsigned)((t0 + row) * S_ + (((l & 15) ^ (row & 15)) * 8)) * 2u;
    }
    const unsigned oo = (unsigned)((8 * lw + (l >> 3)) * D_ + cb * 64 + (((l & 7) ^ (((l >> 4) & 3) << 1)) * 8)) * 2u;
#define LOAD_W(st)                                                                                \
  {                                                                                               \
    const int sk_ = ((st) + koff) & 15;                                                           \
    char* dst_ = smem + ring_slot(st) + lw * 1024;                                                \
    _Pragma("unroll") for (int i = 0; i < 4; ++i) blds16(rsA, dst_ + i * 4096, ap[i], 256 * sk_); \
  }
#define LOAD_O(c)        \
  {                                                                                                           \
    char* dst_ = smem + ring_slot((c) + 7) + lw * 1024;                                                       \
    _Pragma("unroll") for (int i = 0; i < 4; ++i) blds16(rsO, dst_ + i * 4096, oo, (128 * (c) + 32 * i) * D_ * 2); \
  }
    ld.stage_x(smem, 0); ld.stage_w(smem, 0, FH);
    ld.stage_x(smem, 1); ld.stage_w(smem, 1, FH);
    WAITV(11); BAR();
    ld.stage_x(smem, 2); ld.stage_w(smem, 2, FH);
    ld.stage_x(smem, 3); ld.stage_w(smem, 3, FH);
    {
      const int c = bid * 196 + min(lw * 64 + l, 195);
      blds16(ld.rw, smem + XW_LDS + 4096, (unsigned)((c / 112) * F_ + (c % 112) * 8) * 2u, 0);
      blds16(make_rsrc(w_out, FH * D_ * 2), smem + XW_LDS + 4096, (unsigned)c * 16u, 0);
    }
    WAITV(18); BAR();
    ld.stage_w(smem, 0, 0);
    WAITV(14); BAR();
    ld.stage_w(smem, 1, 0);
    WAITV(16); BAR();
    ld.stage_w(smem, 2, 0);
    WAITV(14); BAR();
    ld.stage_w(smem, 3, 0);
    WAITV(10); BAR();
    LOAD_W(0) LOAD_W(1)
#pragma unroll
    for (int i = 0; i < 8; ++i) blds16(rsA, smem + XW_LDS + 4096, (unsigned)(lw * 64 + l) * 16u, bid * 32768 + i * 4096);
    WAITV(19); BAR();
    LOAD_W(2) LOAD_W(3) LOAD_W(4)
    WAITV(28); BAR();
    LOAD_W(5) LOAD_W(6)
    BAR();
    LOAD_W(7) LOAD_W(8)
    WAITV(32); BAR();
    WAITV(28); BAR();
    LOAD_W(9) WAITV(28); BAR();
    WAITV(24); BAR();
    LOAD_W(10) WAITV(24); BAR();
    WAITV(20); BAR();
    LOAD_W(11) WAITV(20); BAR();
    WAITV(16); BAR();
    LOAD_W(12) WAITV(16); BAR();
    WAITV(12); BAR();
    LOAD_W(13) WAITV(12); BAR();
    LOAD_W(14) WAITV(12); BAR();
    LOAD_W(15) WAITV(12); BAR();
    LOAD_O(0) WAITV(12); BAR();
    LOAD_O(1) WAITV(12); BAR();
    LOAD_O(2) WAITV(12); BAR();
    LOAD_O(3) BAR();
    LOAD_O(4) BAR();
    LOAD_O(5) LOAD_O(6)
    WAITV(0); BAR();
    return;
  }

  const int fg = w & 3, kg = w >> 2, g = l >> 4, c16 = l & 15;
  const bool two = fg < 3;
  STAMPR(0, 0); STAMP(0, 1);
  const u64 nonce = __hip_atomic_load((const gu64*)sync, RLX_AGENT);
  const u64 tag = (((u64)(unsigned)__builtin_amdgcn_readfirstlane((unsigned)(nonce >> 32)) << 32) |
                   (unsigned)__builtin_amdgcn_readfirstlane((unsigned)nonce)) + 1;
  u64* sflag = sync + 32;
  u64* zflag = sflag + 256 * 32;
  u64* hflag = zflag + 256 * 32;
  const rsrc_t rsN = make_rsrc(z2N, S_ * FH * 2);
  const rsrc_t rsN1 = make_rsrc(z2N, two ? S_ * FH * 2 : 0);
  f32x2* sst = (f32x2*)(smem + XW_LDS);

  u32x4 zp[2];
  {
    const XwAddr<true> ad(l, fg, kg);
    f32x4 acc[2][2];
#pragma unroll
    for (int rt = 0; rt < 2; ++rt)
#pragma unroll
      for (int jj = 0; jj < 2; ++jj) acc[rt][jj] = f32x4{0.f, 0.f, 0.f, 0.f};
    BAR(); STAMP(0, 2); xw_compute<true, 0, 4>(ad, smem, acc);
    BAR(); xw_compute<true, 4, 4>(ad, smem, acc);
    BAR(); xw_compute<true, 8, 4>(ad, smem, acc);
    BAR(); xw_compute<true, 12, 2>(ad, smem, acc);
#pragma unroll
    for (int jj = 0; jj < 2; ++jj)
      zp[jj] = u32x4{gelu_pair(acc[0][jj][0], acc[0][jj][1]), gelu_pair(acc[0][jj][2], acc[0][jj][3]),
                     gelu_pair(acc[1][jj][0], acc[1][jj][1]), gelu_pair(acc[1][jj][2], acc[1][jj][3])};
    if (!two) zp[1] = u32x4{0u, 0u, 0u, 0u};
    float v[16];
#pragma unroll
    for (int e = 0; e < 4; ++e) {
      const f32x2 a0 = {bf_lo(zp[0][e]), bf_hi(zp[0][e])}, a1 = {bf_lo(zp[1][e]), bf_hi(zp[1][e])};
      const f32x2 s1 = a0 + a1, s2 = __builtin_elementwise_fma(a0, a0, a1 * a1);
      v[4 * e] = s1[0]; v[4 * e + 1] = s2[0]; v[4 * e + 2] = s1[1]; v[4 * e + 3] = s2[1];
    }
#pragma unroll
    for (int i = 0; i < 8; ++i) { const bool b = c16 & 1; v[i] = (b ? v[i + 8] : v[i]) + dpp_mov<0xB1>(b ? v[i] : v[i + 8]); }
#pragma unroll
    for (int i = 0; i < 4; ++i) { const bool b = c16 & 2; v[i] = (b ? v[i + 4] : v[i]) + dpp_mov<0x4E>(b ? v[i] : v[i + 4]); }
#pragma unroll
    for (int i = 0; i < 2; ++i) { const bool b = c16 & 4; v[i] = (b ? v[i + 2] : v[i]) + dpp_mov<0x124>(b ? v[i] : v[i + 2]); }
    { const bool b = c16 & 8; v[0] = (b ? v[1] : v[0]) + dpp_mov<0x128>(b ? v[0] : v[1]); }
    const int idx = ((c16 & 1) << 3) | ((c16 & 2) << 1) | ((c16 & 4) >> 1) | (c16 >> 3);
    ((float*)(sst + fg * 64 + 32 * kg + 8 * g))[idx] = v[0];
  }
  STAMP(0, 3);

  u32x2 z1p[2][2];
  {
    const XwAddr<false> ad(l, fg, kg);
    f32x4 za[2][2];
#pragma unroll
    for (int i = 0; i < 2; ++i)
#pragma unroll
      for (int j = 0; j < 2; ++j) za[i][j] = f32x4{0.f, 0.f, 0.f, 0.f};
    BAR();
    STAMP(0, 13);
    const unsigned tagA = (unsigned)tag, tagB = (unsigned)(tag >> 32) ^ (tagA * 0x9E3779B1u);
    if (w == 7) {
      const f32x2 t = sst[l] + sst[64 + l] + sst[128 + l] + sst[192 + l];
      const float m2 = fmaxf(t[1] - t[0] * t[0] * (1.0f / 112.0f), 0.f);
      gu64* gp = (gu64*)stats + (size_t)(rb * 8 + cb) * 128 + l;
      __hip_atomic_store(gp, ((u64)tagA << 32) | __builtin_bit_cast(unsigned, t[0]), RLX_AGENT);
      __hip_atomic_store(gp + 64, ((u64)tagB << 32) | __builtin_bit_cast(unsigned, m2), RLX_AGENT);
    }
    xw_compute<false, 0, 4>(ad, smem, za);
    BAR(); STAMP(0, 14);
    f32x2* mrs = (f32x2*)(smem + XW_LDS + 2048);
    const gu64* gq = (const gu64*)stats + (size_t)rb * 1024 + l;
    u64 gv[16];
    if (w == 0) {
#pragma unroll
      for (int i = 0; i < 16; ++i) gv[i] = __hip_atomic_load(gq + 64 * i, RLX_AGENT);
    }
    xw_compute<false, 4, 4>(ad, smem, za);
    if (w == 0) {
      for (unsigned spins = 0; spins < (1u << 19); ++spins) {
        bool ok = true;
#pragma unroll
        for (int i = 0; i < 16; ++i) ok &= (unsigned)(gv[i] >> 32) == ((i & 1) ? tagB : tagA);
        if (__all(ok)) break;
#pragma unroll
        for (int i = 0; i < 16; ++i) gv[i] = __hip_atomic_load(gq + 64 * i, RLX_AGENT);
      }
      float sum = 0.f, m2 = 0.f;
#pragma unroll
      for (int i = 0; i < 8; ++i) {
        sum += __builtin_bit_cast(float, (unsigned)gv[2 * i]);
        m2 += __builtin_bit_cast(float, (unsigned)gv[2 * i + 1]);
      }
      const float mean = sum * (1.0f / 896.0f);
#pragma unroll
      for (int i = 0; i < 8; ++i) {
        const float d = __builtin_bit_cast(float, (unsigned)gv[2 * i]) * (1.0f / 112.0f) - mean;
        m2 += 112.0f * d * d;
      }
      const float var = bfr(m2 * (1.0f / 896.0f));
      const float den = bfr(sqrtf(bfr(var + 1e-5f)));
      mrs[l] = f32x2{bfr(mean), 1.0f / den};
    }
    BAR();
    STAMP(0, 4);
    const unsigned zoff = (unsigned)(((cb * 7 + 2 * fg) * 64 + 2 * rb + kg) * 64 + l) * 16u;
#pragma unroll
    for (int jj = 0; jj < 2; ++jj) {
      u32x4 v;
#pragma unroll
      for (int e = 0; e < 4; ++e) {
        const f32x4 mr = *(const f32x4*)(mrs + 32 * kg + 8 * g + 2 * e);
        v[e] = pack2(bfr(bf_lo(zp[jj][e]) - mr[0]) * mr[1], bfr(bf_hi(zp[jj][e]) - mr[2]) * mr[3]);
      }
      __builtin_amdgcn_raw_buffer_store_b128(v, jj ? rsN1 : rsN, zoff + 65536u * jj, 0, 16);
    }
    xw_compute<false, 8, 4>(ad, smem, za);
    BAR();
    xw_compute<false, 12, 2>(ad, smem, za);
#pragma unroll
    for (int i = 0; i < 2; ++i)
#pragma unroll
      for (int j = 0; j < 2; ++j) {
        z1p[i][j] = u32x2{gelu_pair(za[i][j][0], za[i][j][1]), gelu_pair(za[i][j][2], za[i][j][3])};
        asm volatile("" : "+v"(z1p[i][j]));
      }
    WAITV(0);
    BAR();
    if (w == 0) {
      if (l == 0) __hip_atomic_store((gu64*)zflag + (cb * 32 + rb) * 32, tag, RLX_AGENT);
      wait_flags(zflag + cb * 32 * 32, 32, l, tag);
    }
  }
  STAMP(0, 5);

  const unsigned boff = (unsigned)(((cb * 7 + 2 * fg) * 64 + kg) * 64 + l) * 16u;
  u32x4 bst[4][4];
#define LOAD_B(slot, st)                                                                          \
  {                                                                                               \
    const int sk_ = ((st) + koff) & 15;                                                           \
    bst[slot][0] = __builtin_amdgcn_raw_buffer_load_b128(rsN, boff, 4096 * sk_, 16);              \
    bst[slot][1] = __builtin_amdgcn_raw_buffer_load_b128(rsN1, boff + 65536, 4096 * sk_, 16);     \
    bst[slot][2] = __builtin_amdgcn_raw_buffer_load_b128(rsN, boff + 2048, 4096 * sk_, 16);       \
    bst[slot][3] = __builtin_amdgcn_raw_buffer_load_b128(rsN1, boff + 65536 + 2048, 4096 * sk_, 16); \
  }
  unsigned ard[2][2][4];
#pragma unroll
  for (int h = 0; h < 2; ++h)
#pragma unroll
    for (int i = 0; i < 4; ++i) {
      ard[0][h][i] = (unsigned)(size_t)smem + (((16 * ((i + 2 * kg) & 3) + c16) * 256 + (((4 * kg + g) ^ c16) * 16)) ^ (128 * h));
      ard[1][h][i] = ard[0][h][i] + 65536u;
      asm volatile("" : "+v"(ard[1][h][i]));
    }
  f32x4 acc[4][2];
#pragma unroll
  for (int i = 0; i < 4; ++i)
#pragma unroll
    for (int j = 0; j < 2; ++j) acc[i][j] = f32x4{0.f, 0.f, 0.f, 0.f};
  bf16x8 af[2][4];
#define READ_A(st, h)                                                                                    \
  {                                                                                                      \
    constexpr int so_ = ring_slot(st);                                                                   \
    _Pragma("unroll") for (int i = 0; i < 4; ++i) {                                                      \
      unsigned a_ = ard[so_ >= 65536][h][i];                                                             \
      if (so_ >= 131072) asm volatile("v_add_u32 %0, 0x10000, %0" : "+v"(a_));                           \
      af[h][i] = *(const LAS bf16x8*)(size_t)(a_ + (so_ & 65535));                                       \
    }                                                                                                    \
    __builtin_amdgcn_sched_barrier(0);                                                                   \
  }
#define MMA(u, h)                                                                                        \
  {                                                                                                      \
    _Pragma("unroll") for (int j = 0; j < 2; ++j) {                                                      \
      const bf16x8 zf = __builtin_bit_cast(bf16x8, bst[u][2 * (h) + j]);                                 \
      _Pragma("unroll") for (int i = 0; i < 4; ++i)                                                      \
        acc[i][j] = __builtin_amdgcn_mfma_f32_16x16x32_bf16(zf, af[h][i], acc[i][j], 0, 0, 0);           \
    }                                                                                                    \
    __builtin_amdgcn_sched_barrier(0);                                                                   \
  }
#define ITER(it)                                                \
  {                                                             \
    BAR();                                                      \
    READ_A(it, 0)                                               \
    MMA(((it) + 3) & 3, 1)                                      \
    READ_A(it, 1)                                               \
    if ((it) + 3 < 16) { LOAD_B(((it) + 3) & 3, (it) + 3) }     \
    MMA((it) & 3, 0)                                            \
  }
  BAR();
  STAMP(0, 6);
  LOAD_B(0, 0) LOAD_B(1, 1) LOAD_B(2, 2) LOAD_B(3, 3)
  READ_A(0, 0)
  READ_A(0, 1)
  MMA(0, 0)
  STAMP(0, 7);
  ITER(1) ITER(2) ITER(3) ITER(4) ITER(5) ITER(6) ITER(7) ITER(8)
  STAMP(0, 8);
  ITER(9) ITER(10) ITER(11) ITER(12) ITER(13)
  int lz = __builtin_amdgcn_mbcnt_hi(~0u, __builtin_amdgcn_mbcnt_lo(~0u, 0u));
  asm volatile("" : "+v"(lz));
  float bias[2];
#pragma unroll
  for (int i = 0; i < 2; ++i) bias[i] = bf_lo(b_sp[(rb * 4 + 2 * kg + i) * 16 + (lz & 15)]);
  ITER(14) ITER(15)
  MMA(3, 1)
  STAMP(0, 9);
  BAR();
  char* redw = smem + (kg ? ring_slot(14) : ring_slot(15)) + fg * 4096 + lz * 16;
  const char* redr = smem + (kg ? ring_slot(15) : ring_slot(14)) + fg * 4096 + lz * 16;
#pragma unroll
  for (int i = 0; i < 2; ++i)
#pragma unroll
    for (int j = 0; j < 2; ++j) *(f32x4*)(redw + (2 * i + j) * 1024) = acc[2 + i][j];
  BAR();
  const rsrc_t rsH = make_rsrc(hF, S_ * FH * 2);
#pragma unroll
  for (int i = 0; i < 2; ++i) {
    u32x2 hv[2];
#pragma unroll
    for (int j = 0; j < 2; ++j) {
      const f32x4 a = acc[i][j] + *(const f32x4*)(redr + (2 * i + j) * 1024);
      const unsigned g01 = pack2(a[0], a[1]), g23 = pack2(a[2], a[3]);
      const unsigned s01 = pack2(bf_lo(g01) + bias[i], bf_hi(g01) + bias[i]);
      const unsigned s23 = pack2(bf_lo(g23) + bias[i], bf_hi(g23) + bias[i]);
      hv[j] = u32x2{pack2(bf_lo(z1p[i][j][0]) * bf_lo(s01), bf_hi(z1p[i][j][0]) * bf_hi(s01)),
                    pack2(bf_lo(z1p[i][j][1]) * bf_lo(s23), bf_hi(z1p[i][j][1]) * bf_hi(s23))};
    }
    const unsigned ho = (unsigned)(((rb * 4 + 2 * kg + i) * 28 + 7 * (cb >> 1)) * 64 + lz) * 16u;
    if (two) __builtin_amdgcn_raw_buffer_store_b128(u32x4{hv[0][0], hv[0][1], hv[1][0], hv[1][1]}, rsH, ho + (3 * (cb & 1) + fg) * 1024u, 0, 16);
    else     __builtin_amdgcn_raw_buffer_store_b64(hv[0], rsH, ho + 6 * 1024u + 8u * (cb & 1), 0, 16);
  }
  {
    const int tq = w & 3, gz = lz >> 4, cz = lz & 15;
    const int rrow = 4 * gz + (cz >> 2);
    int rb4[4];
#pragma unroll
    for (int j = 0; j < 4; ++j) rb4[j] = rrow * 128 + ((j ^ ((rrow >> 1) & 3)) * 32) + 8 * (lz & 3);
#define OUT_TILE(t) (ring_slot(((t) >> 3) + 7) + ((t) & 7) * 2048)
#define OUT_FRAG(dst, ks)                                                                                 \
  {                                                                                                       \
    constexpr int pc_ = (ks) / 7, e_ = (ks) % 7;                                                          \
    constexpr int tA_ = (e_ < 6) ? (14 * pc_ + 7 * (e_ / 3) + 2 * (e_ % 3)) : (14 * pc_ + 6);             \
    constexpr int tB_ = (e_ < 6) ? (tA_ + 1) : (14 * pc_ + 13);                                           \
    _Pragma("unroll") for (int j = 0; j < 4; ++j)                                                         \
      dst[j] = tr_frag(smem + rb4[j] + OUT_TILE(tA_), smem + rb4[j] + OUT_TILE(tB_));                     \
  }
    BAR();
    bf16x8 wpre[5][4];
    if (kg == 0) { OUT_FRAG(wpre[0], 0) OUT_FRAG(wpre[1], 2) OUT_FRAG(wpre[2], 4) OUT_FRAG(wpre[3], 6) OUT_FRAG(wpre[4], 8) }
    else         { OUT_FRAG(wpre[0], 1) OUT_FRAG(wpre[1], 3) OUT_FRAG(wpre[2], 5) OUT_FRAG(wpre[3], 7) OUT_FRAG(wpre[4], 9) }
    WAITV(0);
    BAR();
    STAMP(0, 10);
    if (w == 0) {
      if (lz == 0) __hip_atomic_store((gu64*)hflag + (rb * 8 + cb) * 32, tag, RLX_AGENT);
      if (cb < 7) wait_flags(hflag + rb * 8 * 32, 8, lz, tag);
    }
    if (cb == 7) return;
    BAR();
    STAMP(0, 11);
    const unsigned ho = (unsigned)(((rb * 4 + tq) * 28 + kg) * 64 + lz) * 16u;
    u32x4 hf[14];
#pragma unroll
    for (int i = 0; i < 14; ++i) hf[i] = __builtin_amdgcn_raw_buffer_load_b128(rsH, ho, 2048 * i, 16);
    f32x4 oc[4];
#pragma unroll
    for (int j = 0; j < 4; ++j) oc[j] = f32x4{0.f, 0.f, 0.f, 0.f};
#pragma unroll
    for (int i = 0; i < 5; ++i)
#pragma unroll
      for (int j = 0; j < 4; ++j)
        oc[j] = __builtin_amdgcn_mfma_f32_16x16x32_bf16(wpre[i][j], __builtin_bit_cast(bf16x8, hf[i]), oc[j], 0, 0, 0);
#define OUT_STEP(i, ks)                                                                                   \
  {                                                                                                       \
    bf16x8 wf[4];                                                                                         \
    OUT_FRAG(wf, ks)                                                                                      \
    _Pragma("unroll") for (int j = 0; j < 4; ++j)                                                         \
      oc[j] = __builtin_amdgcn_mfma_f32_16x16x32_bf16(wf[j], __builtin_bit_cast(bf16x8, hf[i]), oc[j], 0, 0, 0); \
  }
#define OUT_ALL(par)                                                                                       \
  OUT_STEP(5, 10 + par) OUT_STEP(6, 12 + par) OUT_STEP(7, 14 + par) OUT_STEP(8, 16 + par) OUT_STEP(9, 18 + par) \
  OUT_STEP(10, 20 + par) OUT_STEP(11, 22 + par) OUT_STEP(12, 24 + par) OUT_STEP(13, 26 + par)
    if (kg == 0) { OUT_ALL(0) } else { OUT_ALL(1) }
    char* red = smem + ring_slot(14);
    *(f32x4*)(red + (w ^ 4) * 2048 + lz * 16) = kg ? oc[0] : oc[2];
    *(f32x4*)(red + (w ^ 4) * 2048 + 1024 + lz * 16) = kg ? oc[1] : oc[3];
    BAR();
    const f32x4 a0 = (kg ? oc[2] : oc[0]) + *(const f32x4*)(red + w * 2048 + lz * 16);
    const f32x4 a1 = (kg ? oc[3] : oc[1]) + *(const f32x4*)(red + w * 2048 + 1024 + lz * 16);
    u16* op = out + (size_t)((rb * 4 + tq) * 16 + cz) * D_ + cb * 64 + 32 * kg + 4 * gz;
    *(u32x2*)op = u32x2{pack2(a0[0], a0[1]), pack2(a0[2], a0[3])};
    *(u32x2*)(op + 16) = u32x2{pack2(a1[0], a1[1]), pack2(a1[2], a1[3])};
  }
  if (bid == 0 && w == 0 && lz == 0) __hip_atomic_store((gu64*)sync, tag, RLX_AGENT);
  STAMP(0, 12); STAMPR(0, 15);
}

extern "C" void kernel_launch(void* const* d_in, const int* in_sizes, int n_in, void* d_out, int out_size,
                              void* d_ws, size_t ws_size, hipStream_t stream) {
  const u16* x = (const u16*)d_in[0];
  const u16* w_in = (const u16*)d_in[1];
  const u16* w_sp = (const u16*)d_in[2];
  const u16* b_sp = (const u16*)d_in[3];
  const u16* w_out = (const u16*)d_in[4];
  char* ws = (char*)d_ws;
  u16* z2N = (u16*)ws;
  u16* hF = (u16*)(ws + (size_t)4 * 1024 * 1024);
  float* stats = (float*)(ws + (size_t)8 * 1024 * 1024);
  u64* sync = (u64*)(ws + (size_t)9 * 1024 * 1024);
  gmlp_fused<<<256, 768, 0, stream>>>(x, w_in, w_sp, b_sp, w_out, (u16*)d_out, stats, z2N, hF, sync);
}
```

```cpp
#include <hip/hip_runtime.h>
#include <hip/hip_bf16.h>

typedef __bf16 bf16x8 __attribute__((ext_vector_type(8)));
typedef __bf16 bf16x4 __attribute__((ext_vector_type(4)));
typedef __bf16 bf16x2 __attribute__((ext_vector_type(2)));
typedef float f32x4 __attribute__((ext_vector_type(4)));
typedef float f32x2 __attribute__((ext_vector_type(2)));
typedef unsigned u32x4 __attribute__((ext_vector_type(4)));
typedef unsigned u32x2 __attribute__((ext_vector_type(2)));
typedef unsigned short u16;
#define LAS __attribute__((address_space(3)))
#define DI __device__ __forceinline__

constexpr int S_ = 2048, D_ = 448, F_ = 1792, FH = 896;

DI float bf_lo(unsigned u) { return __builtin_bit_cast(float, u << 16); }
DI float bf_hi(unsigned u) { return __builtin_bit_cast(float, u & 0xffff0000u); }
DI unsigned pack2(float a, float b) {
  f32x2 f = {a, b};
  return __builtin_bit_cast(unsigned, __builtin_convertvector(f, bf16x2));
}
DI float bfr(float a) { return (float)(__bf16)a; }
DI bf16x8 tr_frag(const char* a0, const char* a1) {
  bf16x4 lo = __builtin_amdgcn_ds_read_tr16_b64_v4bf16((LAS bf16x4*)a0);
  bf16x4 hi = __builtin_amdgcn_ds_read_tr16_b64_v4bf16((LAS bf16x4*)a1);
  return __builtin_shufflevector(lo, hi, 0, 1, 2, 3, 4, 5, 6, 7);
}
template <int CTRL> DI float dpp_mov(float x) {
  return __builtin_bit_cast(float, __builtin_amdgcn_update_dpp(0, __builtin_bit_cast(int, x), CTRL, 0xf, 0xf, true));
}

#define BAR()                                              \
  do {                                                     \
    asm volatile("s_waitcnt lgkmcnt(0)" ::: "memory");     \
    __builtin_amdgcn_s_barrier();                          \
    asm volatile("" ::: "memory");                         \
  } while (0)
#define WAITV(n) asm volatile("s_waitcnt vmcnt(" #n ")" ::: "memory")
#define ORDER() asm volatile("" ::: "memory")
typedef __amdgpu_buffer_rsrc_t rsrc_t;
DI rsrc_t make_rsrc(const void* p, unsigned bytes) { return __builtin_amdgcn_make_buffer_rsrc((void*)p, 0, bytes, 0x00020000); }
DI void blds16(rsrc_t rs, char* lds_wave_base, unsigned voff, unsigned soff) {
  __builtin_amdgcn_raw_ptr_buffer_load_lds(rs, (LAS void*)lds_wave_base, 16, voff, soff, 0, 0);
}

constexpr int XW_XB = 7 * 8192, XW_WB = 448 * 224, XW_LDS = XW_XB + XW_WB;

DI int x_swz(int row) { return ((row >> 1) & 1) | (((row >> 3) & 1) << 1) | ((((row >> 2) ^ (row >> 4)) & 1) << 2); }

struct XwLoader {
  rsrc_t rx, rw;
  unsigned xo[4], wo[7];
  int lw;
  DI XwLoader(const u16* x, const u16* w_in, int rb, int n0, int lw_, int l) : lw(lw_) {
    rx = make_rsrc(x, S_ * D_ * 2);
    rw = make_rsrc(w_in, D_ * F_ * 2);
#pragma unroll
    for (int i = 0; i < 4; ++i) {
      const int pc = 4 * i + lw, row = 8 * (pc & 7) + (l >> 3);
      xo[i] = (unsigned)((rb * 64 + row) * D_ + (pc >> 3) * 64 + (((l & 7) ^ x_swz(row)) * 8)) * 2u;
    }
#pragma unroll
    for (int i = 0; i < 7; ++i) {
      const int c = (4 * i + lw) * 64 + l, row = c / 14, cc = c - row * 14;
      wo[i] = (unsigned)(row * F_ + n0 + cc * 8) * 2u;
    }
  }
  DI void stage_x(char* smem, int s) const {
#pragma unroll
    for (int i = 0; i < (s < 3 ? 4 : 2); ++i) blds16(rx, smem + 2 * s * 8192 + (4 * i + lw) * 1024, xo[i], 256 * s);
    ORDER();
  }
  DI void stage_w(char* smem, int s, int col0) const {
#pragma unroll
    for (int i = 0; i < (s < 3 ? 7 : 4); ++i)
      if (s < 3 || 4 * i + lw < 14) blds16(rw, smem + XW_XB + (28 * s + 4 * i + lw) * 1024, wo[i], 128 * F_ * 2 * s + col0 * 2);
    ORDER();
  }
};
template <bool Z2> struct XwAddr {
  int xa[2], wa[2];
  DI XwAddr(int l, int fg, int mh) {
    const int g = l >> 4, c16 = l & 15, q = c16 >> 2, p = l & 3;
#pragma unroll
    for (int rt = 0; rt < 2; ++rt) {
      const int row = 32 * mh + (Z2 ? (8 * q + 4 * rt + p) : (16 * rt + c16));
      xa[rt] = row * 128 + (((g >> 1) ^ x_swz(row)) * 16) + (g & 1) * 8;
    }
#pragma unroll
    for (int jj = 0; jj < 2; ++jj) wa[jj] = XW_XB + (4 * g + q) * 224 + min(2 * fg + jj, 6) * 32 + 8 * p;
  }
};
template <int OFF> DI u32x2 lds_read64(unsigned addr) {
  u32x2 r;
  asm volatile("ds_read_b64 %0, %1 offset:%2" : "=v"(r) : "v"(addr), "i"(OFF) : "memory");
  return r;
}
template <bool Z2, int KS0, int NKS>
DI void xw_compute(const XwAddr<Z2>& ad, const char* smem, f32x4 (&acc)[2][2]) {
  u32x2 xl[NKS][2], xh[NKS][2];
  bf16x8 wf[NKS][2];
  const unsigned sb = (unsigned)(size_t)smem;
#pragma unroll
  for (int k = 0; k < NKS; ++k) {
#pragma unroll
    for (int rt = 0; rt < 2; ++rt) {
      const unsigned o = sb + (ad.xa[rt] ^ (64 * ((KS0 + k) & 1)));
      if (k < 2) { xl[k][rt] = lds_read64<8192 * (KS0 >> 1)>(o); xh[k][rt] = lds_read64<8192 * (KS0 >> 1)>(o ^ 32); }
      else       { xl[k][rt] = lds_read64<8192 * (KS0 >> 1) + 8192>(o); xh[k][rt] = lds_read64<8192 * (KS0 >> 1) + 8192>(o ^ 32); }
    }
#pragma unroll
    for (int jj = 0; jj < 2; ++jj) {
      const char* a = smem + ad.wa[jj] + 32 * (KS0 + k) * 224;
      wf[k][jj] = tr_frag(a, a + 16 * 224);
    }
  }
  asm volatile("s_waitcnt lgkmcnt(0)" ::: "memory");
  __builtin_amdgcn_sched_barrier(0);
#pragma unroll
  for (int k = 0; k < NKS; ++k)
#pragma unroll
    for (int rt = 0; rt < 2; ++rt) {
      const bf16x8 xf = __builtin_bit_cast(bf16x8, u32x4{xl[k][rt][0], xl[k][rt][1], xh[k][rt][0], xh[k][rt][1]});
#pragma unroll
      for (int jj = 0; jj < 2; ++jj) {
        if (Z2) acc[rt][jj] = __builtin_amdgcn_mfma_f32_16x16x32_bf16(xf, wf[k][jj], acc[rt][jj], 0, 0, 0);
        else    acc[rt][jj] = __builtin_amdgcn_mfma_f32_16x16x32_bf16(wf[k][jj], xf, acc[rt][jj], 0, 0, 0);
      }
    }
}
DI unsigned gelu_pair(float a, float b) {
  const unsigned u = pack2(a, b);
  const f32x2 z = {bf_lo(u), bf_hi(u)};
  const f32x2 y = z * __builtin_elementwise_fma(z * z, f32x2{-0.10294324f, -0.10294324f}, f32x2{-2.3022082f, -2.3022082f});
  const f32x2 d = f32x2{__builtin_amdgcn_exp2f(y[0]), __builtin_amdgcn_exp2f(y[1])} + f32x2{1.0f, 1.0f};
  const f32x2 o = z * f32x2{__builtin_amdgcn_rcpf(d[0]), __builtin_amdgcn_rcpf(d[1])};
  return pack2(o[0], o[1]);
}

DI void tile_of_block(int bid, int& rb, int& cb) {
  const int xcd = bid & 7, idx = bid >> 3;
  rb = (xcd >> 1) * 8 + (idx & 7);
  cb = (xcd & 1) * 4 + (idx >> 3);
}

typedef unsigned long long u64;
typedef __attribute__((address_space(1))) u64 gu64;
#define RLX_AGENT __ATOMIC_RELAXED, __HIP_MEMORY_SCOPE_AGENT
DI constexpr int ring_slot(int st) {
  return 16384 * ((st % 9) == 0 ? 0 : (st % 9) == 1 ? 4 : (st % 9) == 2 ? 1 : (st % 9) == 3 ? 5 : (st % 9) == 4 ? 6 :
                  (st % 9) == 5 ? 2 : (st % 9) == 6 ? 7 : (st % 9) == 7 ? 3 : 8);
}
DI void wait_flags(const u64* f, int n, int l, u64 tag) {
  const gu64* fp = (const gu64*)f + (l < n ? l : 0) * 32;
  for (unsigned spins = 0; spins < (1u << 20); ++spins) {
    if (__all(__hip_atomic_load(fp, RLX_AGENT) == tag)) break;
    __builtin_amdgcn_s_sleep(4);
  }
}

__global__ __launch_bounds__(768, 3) void gmlp_fused(const u16* __restrict__ x, const u16* __restrict__ w_in,
                                                    const u16* __restrict__ w_sp, const u16* __restrict__ b_sp,
                                                    const u16* __restrict__ w_out, u16* __restrict__ out,
                                                    float* stats, u16* z2N, u16* hF, u64* sync) {
  __shared__ __attribute__((aligned(16))) char smem[XW_LDS + 4096 + 1024];
  const int bid = blockIdx.x;
  int rb, cb;
  tile_of_block(bid, rb, cb);
  const int tid = threadIdx.x, l = tid & 63, w = __builtin_amdgcn_readfirstlane(tid >> 6);
  const int t0 = rb * 64;
  const int koff = (4 * (rb & 3)) & 15;

  if (w >= 8) {
    const int lw = w - 8;
    const XwLoader ld(x, w_in, rb, cb * 112, lw, l);
    const rsrc_t rsA = make_rsrc(w_sp, S_ * S_ * 2);
    const rsrc_t rsO = make_rsrc(w_out, cb < 7 ? FH * D_ * 2 : 0);
    unsigned ap[4];
#pragma unroll
    for (int i = 0; i < 4; ++i) {
      const int row = 4 * (4 * i + lw) + (l >> 4);
      ap[i] = (unsigned)((t0 + row) * S_ + (((l & 15) ^ (row & 15)) * 8)) * 2u;
    }
    const unsigned oo = (unsigned)((8 * lw + (l >> 3)) * D_ + cb * 64 + (((l & 7) ^ (((l >> 4) & 3) << 1)) * 8)) * 2u;
#define LOAD_W(st)                                                                                \
  {                                                                                               \
    const int sk_ = ((st) + koff) & 15;                                                           \
    char* dst_ = smem + ring_slot(st) + lw * 1024;                                                \
    _Pragma("unroll") for (int i = 0; i < 4; ++i) blds16(rsA, dst_ + i * 4096, ap[i], 256 * sk_); \
    ORDER();                                                                                      \
  }
#define LOAD_O(c)        \
  {                                                                                                           \
    char* dst_ = smem + ring_slot((c) + 7) + lw * 1024;                                                       \
    _Pragma("unroll") for (int i = 0; i < 4; ++i) blds16(rsO, dst_ + i * 4096, oo, (128 * (c) + 32 * i) * D_ * 2); \
    ORDER();                                                                                                  \
  }
    ld.stage_x(smem, 0); ld.stage_w(smem, 0, FH);
    ld.stage_x(smem, 1); ld.stage_w(smem, 1, FH);
    WAITV(11); BAR();
    ld.stage_x(smem, 2); ld.stage_w(smem, 2, FH);
    ld.stage_x(smem, 3); ld.stage_w(smem, 3, FH);
    {
      const int c = bid * 196 + min(lw * 64 + l, 195);
      blds16(ld.rw, smem + XW_LDS + 4096, (unsigned)((c / 112) * F_ + (c % 112) * 8) * 2u, 0);
      blds16(make_rsrc(w_out, FH * D_ * 2), smem + XW_LDS + 4096, (unsigned)c * 16u, 0);
    }
#define PF_W(i) blds16(rsA, smem + XW_LDS + 4096, (unsigned)(lw * 64 + l) * 16u, bid * 32768 + (i) * 4096);
    WAITV(18); BAR();
    ld.stage_w(smem, 0, 0); PF_W(0) PF_W(1)
    WAITV(16); BAR();
    ld.stage_w(smem, 1, 0); PF_W(2) PF_W(3)
    WAITV(20); BAR();
    ld.stage_w(smem, 2, 0); PF_W(4) PF_W(5)
    WAITV(20); BAR();
    ld.stage_w(smem, 3, 0); PF_W(6) PF_W(7)
    WAITV(16); BAR();
    LOAD_W(0) LOAD_W(1)
    WAITV(15); BAR();
    LOAD_W(2) LOAD_W(3) LOAD_W(4)
    WAITV(22); BAR();
    LOAD_W(5) LOAD_W(6)
    BAR();
    LOAD_W(7) LOAD_W(8)
    WAITV(32); BAR();
    WAITV(28); BAR();
    LOAD_W(9) WAITV(28); BAR();
    WAITV(24); BAR();
    LOAD_W(10) WAITV(24); BAR();
    WAITV(20); BAR();
    LOAD_W(11) WAITV(20); BAR();
    WAITV(16); BAR();
    LOAD_W(12) WAITV(16); BAR();
    WAITV(12); BAR();
    LOAD_W(13) WAITV(12); BAR();
    LOAD_W(14) WAITV(12); BAR();
    LOAD_W(15) WAITV(12); BAR();
    LOAD_O(0) WAITV(12); BAR();
    LOAD_O(1) WAITV(12); BAR();
    LOAD_O(2) WAITV(12); BAR();
    LOAD_O(3) BAR();
    LOAD_O(4) BAR();
    LOAD_O(5) LOAD_O(6)
    WAITV(0); BAR();
    return;
  }

  const int fg = w & 3, kg = w >> 2, g = l >> 4, c16 = l & 15;
  const bool two = fg < 3;

  const u64 nonce = __hip_atomic_load((const gu64*)sync, RLX_AGENT);
  const u64 tag = (((u64)(unsigned)__builtin_amdgcn_readfirstlane((unsigned)(nonce >> 32)) << 32) |
                   (unsigned)__builtin_amdgcn_readfirstlane((unsigned)nonce)) + 1;
  u64* zflag = sync + 32;
  u64* hflag = zflag + 256 * 32;
  const rsrc_t rsN = make_rsrc(z2N, S_ * FH * 2);
  const rsrc_t rsN1 = make_rsrc(z2N, two ? S_ * FH * 2 : 0);
  f32x2* sst = (f32x2*)(smem + XW_LDS);

  u32x4 zp[2];
  {
    const XwAddr<true> ad(l, fg, kg);
    f32x4 acc[2][2];
#pragma unroll
    for (int rt = 0; rt < 2; ++rt)
#pragma unroll
      for (int jj = 0; jj < 2; ++jj) acc[rt][jj] = f32x4{0.f, 0.f, 0.f, 0.f};
    BAR(); xw_compute<true, 0, 4>(ad, smem, acc);
    BAR(); xw_compute<true, 4, 4>(ad, smem, acc);
    BAR(); xw_compute<true, 8, 4>(ad, smem, acc);
    BAR(); xw_compute<true, 12, 2>(ad, smem, acc);
#pragma unroll
    for (int jj = 0; jj < 2; ++jj)
      zp[jj] = u32x4{gelu_pair(acc[0][jj][0], acc[0][jj][1]), gelu_pair(acc[0][jj][2], acc[0][jj][3]),
                     gelu_pair(acc[1][jj][0], acc[1][jj][1]), gelu_pair(acc[1][jj][2], acc[1][jj][3])};
    if (!two) zp[1] = u32x4{0u, 0u, 0u, 0u};
    float v[16];
#pragma unroll
    for (int e = 0; e < 4; ++e) {
      const f32x2 a0 = {bf_lo(zp[0][e]), bf_hi(zp[0][e])}, a1 = {bf_lo(zp[1][e]), bf_hi(zp[1][e])};
      const f32x2 s1 = a0 + a1, s2 = __builtin_elementwise_fma(a0, a0, a1 * a1);
      v[4 * e] = s1[0]; v[4 * e + 1] = s2[0]; v[4 * e + 2] = s1[1]; v[4 * e + 3] = s2[1];
    }
#pragma unroll
    for (int i = 0; i < 8; ++i) { const bool b = c16 & 1; v[i] = (b ? v[i + 8] : v[i]) + dpp_mov<0xB1>(b ? v[i] : v[i + 8]); }
#pragma unroll
    for (int i = 0; i < 4; ++i) { const bool b = c16 & 2; v[i] = (b ? v[i + 4] : v[i]) + dpp_mov<0x4E>(b ? v[i] : v[i + 4]); }
#pragma unroll
    for (int i = 0; i < 2; ++i) { const bool b = c16 & 4; v[i] = (b ? v[i + 2] : v[i]) + dpp_mov<0x124>(b ? v[i] : v[i + 2]); }
    { const bool b = c16 & 8; v[0] = (b ? v[1] : v[0]) + dpp_mov<0x128>(b ? v[0] : v[1]); }
    const int idx = ((c16 & 1) << 3) | ((c16 & 2) << 1) | ((c16 & 4) >> 1) | (c16 >> 3);
    ((float*)(sst + fg * 64 + 32 * kg + 8 * g))[idx] = v[0];
  }

  u32x2 z1p[2][2];
  {
    const XwAddr<false> ad(l, fg, kg);
    f32x4 za[2][2];
#pragma unroll
    for (int i = 0; i < 2; ++i)
#pragma unroll
      for (int j = 0; j < 2; ++j) za[i][j] = f32x4{0.f, 0.f, 0.f, 0.f};
    BAR();

    const unsigned tagA = (unsigned)tag, tagB = (unsigned)(tag >> 32) ^ (tagA * 0x9E3779B1u);
    if (w == 7) {
      const f32x2 t = sst[l] + sst[64 + l] + sst[128 + l] + sst[192 + l];
      const float m2 = fmaxf(t[1] - t[0] * t[0] * (1.0f / 112.0f), 0.f);
      gu64* gp = (gu64*)stats + (size_t)(rb * 8 + cb) * 128 + l;
      __hip_atomic_store(gp, ((u64)tagA << 32) | __builtin_bit_cast(unsigned, t[0]), RLX_AGENT);
      __hip_atomic_store(gp + 64, ((u64)tagB << 32) | __builtin_bit_cast(unsigned, m2), RLX_AGENT);
    }
    xw_compute<false, 0, 4>(ad, smem, za);
    BAR();
    f32x2* mrs = (f32x2*)(smem + XW_LDS + 2048);
    const gu64* gq = (const gu64*)stats + (size_t)rb * 1024 + l;
    u64 gv[16];
    if (w == 0) {
#pragma unroll
      for (int i = 0; i < 16; ++i) gv[i] = __hip_atomic_load(gq + 64 * i, RLX_AGENT);
    }
    xw_compute<false, 4, 4>(ad, smem, za);
    if (w == 0) {
      for (unsigned spins = 0; spins < (1u << 19); ++spins) {
        bool ok = true;
#pragma unroll
        for (int i = 0; i < 16; ++i) ok &= (unsigned)(gv[i] >> 32) == ((i & 1) ? tagB : tagA);
        if (__all(ok)) break;
#pragma unroll
        for (int i = 0; i < 16; ++i) gv[i] = __hip_atomic_load(gq + 64 * i, RLX_AGENT);
      }
      float sum = 0.f, m2 = 0.f;
#pragma unroll
      for (int i = 0; i < 8; ++i) {
        sum += __builtin_bit_cast(float, (unsigned)gv[2 * i]);
        m2 += __builtin_bit_cast(float, (unsigned)gv[2 * i + 1]);
      }
      const float mean = sum * (1.0f / 896.0f);
#pragma unroll
      for (int i = 0; i < 8; ++i) {
        const float d = __builtin_bit_cast(float, (unsigned)gv[2 * i]) * (1.0f / 112.0f) - mean;
        m2 += 112.0f * d * d;
      }
      const float var = bfr(m2 * (1.0f / 896.0f));
      const float den = bfr(sqrtf(bfr(var + 1e-5f)));
      mrs[l] = f32x2{bfr(mean), 1.0f / den};
    }
    BAR();

    const unsigned zoff = (unsigned)(((cb * 7 + 2 * fg) * 64 + 2 * rb + kg) * 64 + l) * 16u;
#pragma unroll
    for (int jj = 0; jj < 2; ++jj) {
      u32x4 v;
#pragma unroll
      for (int e = 0; e < 4; ++e) {
        const f32x4 mr = *(const f32x4*)(mrs + 32 * kg + 8 * g + 2 * e);
        v[e] = pack2(bfr(bf_lo(zp[jj][e]) - mr[0]) * mr[1], bfr(bf_hi(zp[jj][e]) - mr[2]) * mr[3]);
      }
      __builtin_amdgcn_raw_buffer_store_b128(v, jj ? rsN1 : rsN, zoff + 65536u * jj, 0, 16);
    }
    xw_compute<false, 8, 4>(ad, smem, za);
    BAR();
    xw_compute<false, 12, 2>(ad, smem, za);
#pragma unroll
    for (int i = 0; i < 2; ++i)
#pragma unroll
      for (int j = 0; j < 2; ++j) {
        z1p[i][j] = u32x2{gelu_pair(za[i][j][0], za[i][j][1]), gelu_pair(za[i][j][2], za[i][j][3])};
        asm volatile("" : "+v"(z1p[i][j]));
      }
    WAITV(0);
    BAR();
    if (w == 0) {
      if (l == 0) __hip_atomic_store((gu64*)zflag + (cb * 32 + rb) * 32, tag, RLX_AGENT);
      wait_flags(zflag + cb * 32 * 32, 32, l, tag);
    }
  }

  const unsigned boff = (unsigned)(((cb * 7 + 2 * fg) * 64 + kg) * 64 + l) * 16u;
  u32x4 bst[4][4];
#define LOAD_B(slot, st)                                                                          \
  {                                                                                               \
    const int sk_ = ((st) + koff) & 15;                                                           \
    bst[slot][0] = __builtin_amdgcn_raw_buffer_load_b128(rsN, boff, 4096 * sk_, 16);              \
    bst[slot][1] = __builtin_amdgcn_raw_buffer_load_b128(rsN1, boff + 65536, 4096 * sk_, 16);     \
    bst[slot][2] = __builtin_amdgcn_raw_buffer_load_b128(rsN, boff + 2048, 4096 * sk_, 16);       \
    bst[slot][3] = __builtin_amdgcn_raw_buffer_load_b128(rsN1, boff + 65536 + 2048, 4096 * sk_, 16); \
  }
  unsigned ard[2][2][4];
#pragma unroll
  for (int h = 0; h < 2; ++h)
#pragma unroll
    for (int i = 0; i < 4; ++i) {
      ard[0][h][i] = (unsigned)(size_t)smem + (((16 * ((i + 2 * kg) & 3) + c16) * 256 + (((4 * kg + g) ^ c16) * 16)) ^ (128 * h));
      ard[1][h][i] = ard[0][h][i] + 65536u;
      asm volatile("" : "+v"(ard[1][h][i]));
    }
  f32x4 acc[4][2];
#pragma unroll
  for (int i = 0; i < 4; ++i)
#pragma unroll
    for (int j = 0; j < 2; ++j) acc[i][j] = f32x4{0.f, 0.f, 0.f, 0.f};
  bf16x8 af[2][4];
#define READ_A(st, h)                                                                                    \
  {                                                                                                      \
    constexpr int so_ = ring_slot(st);                                                                   \
    _Pragma("unroll") for (int i = 0; i < 4; ++i) {                                                      \
      unsigned a_ = ard[so_ >= 65536][h][i];                                                             \
      if (so_ >= 131072) asm volatile("v_add_u32 %0, 0x10000, %0" : "+v"(a_));                           \
      af[h][i] = *(const LAS bf16x8*)(size_t)(a_ + (so_ & 65535));                                       \
    }                                                                                                    \
    __builtin_amdgcn_sched_barrier(0);                                                                   \
  }
#define MMA(u, h)                                                                                        \
  {                                                                                                      \
    _Pragma("unroll") for (int j = 0; j < 2; ++j) {                                                      \
      const bf16x8 zf = __builtin_bit_cast(bf16x8, bst[u][2 * (h) + j]);                                 \
      _Pragma("unroll") for (int i = 0; i < 4; ++i)                                                      \
        acc[i][j] = __builtin_amdgcn_mfma_f32_16x16x32_bf16(zf, af[h][i], acc[i][j], 0, 0, 0);           \
    }                                                                                                    \
    __builtin_amdgcn_sched_barrier(0);                                                                   \
  }
#define ITER(it)                                                \
  {                                                             \
    BAR();                                                      \
    READ_A(it, 0)                                               \
    MMA(((it) + 3) & 3, 1)                                      \
    READ_A(it, 1)                                               \
    if ((it) + 3 < 16) { LOAD_B(((it) + 3) & 3, (it) + 3) }     \
    MMA((it) & 3, 0)                                            \
  }
  BAR();

  LOAD_B(0, 0) LOAD_B(1, 1) LOAD_B(2, 2) LOAD_B(3, 3)
  READ_A(0, 0)
  READ_A(0, 1)
  MMA(0, 0)

  ITER(1) ITER(2) ITER(3) ITER(4) ITER(5) ITER(6) ITER(7) ITER(8)

  ITER(9) ITER(10) ITER(11) ITER(12) ITER(13)
  int lz = __builtin_amdgcn_mbcnt_hi(~0u, __builtin_amdgcn_mbcnt_lo(~0u, 0u));
  asm volatile("" : "+v"(lz));
  float bias[2];
#pragma unroll
  for (int i = 0; i < 2; ++i) bias[i] = bf_lo(b_sp[(rb * 4 + 2 * kg + i) * 16 + (lz & 15)]);
  ITER(14) ITER(15)
  MMA(3, 1)

  BAR();
  char* redw = smem + (kg ? ring_slot(14) : ring_slot(15)) + fg * 4096 + lz * 16;
  const char* redr = smem + (kg ? ring_slot(15) : ring_slot(14)) + fg * 4096 + lz * 16;
#pragma unroll
  for (int i = 0; i < 2; ++i)
#pragma unroll
    for (int j = 0; j < 2; ++j) *(f32x4*)(redw + (2 * i + j) * 1024) = acc[2 + i][j];
  BAR();
  const rsrc_t rsH = make_rsrc(hF, S_ * FH * 2);
#pragma unroll
  for (int i = 0; i < 2; ++i) {
    u32x2 hv[2];
#pragma unroll
    for (int j = 0; j < 2; ++j) {
      const f32x4 a = acc[i][j] + *(const f32x4*)(redr + (2 * i + j) * 1024);
      const unsigned g01 = pack2(a[0], a[1]), g23 = pack2(a[2], a[3]);
      const unsigned s01 = pack2(bf_lo(g01) + bias[i], bf_hi(g01) + bias[i]);
      const unsigned s23 = pack2(bf_lo(g23) + bias[i], bf_hi(g23) + bias[i]);
      hv[j] = u32x2{pack2(bf_lo(z1p[i][j][0]) * bf_lo(s01), bf_hi(z1p[i][j][0]) * bf_hi(s01)),
                    pack2(bf_lo(z1p[i][j][1]) * bf_lo(s23), bf_hi(z1p[i][j][1]) * bf_hi(s23))};
    }
    const unsigned ho = (unsigned)(((rb * 4 + 2 * kg + i) * 28 + 7 * (cb >> 1)) * 64 + lz) * 16u;
    if (two) __builtin_amdgcn_raw_buffer_store_b128(u32x4{hv[0][0], hv[0][1], hv[1][0], hv[1][1]}, rsH, ho + (3 * (cb & 1) + fg) * 1024u, 0, 16);
    else     __builtin_amdgcn_raw_buffer_store_b64(hv[0], rsH, ho + 6 * 1024u + 8u * (cb & 1), 0, 16);
  }
  {
    const int tq = w & 3, gz = lz >> 4, cz = lz & 15;
    const int rrow = 4 * gz + (cz >> 2);
    int rb4[4];
#pragma unroll
    for (int j = 0; j < 4; ++j) rb4[j] = rrow * 128 + ((j ^ ((rrow >> 1) & 3)) * 32) + 8 * (lz & 3);
#define OUT_TILE(t) (ring_slot(((t) >> 3) + 7) + ((t) & 7) * 2048)
#define OUT_FRAG(dst, ks)                                                                                 \
  {                                                                                                       \
    constexpr int pc_ = (ks) / 7, e_ = (ks) % 7;                                                          \
    constexpr int tA_ = (e_ < 6) ? (14 * pc_ + 7 * (e_ / 3) + 2 * (e_ % 3)) : (14 * pc_ + 6);             \
    constexpr int tB_ = (e_ < 6) ? (tA_ + 1) : (14 * pc_ + 13);                                           \
    _Pragma("unroll") for (int j = 0; j < 4; ++j)                                                         \
      dst[j] = tr_frag(smem + rb4[j] + OUT_TILE(tA_), smem + rb4[j] + OUT_TILE(tB_));                     \
  }
    BAR();
    bf16x8 wpre[5][4];
    if (kg == 0) { OUT_FRAG(wpre[0], 0) OUT_FRAG(wpre[1], 2) OUT_FRAG(wpre[2], 4) OUT_FRAG(wpre[3], 6) OUT_FRAG(wpre[4], 8) }
    else         { OUT_FRAG(wpre[0], 1) OUT_FRAG(wpre[1], 3) OUT_FRAG(wpre[2], 5) OUT_FRAG(wpre[3], 7) OUT_FRAG(wpre[4], 9) }
    WAITV(0);
    BAR();

    if (w == 0) {
      if (lz == 0) __hip_atomic_store((gu64*)hflag + (rb * 8 + cb) * 32, tag, RLX_AGENT);
      if (cb < 7) wait_flags(hflag + rb * 8 * 32, 8, lz, tag);
    }
    if (cb == 7) return;
    BAR();

    const unsigned ho = (unsigned)(((rb * 4 + tq) * 28 + kg) * 64 + lz) * 16u;
    u32x4 hf[14];
#pragma unroll
    for (int i = 0; i < 14; ++i) hf[i] = __builtin_amdgcn_raw_buffer_load_b128(rsH, ho, 2048 * i, 16);
    f32x4 oc[4];
#pragma unroll
    for (int j = 0; j < 4; ++j) oc[j] = f32x4{0.f, 0.f, 0.f, 0.f};
#pragma unroll
    for (int i = 0; i < 5; ++i)
#pragma unroll
      for (int j = 0; j < 4; ++j)
        oc[j] = __builtin_amdgcn_mfma_f32_16x16x32_bf16(wpre[i][j], __builtin_bit_cast(bf16x8, hf[i]), oc[j], 0, 0, 0);
#define OUT_STEP(i, ks)                                                                                   \
  {                                                                                                       \
    bf16x8 wf[4];                                                                                         \
    OUT_FRAG(wf, ks)                                                                                      \
    _Pragma("unroll") for (int j = 0; j < 4; ++j)                                                         \
      oc[j] = __builtin_amdgcn_mfma_f32_16x16x32_bf16(wf[j], __builtin_bit_cast(bf16x8, hf[i]), oc[j], 0, 0, 0); \
  }
#define OUT_ALL(par)                                                                                       \
  OUT_STEP(5, 10 + par) OUT_STEP(6, 12 + par) OUT_STEP(7, 14 + par) OUT_STEP(8, 16 + par) OUT_STEP(9, 18 + par) \
  OUT_STEP(10, 20 + par) OUT_STEP(11, 22 + par) OUT_STEP(12, 24 + par) OUT_STEP(13, 26 + par)
    if (kg == 0) { OUT_ALL(0) } else { OUT_ALL(1) }
    char* red = smem + ring_slot(14);
    *(f32x4*)(red + (w ^ 4) * 2048 + lz * 16) = kg ? oc[0] : oc[2];
    *(f32x4*)(red + (w ^ 4) * 2048 + 1024 + lz * 16) = kg ? oc[1] : oc[3];
    BAR();
    const f32x4 a0 = (kg ? oc[2] : oc[0]) + *(const f32x4*)(red + w * 2048 + lz * 16);
    const f32x4 a1 = (kg ? oc[3] : oc[1]) + *(const f32x4*)(red + w * 2048 + 1024 + lz * 16);
    u16* op = out + (size_t)((rb * 4 + tq) * 16 + cz) * D_ + cb * 64 + 32 * kg + 4 * gz;
    *(u32x2*)op = u32x2{pack2(a0[0], a0[1]), pack2(a0[2], a0[3])};
    *(u32x2*)(op + 16) = u32x2{pack2(a1[0], a1[1]), pack2(a1[2], a1[3])};
  }
  if (bid == 0 && w == 0 && lz == 0) __hip_atomic_store((gu64*)sync, tag, RLX_AGENT);

}

extern "C" void kernel_launch(void* const* d_in, const int* in_sizes, int n_in, void* d_out, int out_size,
                              void* d_ws, size_t ws_size, hipStream_t stream) {
  const u16* x = (const u16*)d_in[0];
  const u16* w_in = (const u16*)d_in[1];
  const u16* w_sp = (const u16*)d_in[2];
  const u16* b_sp = (const u16*)d_in[3];
  const u16* w_out = (const u16*)d_in[4];
  char* ws = (char*)d_ws;
  u16* z2N = (u16*)ws;
  u16* hF = (u16*)(ws + (size_t)4 * 1024 * 1024);
  float* stats = (float*)(ws + (size_t)8 * 1024 * 1024);
  u64* sync = (u64*)(ws + (size_t)9 * 1024 * 1024);
  gmlp_fused<<<256, 768, 0, stream>>>(x, w_in, w_sp, b_sp, w_out, (u16*)d_out, stats, z2N, hF, sync);
}
```
